```python
import torch

def _softmax(x, axis=-1):
    return torch.softmax(x, dim=axis)
def _gelu(x):
    return torch.nn.functional.gelu(x, approximate="tanh")
def _silu(x):
    return torch.nn.functional.silu(x)
def _sigmoid(x):
    return torch.sigmoid(x)
def _layernorm(x, axis=-1, eps=1e-5):
    m = x.mean(dim=axis, keepdim=True)
    v = x.var(dim=axis, keepdim=True, unbiased=False)
    return (x - m) / torch.sqrt(v + eps)
def _scatter_add(tgt, idx, src, axis=0):
    return tgt.clone().index_add_(axis, idx, src)
def _segment_sum(src, seg, n):
    out = torch.zeros((n,) + src.shape[1:], dtype=src.dtype)
    return out.index_add_(0, seg, src)
def _pad(x, pw):
    flat = tuple(v for pair in reversed(pw) for v in pair)
    return torch.nn.functional.pad(x, flat)

kernel_name = 'standalone__cheap_equivariant_egnn__graph'

def setup_inputs():

    _w = (lambda a: (lambda t: t.to(torch.bfloat16) if t.is_floating_point() else t)(torch.as_tensor(a)))
    N, deg, D, De = 10000, 16, 128, max(0, 1)
    E = N * deg
    return {
        "h": _w((lambda *s: torch.randn(*s, dtype=torch.bfloat16) * 0.2)(N, D)),
        "pos": _w((lambda *s: torch.randn(*s, dtype=torch.bfloat16) * 0.8)(N, 3)),
        "e_attr": _w((lambda *s: torch.randn(*s, dtype=torch.bfloat16) * 0.8)(E, De)),
        "src": _w((lambda lo, hi, *s: torch.randint(lo, hi, s))(0, N, E).to(torch.int64)),
        "dst": _w((lambda lo, hi, *s: torch.randint(lo, hi, s))(0, N, E).to(torch.int64)),
        "w_e": _w(0.1 * (lambda *s: torch.randn(*s, dtype=torch.bfloat16) * 0.8)(2 * D + 1 + De, D)),
        "w_x": _w(0.1 * (lambda *s: torch.randn(*s, dtype=torch.bfloat16) * 0.2)(D, 1)),
        "w_h": _w(0.1 * (lambda *s: torch.randn(*s, dtype=torch.bfloat16) * 0.2)(2 * D, D)),
    }

def reference(h, pos, e_attr, src, dst, w_e, w_x, w_h):
    N, D = h.shape[0], h.shape[1]
    diff = pos[dst] - pos[src]
    dist2 = torch.sum(diff * diff, axis=-1, keepdims=True)
    e_in = torch.concatenate([h[dst], h[src], dist2, e_attr], axis=-1)
    m_ij = _silu(e_in @ w_e)
    coord_scale = m_ij @ w_x
    pos_new = pos + _segment_sum(diff * coord_scale, dst, N)
    m_i = _segment_sum(m_ij, dst, N)
    h_new = h + _silu(torch.concatenate([h, m_i], axis=-1) @ w_h)
    return h_new, pos_new
```

```cpp
#include <hip/hip_runtime.h>
#include <hip/hip_bf16.h>

typedef unsigned int u32;
typedef unsigned short u16;
typedef unsigned long long u64;
typedef u32 u32x2 __attribute__((ext_vector_type(2)));
typedef u32 u32x4 __attribute__((ext_vector_type(4)));
typedef short bf16x8 __attribute__((ext_vector_type(8)));
typedef float f32x2 __attribute__((ext_vector_type(2)));
typedef float f32x4 __attribute__((ext_vector_type(4)));
typedef float f32x16 __attribute__((ext_vector_type(16)));
typedef __bf16 bf16x2_t __attribute__((ext_vector_type(2)));
#define DI __device__ __forceinline__

constexpr int D = 128;
constexpr int NPB = 40;
constexpr int CH = 640;
constexpr int MAXB = 256;
constexpr int CAPW = 128;
constexpr float LOG2E = 1.4426950408889634f;
constexpr float LN2 = 0.6931471805599453f;

constexpr size_t WS_BT = 0;
constexpr size_t WS_P8 = (size_t)4 << 20;
constexpr size_t WS_REC = (size_t)5 << 20;
constexpr size_t WS_OFF = (size_t)7 << 20;
constexpr size_t WS_WT = (size_t)8 << 20;

#define STAMP(k) do { } while (0)
#define STAMPT(k, t) do { } while (0)
#define STAMPC(k, t) do { } while (0)

DI float bf2f(u32 b) { return __builtin_bit_cast(float, b << 16); }
DI u32 pk_bf16(float lo, float hi) {
  f32x2 v = {lo, hi};
  bf16x2_t r = __builtin_convertvector(v, bf16x2_t);
  return __builtin_bit_cast(u32, r);
}
DI float rbf(float x) { return bf2f(pk_bf16(x, x) & 0xffffu); }
DI f32x4 mfma16(u32x4 a, u32x4 b, f32x4 c) {
  return __builtin_amdgcn_mfma_f32_16x16x32_bf16(__builtin_bit_cast(bf16x8, a), __builtin_bit_cast(bf16x8, b), c, 0, 0, 0);
}
DI f32x16 mfma32(u32x4 a, u32x4 b, f32x16 c) {
  return __builtin_amdgcn_mfma_f32_32x32x16_bf16(__builtin_bit_cast(bf16x8, a), __builtin_bit_cast(bf16x8, b), c, 0, 0, 0);
}

__global__ __launch_bounds__(1024) void k_prep(const u16* __restrict__ h, const u16* __restrict__ pos,
                                               const u16* __restrict__ ea, const int* __restrict__ src,
                                               const int* __restrict__ dst, const u16* __restrict__ w_e,
                                               const u16* __restrict__ w_h, char* __restrict__ ws, int N, int E) {
  __shared__ __attribute__((aligned(16))) char lds[1024 + 10 * 1024 + NPB * 272];
  u32* cnt = (u32*)lds;
  char* hs = lds + 11 * 1024;
  const int tid = threadIdx.x, lane = tid & 63, b = blockIdx.x;
  const int wv = __builtin_amdgcn_readfirstlane(tid >> 6);
  STAMP(0);
  if (wv < 10) {
    const int e = b * CH + tid;
    const bool valid = e < E;
    int d = 0, s = 0; u32 a = 0;
    if (valid) { d = dst[e]; s = src[e]; a = ea[e]; }
    int hn = b * NPB + (tid >> 4); hn = hn < N ? hn : N - 1;
    const u32x4 hv = *(const u32x4*)(h + (size_t)hn * D + (tid & 15) * 8);
    if (tid < MAXB) cnt[tid] = 0;
    __syncthreads();
    const u32 bk = (u32)d / NPB, nl = (u32)d - bk * NPB;
    u32 rank = 0;
    if (valid) rank = atomicAdd(&cnt[bk], 1u);
    *(u32x4*)(hs + (tid >> 4) * 272 + (tid & 15) * 16) = hv;
    STAMP(1);
    __syncthreads();
    const u32x4 c4 = *(const u32x4*)(cnt + lane * 4);
    const u32 tot = c4[0] + c4[1] + c4[2] + c4[3];
    u32 inc = tot;
#pragma unroll
    for (int o = 1; o < 64; o <<= 1) { u32 t = __shfl_up(inc, o, 64); if (lane >= o) inc += t; }
    const u32 ex = inc - tot;
    u32x4 st4 = {ex, ex + c4[0], ex + c4[0] + c4[1], ex + c4[0] + c4[1] + c4[2]};
    u32* mine = (u32*)(lds + 1024 + wv * 1024);
    *(u32x4*)(mine + lane * 4) = st4;
    if (wv == 0) { u32x2 o2 = {st4[0] | (st4[1] << 16), st4[2] | (st4[3] << 16)}; *(u32x2*)(ws + WS_OFF + (size_t)b * 512 + lane * 8) = o2; }
    __builtin_amdgcn_wave_barrier();
    if (valid) {
      const u32 p = mine[bk] + rank;
      u32x2 r = {(u32)s, nl | (a << 16)};
      *(u32x2*)(ws + WS_REC + ((size_t)b * CH + p) * 8) = r;
    }
    STAMP(2);
  } else if (wv >= 12) {
    const int u = wv - 12, c = lane & 15, g = lane >> 4;
    u32 w[4][8];
    const u16* wp = w_e + (size_t)(D + 8 * g) * D + 32 * u + 2 * c;
#pragma unroll
    for (int s = 0; s < 4; s++)
#pragma unroll
      for (int j = 0; j < 8; j++) w[s][j] = *(const u32*)(wp + (32 * s + j) * D);
    __syncthreads();
    __syncthreads();
    f32x4 acc[3][2];
#pragma unroll
    for (int t = 0; t < 3; t++) { acc[t][0] = f32x4{0, 0, 0, 0}; acc[t][1] = f32x4{0, 0, 0, 0}; }
#pragma unroll
    for (int s = 0; s < 4; s++) {
      u32x4 fa, fb;
#pragma unroll
      for (int q = 0; q < 4; q++) {
        fa[q] = __builtin_amdgcn_perm(w[s][2 * q + 1], w[s][2 * q], 0x05040100u);
        fb[q] = __builtin_amdgcn_perm(w[s][2 * q + 1], w[s][2 * q], 0x07060302u);
      }
#pragma unroll
      for (int t = 0; t < 3; t++) {
        int row = 16 * t + c; row = row < NPB ? row : NPB - 1;
        const u32x4 hb = *(const u32x4*)(hs + row * 272 + s * 64 + g * 16);
        acc[t][0] = mfma16(fa, hb, acc[t][0]);
        acc[t][1] = mfma16(fb, hb, acc[t][1]);
      }
    }
#pragma unroll
    for (int t = 0; t < 3; t++) {
      const int row = 16 * t + c, node = b * NPB + row;
      u32x4 o;
#pragma unroll
      for (int i = 0; i < 4; i++) o[i] = pk_bf16(acc[t][0][i] * LOG2E, acc[t][1][i] * LOG2E);
      if (row < NPB && node < N) *(u32x4*)(ws + WS_BT + (size_t)node * 256 + u * 64 + g * 16) = o;
    }
    STAMPT(14, 768);
  } else if (wv == 10) {
    for (int f = b; f < 96; f += gridDim.x) {
      const int m = f >> 5, idx = f & 31, s = idx & 3, ct = idx >> 2, c = lane & 15, g = lane >> 4;
      const u16* base = m == 0 ? w_e : (w_h + (size_t)(m - 1) * D * D);
      const int n = m == 0 ? 16 * ct + c : 32 * (ct >> 1) + 8 * (c >> 2) + 4 * (ct & 1) + (c & 3);
      u32x4 o;
#pragma unroll
      for (int q = 0; q < 4; q++) {
        const u32 lo = base[(size_t)(32 * s + 8 * g + 2 * q) * D + n], hi = base[(size_t)(32 * s + 8 * g + 2 * q + 1) * D + n];
        o[q] = lo | (hi << 16);
      }
      *(u32x4*)(ws + WS_WT + (size_t)f * 1024 + lane * 16) = o;
    }
    STAMPT(15, 640);
    __syncthreads();
    __syncthreads();
  } else {
    const int node = b * NPB + lane;
    if (lane < NPB && node < N) {
      const u32 x = pos[node * 3], y = pos[node * 3 + 1], z = pos[node * 3 + 2];
      u32x2 o = {x | (y << 16), z};
      *(u32x2*)(ws + WS_P8 + (size_t)node * 8) = o;
    }
    __syncthreads();
    __syncthreads();
  }
}

constexpr int L_SM = 0;
constexpr int L_CF = L_SM + 8 * 8 * 640;
constexpr int L_ZERO = L_CF + 8 * CAPW * 16;
constexpr int L_CNT = L_ZERO + 16;
constexpr int L_SRC = L_CNT + 48;
constexpr int L_KB = L_SRC + 8 * CAPW * 4 + 64;
constexpr int L_CP = L_KB + 8 * D * 16;
constexpr int L_MI = L_CP + 12 * 64 * 32;
constexpr int L_HS = L_MI + NPB * 272;
constexpr int L_PC = L_HS + NPB * 272;
constexpr int L_WX = L_PC + NPB * 48;
constexpr int L_PD = L_WX + 512;
constexpr int L_OFF = L_PD + NPB * 8;
constexpr int L_LONG = L_OFF + 1024;
constexpr int L_END = L_LONG + 1024;
static_assert(L_END <= 160 * 1024, "lds");
static_assert((L_CF % 16) == 0 && (L_SRC % 16) == 0 && (L_KB % 16) == 0 && (L_CP % 16) == 0 && (L_MI % 16) == 0 && (L_HS % 16) == 0 && (L_PC % 16) == 0 && (L_WX % 16) == 0, "align");
constexpr u32 OOB = 0xfffff000u;

DI int tile_node(int t, int c) {
  return t < 2 ? ((c & 3) * 8 + 4 * t + (c >> 2)) : (32 + (c & 7));
}
DI u32 take_slot(char* lds, u32x2 rec) {
  u32* wcnt = (u32*)(lds + L_CNT);
  const u32 win = rec[1] & 7u;
  const u32 rank = atomicAdd(&wcnt[win], 1u);
  u32 sp = 0xffffffffu;
  if (rank < CAPW) {
    sp = win * CAPW + (rank & ~15u) + ((rank & 1u) << 3) + ((rank & 15u) >> 1);
    *(u32*)(lds + L_SRC + sp * 4) = rec[0] << 8;
  } else wcnt[8] = 1;
  return sp;
}
DI void put_coef(char* lds, u32x2 rec, u32x2 ps, u32 sp) {
  const u32 nl = rec[1] & 0xffu, slot = nl >> 3, eab = rec[1] >> 16;
  const u32x2 pd = *(const u32x2*)(lds + L_PD + nl * 8);
  const float dx = rbf(bf2f(pd[0] & 0xffffu) - bf2f(ps[0] & 0xffffu));
  const float dy = rbf(bf2f(pd[0] >> 16) - bf2f(ps[0] >> 16));
  const float dz = rbf(bf2f(pd[1] & 0xffffu) - bf2f(ps[1] & 0xffffu));
  const float s2 = rbf(dx * dx) + rbf(dy * dy) + rbf(dz * dz);
  const u32 d2 = pk_bf16(s2, s2);
  const u32 one = 0x3f80u << ((slot & 1u) * 16);
  u32x4 cf = {slot < 2 ? one : 0u, (slot >> 1) == 1 ? one : 0u, (slot == 4 ? 0x3f80u : 0u) | (eab << 16), d2};
  *(u32x4*)(lds + L_CF + sp * 16) = cf;
  char* sm = lds + L_SM + (sp >> 4) * 640 + slot * 32 + (sp & 15u) * 2;
  *(u16*)(sm) = (u16)0x3f80u;
  *(u16*)(sm + 160) = (u16)(__builtin_bit_cast(u32, dx) >> 16);
  *(u16*)(sm + 320) = (u16)(__builtin_bit_cast(u32, dy) >> 16);
  *(u16*)(sm + 480) = (u16)(__builtin_bit_cast(u32, dz) >> 16);
}

__global__ __launch_bounds__(1024) void k_main(const u16* __restrict__ h, const u16* __restrict__ w_e,
                                               const u16* __restrict__ w_x, char* __restrict__ ws,
                                               u16* __restrict__ out, int N, int C, int NB) {
  __shared__ __attribute__((aligned(16))) char lds[L_END];
  const int tid = threadIdx.x, lane = tid & 63;
  const int wv = __builtin_amdgcn_readfirstlane(tid >> 6);
  int bk;
  { const int q = NB >> 3, r = NB & 7, x = blockIdx.x & 7, i = blockIdx.x >> 3;
    bk = (x < r ? x * (q + 1) : r * (q + 1) + (x - r) * q) + i; }
  const int nb0 = bk * NPB;
  STAMP(4); STAMPC(12, 0);
  const __amdgpu_buffer_rsrc_t r_bt = __builtin_amdgcn_make_buffer_rsrc((void*)(ws + WS_BT), 0, N * 256, 0x00020000);
  const __amdgpu_buffer_rsrc_t r_p8 = __builtin_amdgcn_make_buffer_rsrc((void*)(ws + WS_P8), 0, N * 8, 0x00020000);
  const __amdgpu_buffer_rsrc_t r_rec = __builtin_amdgcn_make_buffer_rsrc((void*)(ws + WS_REC), 0, C * CH * 8, 0x00020000);
  const __amdgpu_buffer_rsrc_t r_off = __builtin_amdgcn_make_buffer_rsrc((void*)(ws + WS_OFF), 0, C * 512, 0x00020000);
  const __amdgpu_buffer_rsrc_t r_h = __builtin_amdgcn_make_buffer_rsrc((void*)h, 0, N * 256, 0x00020000);
  const int cs = tid >> 2, k4 = tid & 3;
  const u32 o2 = __builtin_amdgcn_raw_buffer_load_b32(r_off, cs * 512 + 2 * bk, 0, 0);
  const u32x4 hv = __builtin_amdgcn_raw_buffer_load_b128(r_h, tid < 640 ? nb0 * 256 + tid * 16 : OOB, 0, 0);
  const u32x2 pdv = __builtin_amdgcn_raw_buffer_load_b64(r_p8, tid < NPB ? (nb0 + tid) * 8 : OOB, 0, 0);
  const u32 wdv = w_e[256 * D + (tid & 127)], wav = w_e[257 * D + (tid & 127)], wxv = w_x[tid & 127];
  u32x4 wf[4];
#pragma unroll
  for (int s = 0; s < 4; s++) wf[s] = *(const u32x4*)(ws + WS_WT + (size_t)(wv * 4 + s) * 1024 + lane * 16);
  {
    const u32x4 z = {0, 0, 0, 0};
#pragma unroll
    for (int i = 0; i < (L_SRC + 16383) / 16384; i++) { const int o = i * 16384 + tid * 16; if (o < L_SRC) *(u32x4*)(lds + o) = z; }
    *(u32*)(lds + L_SRC + tid * 4) = OOB;
    if (tid < 16) *(u32*)(lds + L_SRC + 4096 + tid * 4) = OOB;
  }
  __syncthreads();
  const u32 st = o2 & 0xffffu, cnt = (o2 >> 16) - st;
  u32x2 rec[3];
#pragma unroll
  for (int i = 0; i < 3; i++) rec[i] = __builtin_amdgcn_raw_buffer_load_b64(r_rec, k4 + 4 * i < cnt ? (cs * CH + st + k4 + 4 * i) * 8 : OOB, 0, 0);
  if (tid < 640) *(u32x4*)(lds + L_HS + (tid >> 4) * 272 + (tid & 15) * 16) = hv;
  if (tid < NPB) *(u32x2*)(lds + L_PD + tid * 8) = pdv;
  if (tid < D) *(float*)(lds + L_WX + tid * 4) = bf2f(wxv);
  if (k4 == 0) *(u32*)(lds + L_OFF + cs * 4) = o2;
  {
    const float wd = bf2f(wdv) * LOG2E, wdh = rbf(wd);
    char* kb = lds + L_KB + tid * 16;
    *(u32*)(kb + 12) = pk_bf16(wdh, wd - wdh);
    *(u16*)(kb + 10) = (u16)pk_bf16(bf2f(wav) * LOG2E, 0.f);
  }
  STAMP(5);
  u32* wcnt = (u32*)(lds + L_CNT);
  if (cnt > 12 && k4 == 0) { const u32 i = atomicAdd(&wcnt[9], 1u); *(u32*)(lds + L_LONG + i * 4) = cs; }
  u32 slotp[3];
  u32x2 ps[3];
#pragma unroll
  for (int i = 0; i < 3; i++) ps[i] = __builtin_amdgcn_raw_buffer_load_b64(r_p8, k4 + 4 * i < cnt ? rec[i][0] * 8 : OOB, 0, 0);
#pragma unroll
  for (int i = 0; i < 3; i++) {
    slotp[i] = 0xffffffffu;
    if (k4 + 4 * i < cnt) slotp[i] = take_slot(lds, rec[i]);
  }
  STAMP(6);
  __syncthreads();
  {
    const int nlong = __builtin_amdgcn_readfirstlane(wcnt[9]);
    if (nlong > 0) {
      for (int i = 0; i < nlong; i++) {
        const u32 c = *(const u32*)(lds + L_LONG + i * 4), oo = *(const u32*)(lds + L_OFF + c * 4);
        for (u32 k = (oo & 0xffffu) + 12 + tid; k < (oo >> 16); k += 1024) {
          if (*(volatile u32*)&wcnt[8]) break;
          const u32x2 rc = __builtin_amdgcn_raw_buffer_load_b64(r_rec, (c * CH + k) * 8, 0, 0);
          const u32x2 pv = __builtin_amdgcn_raw_buffer_load_b64(r_p8, rc[0] * 8, 0, 0);
          const u32 sp = take_slot(lds, rc);
          if (sp != 0xffffffffu) put_coef(lds, rc, pv, sp);
        }
      }
      __builtin_amdgcn_s_waitcnt(0x0f70);
      __syncthreads();
    }
  }
  const int fh = wv >> 3, win = (wv + fh) & 7, r = lane & 31, hh = lane >> 5;
  const int ovf = __builtin_amdgcn_readfirstlane(wcnt[8]);
  const int cntw = __builtin_amdgcn_readfirstlane(wcnt[win]);
  const int nsteps = ovf ? 0 : ((cntw + 15) >> 4);
  const u32 lo = fh * 128 + r * 4;
  const char* srcp = lds + L_SRC + win * CAPW * 4 + hh * 32;
  u32 G0[8], G1[8];
  {
    const u32x4 i0 = *(const u32x4*)(srcp), i1 = *(const u32x4*)(srcp + 16);
    const u32x4 i2 = *(const u32x4*)(srcp + 64), i3 = *(const u32x4*)(srcp + 80);
#pragma unroll
    for (int j = 0; j < 4; j++) G0[j] = __builtin_amdgcn_raw_buffer_load_b32(r_bt, i0[j] + lo, 0, 0);
#pragma unroll
    for (int j = 0; j < 4; j++) G0[4 + j] = __builtin_amdgcn_raw_buffer_load_b32(r_bt, i1[j] + lo, 0, 0);
#pragma unroll
    for (int j = 0; j < 4; j++) G1[j] = __builtin_amdgcn_raw_buffer_load_b32(r_bt, i2[j] + lo, 0, 0);
#pragma unroll
    for (int j = 0; j < 4; j++) G1[4 + j] = __builtin_amdgcn_raw_buffer_load_b32(r_bt, i3[j] + lo, 0, 0);
  }
  {
    const int c = lane & 15, g = lane >> 4;
    f32x4 acc[3] = {f32x4{0, 0, 0, 0}, f32x4{0, 0, 0, 0}, f32x4{0, 0, 0, 0}};
    u32x4 hb[3][4];
#pragma unroll
    for (int t = 0; t < 3; t++)
#pragma unroll
      for (int s = 0; s < 4; s++) hb[t][s] = *(const u32x4*)(lds + L_HS + tile_node(t, c) * 272 + g * 16 + s * 64);
    if (wv < 8) {
#pragma unroll
      for (int s = 0; s < 4; s++)
#pragma unroll
        for (int t = 0; t < 3; t++) acc[t] = mfma16(hb[t][s], wf[s], acc[t]);
#pragma unroll
      for (int t = 0; t < 3; t++) acc[t] *= LOG2E;
      char* kb = lds + L_KB + (16 * wv + c) * 16;
#pragma unroll
      for (int t = 0; t < 2; t++) {
        u32x2 v = {pk_bf16(acc[t][0], acc[t][1]), pk_bf16(acc[t][2], acc[t][3])};
        *(u32x2*)(kb + (4 * t + g) * 2048) = v;
      }
      if (g < 2) {
#pragma unroll
        for (int i = 0; i < 4; i++) *(u16*)(kb + (4 * g + i) * 2048 + 8) = (u16)pk_bf16(acc[2][i], 0.f);
      }
    } else {
      const int u = (wv - 8) >> 1, tb = wv & 1;
#pragma unroll
      for (int s = 0; s < 4; s++)
#pragma unroll
        for (int t = 0; t < 3; t++) acc[t] = mfma16(wf[s], hb[t][s], acc[t]);
#pragma unroll
      for (int t = 0; t < 3; t++) *(f32x4*)(lds + L_CP + ((t * 4 + u) * 64 + lane) * 32 + tb * 16) = acc[t] * LOG2E;
    }
  }
#pragma unroll
  for (int i = 0; i < 3; i++)
    if (slotp[i] != 0xffffffffu) put_coef(lds, rec[i], ps[i], slotp[i]);
  STAMP(7);
  __syncthreads();
  STAMP(8);
  if (!ovf) {
    f32x16 acc0, acc1, zero;
#pragma unroll
    for (int i = 0; i < 16; i++) { acc0[i] = 0.f; acc1[i] = 0.f; zero[i] = 0.f; }
    const int rh = (r >> 2) & 1, rp_ = r >> 4, rj = (r & 3) | (((r >> 3) & 1) << 2);
    u32x4 sel0, sel1;
    {
      const u32 one = rh == hh ? 0x3f80u << (rp_ * 16) : 0u;
#pragma unroll
      for (int q = 0; q < 4; q++) { sel0[q] = (rj == q) ? one : 0u; sel1[q] = (rj == 4 + q) ? one : 0u; }
    }
    const u32x4 kbf = *(const u32x4*)(lds + L_KB + win * 2048 + (64 * fh + 2 * r + hh) * 16);
    const bool cfon = rp_ == hh;
    u32 cfa = cfon ? (L_CF + (win * CAPW + 8 * rh + rj) * 16) : L_ZERO;
    const u32 cfs = cfon ? 256u : 0u;
    u32 sma = L_SM + win * 8 * 640 + r * 32 + hh * 16;
    const u32 src0 = L_SRC + win * CAPW * 4;

    auto body = [&](u32 (&G)[8], int s, bool pre) {
      const u32x4 cf = *(const u32x4*)(lds + cfa);
      const u32x4 sf = *(const u32x4*)(lds + sma);
      u32x4 i0, i1;
      if (pre) {
        const u32 nx = (s + 2 < nsteps ? src0 + (s + 2) * 64 : L_SRC + 4096) + hh * 32;
        i0 = *(const u32x4*)(lds + nx); i1 = *(const u32x4*)(lds + nx + 16);
      }
      cfa += cfs; sma += 640;
      f32x16 y = mfma32(cf, kbf, zero);
      y = mfma32(sel0, u32x4{G[0], G[1], G[2], G[3]}, y);
      y = mfma32(sel1, u32x4{G[4], G[5], G[6], G[7]}, y);
      if (pre) {
#pragma unroll
        for (int j = 0; j < 4; j++) G[j] = __builtin_amdgcn_raw_buffer_load_b32(r_bt, i0[j] + lo, 0, 0);
#pragma unroll
        for (int j = 0; j < 4; j++) G[4 + j] = __builtin_amdgcn_raw_buffer_load_b32(r_bt, i1[j] + lo, 0, 0);
      }
      u32x4 m0, m1;
#pragma unroll
      for (int q = 0; q < 8; q++) {
        f32x2 yy = {y[2 * q], y[2 * q + 1]};
        f32x2 e = {__builtin_amdgcn_exp2f(-yy[0]), __builtin_amdgcn_exp2f(-yy[1])};
        e += 1.0f;
        f32x2 rc = {__builtin_amdgcn_rcpf(e[0]), __builtin_amdgcn_rcpf(e[1])};
        yy *= rc;
        const u32 pk = pk_bf16(yy[0], yy[1]);
        if (q < 4) m0[q] = pk; else m1[q - 4] = pk;
      }
      acc0 = mfma32(m0, sf, acc0);
      acc1 = mfma32(m1, sf, acc1);
    };
    int s = 0;
    for (; s + 1 < nsteps; s += 2) { body(G0, s, true); body(G1, s + 1, true); }
    if (s < nsteps) body(G0, s, false);
    STAMP(9); STAMPT(3, 960);
    float t = 0.f;
#pragma unroll
    for (int g4 = 0; g4 < 4; g4++) {
      const f32x4 wa = *(const f32x4*)(lds + L_WX + (64 * fh + 16 * g4 + 8 * hh) * 4);
      const f32x4 wb = *(const f32x4*)(lds + L_WX + (64 * fh + 16 * g4 + 8 * hh) * 4 + 16);
      t += acc0[4 * g4] * wa[0] + acc1[4 * g4] * wa[1] + acc0[4 * g4 + 1] * wa[2] + acc1[4 * g4 + 1] * wa[3]
         + acc0[4 * g4 + 2] * wb[0] + acc1[4 * g4 + 2] * wb[1] + acc0[4 * g4 + 3] * wb[2] + acc1[4 * g4 + 3] * wb[3];
      if (r < 5) {
        u32x4 v;
#pragma unroll
        for (int q = 0; q < 4; q++) v[q] = pk_bf16(acc0[4 * g4 + q], acc1[4 * g4 + q]);
        *(u32x4*)(lds + L_MI + (r * 8 + win) * 272 + (64 * fh + 16 * g4 + 8 * hh) * 2) = v;
      }
    }
    if (r >= 5 && r < 20) {
      const int kind = r / 5, slot = r - kind * 5;
      *(float*)(lds + L_PC + (slot * 8 + win) * 48 + (kind - 1) * 16 + (fh * 2 + hh) * 4) = t;
    }
  } else {
    float* macc = (float*)(lds + L_SM);
    float* pacc = (float*)(lds + L_SM + NPB * D * 4);
    for (int i = tid; i < NPB * D + NPB * 4; i += 1024) macc[i] = 0.f;
    __syncthreads();
    const float wx0 = *(const float*)(lds + L_WX + lane * 8), wx1 = *(const float*)(lds + L_WX + lane * 8 + 4);
    const u32x4 kc0 = *(const u32x4*)(lds + L_KB + (2 * lane) * 16), kc1 = *(const u32x4*)(lds + L_KB + (2 * lane + 1) * 16);
    const float wd0 = bf2f(kc0[3] & 0xffffu) + bf2f(kc0[3] >> 16), wd1 = bf2f(kc1[3] & 0xffffu) + bf2f(kc1[3] >> 16);
    const float wa0 = bf2f(kc0[2] >> 16), wa1 = bf2f(kc1[2] >> 16);
    u32 rot = 0;
    for (int c = 0; c < C; c++) {
      const u32 oo = __builtin_amdgcn_readfirstlane(*(const u32*)(lds + L_OFF + c * 4));
      const u32 s0 = oo & 0xffffu, s1 = oo >> 16;
      for (u32 k = s0 + ((wv - rot) & 15u); k < s1; k += 16) {
        const u32x2 rc = __builtin_amdgcn_raw_buffer_load_b64(r_rec, (c * CH + k) * 8, 0, 0);
        const u32 nl = rc[1] & 0xffu, slot = nl >> 3, wn = nl & 7u;
        const u32x2 psv = __builtin_amdgcn_raw_buffer_load_b64(r_p8, rc[0] * 8, 0, 0);
        const u32 bv = __builtin_amdgcn_raw_buffer_load_b32(r_bt, rc[0] * 256 + lane * 4, 0, 0);
        const u32x2 pd = *(const u32x2*)(lds + L_PD + nl * 8);
        const float dx = rbf(bf2f(pd[0] & 0xffffu) - bf2f(psv[0] & 0xffffu));
        const float dy = rbf(bf2f(pd[0] >> 16) - bf2f(psv[0] >> 16));
        const float dz = rbf(bf2f(pd[1] & 0xffffu) - bf2f(psv[1] & 0xffffu));
        const float d2 = rbf(rbf(dx * dx) + rbf(dy * dy) + rbf(dz * dz));
        const float eav = bf2f(rc[1] >> 16);
        const float a0 = bf2f(*(const u16*)(lds + L_KB + wn * 2048 + (2 * lane) * 16 + slot * 2));
        const float a1 = bf2f(*(const u16*)(lds + L_KB + wn * 2048 + (2 * lane + 1) * 16 + slot * 2));
        const float y0 = a0 + bf2f(bv & 0xffffu) + d2 * wd0 + eav * wa0, y1 = a1 + bf2f(bv >> 16) + d2 * wd1 + eav * wa1;
        const float m0 = rbf(y0 * __builtin_amdgcn_rcpf(1.f + __builtin_amdgcn_exp2f(-y0)));
        const float m1 = rbf(y1 * __builtin_amdgcn_rcpf(1.f + __builtin_amdgcn_exp2f(-y1)));
        atomicAdd(&macc[nl * D + 2 * lane], m0);
        atomicAdd(&macc[nl * D + 2 * lane + 1], m1);
        float csv = m0 * wx0 + m1 * wx1;
#pragma unroll
        for (int o = 32; o > 0; o >>= 1) csv += __shfl_xor(csv, o, 64);
        if (lane < 3) atomicAdd(&pacc[nl * 4 + lane], csv * (lane == 0 ? dx : lane == 1 ? dy : dz));
      }
      rot += s1 - s0;
    }
    __syncthreads();
    for (int i = tid; i < NPB * D / 2; i += 1024) {
      const int n = i >> 6, f = (i & 63) * 2;
      *(u32*)(lds + L_MI + n * 272 + f * 2) = pk_bf16(macc[n * D + f], macc[n * D + f + 1]);
    }
    if (tid < NPB * 3) { const int n = tid / 3, c = tid - n * 3; *(f32x4*)(lds + L_PC + n * 48 + c * 16) = f32x4{pacc[n * 4 + c], 0.f, 0.f, 0.f}; }
  }
  u32x4 w2[8];
  const int u = wv & 3, t = wv >> 2;
#pragma unroll
  for (int q = 0; q < 8; q++) w2[q] = *(const u32x4*)(ws + WS_WT + (size_t)(64 + u * 8 + q) * 1024 + lane * 16);
  __syncthreads();
  STAMP(10);
  {
    const int c = lane & 15, g = lane >> 4, nl = tile_node(t, c);
    f32x4 a0 = *(const f32x4*)(lds + L_CP + (wv * 64 + lane) * 32), a1 = *(const f32x4*)(lds + L_CP + (wv * 64 + lane) * 32 + 16);
    const u32x4 hr = *(const u32x4*)(lds + L_HS + nl * 272 + u * 64 + g * 16);
#pragma unroll
    for (int s = 0; s < 4; s++) {
      const u32x4 mb = *(const u32x4*)(lds + L_MI + nl * 272 + s * 64 + g * 16);
      a0 = mfma16(w2[s], mb, a0);
      a1 = mfma16(w2[4 + s], mb, a1);
    }
    if (wv >= 12 && tid - 768 < NPB * 3) {
      const int i = tid - 768, n = i / 3, cc = i - n * 3;
      const f32x4 p = *(const f32x4*)(lds + L_PC + n * 48 + cc * 16);
      const float p0 = bf2f(*(const u16*)(lds + L_PD + n * 8 + cc * 2));
      if (nb0 + n < N) out[(size_t)N * D + (size_t)nb0 * 3 + i] = (u16)pk_bf16(p0 + LN2 * (p[0] + p[1] + p[2] + p[3]), 0.f);
    }
    float o[8];
#pragma unroll
    for (int i = 0; i < 8; i++) {
      const float y = i < 4 ? a0[i] : a1[i - 4];
      const float hvv = (i & 1) ? bf2f(hr[i >> 1] >> 16) : bf2f(hr[i >> 1] & 0xffffu);
      o[i] = hvv + LN2 * y * __builtin_amdgcn_rcpf(1.f + __builtin_amdgcn_exp2f(-y));
    }
    const u32x4 ov = {pk_bf16(o[0], o[1]), pk_bf16(o[2], o[3]), pk_bf16(o[4], o[5]), pk_bf16(o[6], o[7])};
    const int node = nb0 + nl;
    if ((t < 2 || (t == 2 && c < 8)) && node < N) *(u32x4*)(out + (size_t)node * D + u * 32 + g * 8) = ov;
  }
  STAMP(11); STAMPC(13, 0);
}

extern "C" void kernel_launch(void* const* d_in, const int* in_sizes, int n_in, void* d_out, int out_size, void* d_ws,
                              size_t ws_size, hipStream_t stream) {
  const u16* h = (const u16*)d_in[0];
  const u16* pos = (const u16*)d_in[1];
  const u16* ea = (const u16*)d_in[2];
  const int* src = (const int*)d_in[3];
  const int* dst = (const int*)d_in[4];
  const u16* w_e = (const u16*)d_in[5];
  const u16* w_x = (const u16*)d_in[6];
  const u16* w_h = (const u16*)d_in[7];
  const int N = in_sizes[0] / D, E = in_sizes[3];
  const int NB = (N + NPB - 1) / NPB, C = (E + CH - 1) / CH;
  const int G1 = NB > C ? NB : C;
  k_prep<<<G1, 1024, 0, stream>>>(h, pos, ea, src, dst, w_e, w_h, (char*)d_ws, N, E);
  k_main<<<NB, 1024, 0, stream>>>(h, w_e, w_x, (char*)d_ws, (u16*)d_out, N, C, NB);
}
```

```cpp
#include <hip/hip_runtime.h>
#include <hip/hip_bf16.h>

typedef unsigned int u32;
typedef unsigned short u16;
typedef unsigned long long u64;
typedef u32 u32x2 __attribute__((ext_vector_type(2)));
typedef u32 u32x4 __attribute__((ext_vector_type(4)));
typedef short bf16x8 __attribute__((ext_vector_type(8)));
typedef float f32x2 __attribute__((ext_vector_type(2)));
typedef float f32x4 __attribute__((ext_vector_type(4)));
typedef float f32x16 __attribute__((ext_vector_type(16)));
typedef __bf16 bf16x2_t __attribute__((ext_vector_type(2)));
typedef __attribute__((address_space(1))) u64 gu64;
#define DI __device__ __forceinline__
#define RLX_AGENT __ATOMIC_RELAXED, __HIP_MEMORY_SCOPE_AGENT
#define SC1 16

constexpr int D = 128;
constexpr int NT = 512;
constexpr int NPB = 40;
constexpr int CH = 640;
constexpr int MAXG = 256;
constexpr int CAPW = 128;
constexpr int MAXPOS = 61440;
constexpr float LOG2E = 1.4426950408889634f;
constexpr float LN2 = 0.6931471805599453f;
constexpr u32 OOB = 0xfffff000u;

constexpr size_t WS_EP = 0;
constexpr size_t WS_GR = 4096;
constexpr size_t WS_BT = WS_GR + (size_t)MAXG * MAXG * 8;
constexpr size_t WS_REC = WS_BT + (size_t)MAXG * NPB * 256;

#define STAMP(k) do { } while (0)

DI float bf2f(u32 b) { return __builtin_bit_cast(float, b << 16); }
DI u32 pk_bf16(float lo, float hi) {
  f32x2 v = {lo, hi};
  bf16x2_t r = __builtin_convertvector(v, bf16x2_t);
  return __builtin_bit_cast(u32, r);
}
DI float rbf(float x) { return bf2f(pk_bf16(x, x) & 0xffffu); }
DI f32x4 mfma16(u32x4 a, u32x4 b, f32x4 c) {
  return __builtin_amdgcn_mfma_f32_16x16x32_bf16(__builtin_bit_cast(bf16x8, a), __builtin_bit_cast(bf16x8, b), c, 0, 0, 0);
}
DI f32x16 mfma32(u32x4 a, u32x4 b, f32x16 c) {
  return __builtin_amdgcn_mfma_f32_32x32x16_bf16(__builtin_bit_cast(bf16x8, a), __builtin_bit_cast(bf16x8, b), c, 0, 0, 0);
}
DI __amdgpu_buffer_rsrc_t rsrc(const void* p, u32 bytes) { return __builtin_amdgcn_make_buffer_rsrc((void*)p, 0, bytes, 0x00020000); }
#define SB() __builtin_amdgcn_sched_barrier(0)

constexpr int L_SM = 0;
constexpr int L_CF = L_SM + 8 * 8 * 640;
constexpr int L_ZERO = L_CF + 8 * CAPW * 16;
constexpr int L_CNT = L_ZERO + 16;
constexpr int L_SRC = L_CNT + 48;
constexpr int L_KB = L_SRC + 8 * CAPW * 4 + 64;
constexpr int L_CP = L_KB + 8 * D * 16;
constexpr int L_WF = L_CP + 12 * 64 * 32;
constexpr int L_HS = L_WF + 32768;
constexpr int L_PC = L_HS + NPB * 272;
constexpr int L_WX = L_PC + NPB * 32;
constexpr int L_PD = L_WX + 512;
constexpr int L_BC = L_PD + 256;
constexpr int L_LS = L_BC + 1024;
constexpr int L_END = L_LS + 8192;
constexpr int L_RS = L_CP;
constexpr int L_BS = L_CP + CH * 16;
static_assert(L_END <= 160 * 1024, "lds");
static_assert(MAXPOS <= L_KB, "pos table");
static_assert(L_BS + NPB * 272 <= L_WF, "staging");
static_assert((L_CF % 16) == 0 && (L_SRC % 16) == 0 && (L_KB % 16) == 0 && (L_CP % 16) == 0 && (L_HS % 16) == 0 && (L_PC % 16) == 0 && (L_WX % 16) == 0 && (L_BC % 16) == 0 && (L_LS % 16) == 0, "align");

DI int tile_node(int t, int c) {
  return t < 2 ? ((c & 3) * 8 + 4 * t + (c >> 2)) : (32 + (c & 7));
}
DI int mi_row(int nl) { return L_KB + (nl & 7) * 2048 + (nl >> 3) * 272; }

DI void load_w(u32 (&w)[32], const u16* base, int u, int lane) {
  const u16* wp = base + (size_t)(8 * (lane >> 4)) * D + 32 * u + 2 * (lane & 15);
#pragma unroll
  for (int s = 0; s < 4; s++)
#pragma unroll
    for (int j = 0; j < 8; j++) w[8 * s + j] = *(const u32*)(wp + (32 * s + j) * D);
}
DI void frag_w(const u32 (&w)[32], int s, u32x4& fa, u32x4& fb) {
#pragma unroll
  for (int q = 0; q < 4; q++) {
    fa[q] = __builtin_amdgcn_perm(w[8 * s + 2 * q + 1], w[8 * s + 2 * q], 0x05040100u);
    fb[q] = __builtin_amdgcn_perm(w[8 * s + 2 * q + 1], w[8 * s + 2 * q], 0x07060302u);
  }
}
DI void insert_rec(char* lds, u32x4 rc) {
  u32* wcnt = (u32*)(lds + L_CNT);
  const u32 win = rc[1] & 7u, slot = (rc[1] >> 3) & 7u;
  const u32 rank = atomicAdd(&wcnt[win], 1u);
  if (rank < CAPW) {
    const u32 sp = win * CAPW + (rank & ~15u) + ((rank & 1u) << 3) + ((rank & 15u) >> 1);
    *(u32*)(lds + L_SRC + sp * 4) = rc[0];
    const u32 one = 0x3f80u << ((slot & 1u) * 16);
    const u32x4 cf = {slot < 2 ? one : 0u, (slot >> 1) == 1 ? one : 0u, (slot == 4 ? 0x3f80u : 0u) | (rc[1] & 0xffff0000u),
                      (rc[3] >> 16) | (rc[3] & 0xffff0000u)};
    *(u32x4*)(lds + L_CF + sp * 16) = cf;
    char* sm = lds + L_SM + (sp >> 4) * 640 + slot * 32 + (sp & 15u) * 2;
    *(u16*)(sm) = (u16)0x3f80u;
    *(u16*)(sm + 160) = (u16)rc[2];
    *(u16*)(sm + 320) = (u16)(rc[2] >> 16);
    *(u16*)(sm + 480) = (u16)rc[3];
  } else wcnt[8] = 1;
}
DI u32 silu2(float a, float b) {
  f32x2 yy = {a, b};
  f32x2 e = {__builtin_amdgcn_exp2f(-a), __builtin_amdgcn_exp2f(-b)};
  e += 1.0f;
  f32x2 rc = {__builtin_amdgcn_rcpf(e[0]), __builtin_amdgcn_rcpf(e[1])};
  yy *= rc;
  return pk_bf16(yy[0], yy[1]);
}

__global__ __launch_bounds__(NT, 2) void k_egnn(const u16* __restrict__ h, const u16* __restrict__ pos,
                                                const u16* __restrict__ ea, const int* __restrict__ src,
                                                const int* __restrict__ dst, const u16* __restrict__ w_e,
                                                const u16* __restrict__ w_x, const u16* __restrict__ w_h,
                                                char* __restrict__ ws, u16* __restrict__ out, int N, int E, int G) {
  __shared__ __attribute__((aligned(16))) char lds[L_END];
  const int tid = threadIdx.x, lane = tid & 63, b = blockIdx.x;
  const int wv = __builtin_amdgcn_readfirstlane(tid >> 6);
  const int u = wv & 3, c16 = lane & 15, g4 = lane >> 4;
  const bool role1 = wv >= 4;
  int bk;
  { const int q = G >> 3, r = G & 7, x = b & 7, i = b >> 3;
    bk = (x < r ? x * (q + 1) : r * (q + 1) + (x - r) * q) + i; }
  const int nb0 = bk * NPB;
  STAMP(0);
  const __amdgpu_buffer_rsrc_t r_bt = rsrc(ws + WS_BT, N * 256), r_rec = rsrc(ws + WS_REC, G * CH * 16);
  const __amdgpu_buffer_rsrc_t r_h = rsrc(h, N * 256), r_pos = rsrc(pos, N * 6);

  u32 ed[3], es[3], ee[3];
  {
    const __amdgpu_buffer_rsrc_t r_d = rsrc(dst, E * 4), r_s = rsrc(src, E * 4), r_e = rsrc(ea, E * 2);
#pragma unroll
    for (int i = 0; i < 3; i++) {
      const int k = tid - 256 + 256 * i;
      const u32 e = (role1 && k < CH) ? (u32)(b * CH + k) : 0x20000000u;
      ed[i] = __builtin_amdgcn_raw_buffer_load_b32(r_d, e * 4, 0, 0);
      es[i] = __builtin_amdgcn_raw_buffer_load_b32(r_s, e * 4, 0, 0);
      ee[i] = __builtin_amdgcn_raw_buffer_load_b16(r_e, e * 2, 0, 0);
    }
  }
  SB();
  const u64 epoch = __hip_atomic_load((gu64*)(ws + WS_EP), RLX_AGENT);
  u32x4 pv[8];
#pragma unroll
  for (int i = 0; i < 8; i++) pv[i] = __builtin_amdgcn_raw_buffer_load_b128(r_pos, (i * NT + tid) * 16, 0, 0);
  const u32x4 hv0 = __builtin_amdgcn_raw_buffer_load_b128(r_h, nb0 * 256 + tid * 16, 0, 0);
  const u32x4 hv1 = __builtin_amdgcn_raw_buffer_load_b128(r_h, tid < 128 ? nb0 * 256 + (NT + tid) * 16 : OOB, 0, 0);
  u32 wA[32], wB[32];
  if (!role1) load_w(wA, w_e + D * D, u, lane);
  const u32 wdv = w_e[256 * D + (tid & 127)], wav = w_e[257 * D + (tid & 127)], wxv = w_x[tid & 127];
  if (tid < MAXG) *(u32*)(lds + L_BC + tid * 4) = 0;
  __syncthreads();
  asm volatile("" : "+v"(ed[0]), "+v"(ed[1]), "+v"(ed[2]));
  u32 rank[3], ebk[3];
  if (role1) {
#pragma unroll
    for (int i = 0; i < 3; i++) {
      const int k = tid - 256 + 256 * i;
      ebk[i] = ed[i] / NPB;
      rank[i] = 0;
      if (k < CH && b * CH + k < E) rank[i] = atomicAdd((u32*)(lds + L_BC) + ebk[i], 1u);
    }
  }
#pragma unroll
  for (int i = 0; i < 8; i++) if ((i * NT + tid) * 16 < MAXPOS) *(u32x4*)(lds + (i * NT + tid) * 16) = pv[i];
  *(u32x4*)(lds + L_HS + (tid >> 4) * 272 + (tid & 15) * 16) = hv0;
  if (tid < 128) *(u32x4*)(lds + L_HS + (32 + (tid >> 4)) * 272 + (tid & 15) * 16) = hv1;
  STAMP(1);
  __syncthreads();
  STAMP(12);
  u32x4 hb[3][4];
  u32x4 c4 = {0, 0, 0, 0}, st4 = {0, 0, 0, 0};
  if (!role1) {
    load_w(wB, w_h, u, lane);
#pragma unroll
    for (int t = 0; t < 3; t++)
#pragma unroll
      for (int s = 0; s < 4; s++) hb[t][s] = *(const u32x4*)(lds + L_HS + tile_node(t, c16) * 272 + g4 * 16 + s * 64);
    f32x4 acc[3][2];
#pragma unroll
    for (int t = 0; t < 3; t++) { acc[t][0] = f32x4{0, 0, 0, 0}; acc[t][1] = f32x4{0, 0, 0, 0}; }
#pragma unroll
    for (int s = 0; s < 4; s++) {
      u32x4 fa, fb; frag_w(wA, s, fa, fb);
#pragma unroll
      for (int t = 0; t < 3; t++) { acc[t][0] = mfma16(fa, hb[t][s], acc[t][0]); acc[t][1] = mfma16(fb, hb[t][s], acc[t][1]); }
    }
#pragma unroll
    for (int t = 0; t < 3; t++) {
      u32x4 o;
#pragma unroll
      for (int i = 0; i < 4; i++) o[i] = pk_bf16(acc[t][0][i] * LOG2E, acc[t][1][i] * LOG2E);
      if (t < 2 || c16 < 8) *(u32x4*)(lds + L_BS + tile_node(t, c16) * 272 + u * 64 + g4 * 16) = o;
    }
    if (tid < NPB * 3) *(u16*)(lds + L_PD + tid * 2) = *(const u16*)(lds + min((nb0 * 3 + tid) * 2, MAXPOS - 2));
  } else {
    load_w(wA, w_e, u, lane);
    load_w(wB, w_h + D * D, u, lane);
    c4 = *(const u32x4*)(lds + L_BC + lane * 16);
    const u32 tot = c4[0] + c4[1] + c4[2] + c4[3];
    u32 inc = tot;
#pragma unroll
    for (int o = 1; o < 64; o <<= 1) { const u32 t = __shfl_up(inc, o, 64); if (lane >= o) inc += t; }
    const u32 ex = inc - tot;
    st4 = u32x4{ex, ex + c4[0], ex + c4[0] + c4[1], ex + c4[0] + c4[1] + c4[2]};
    u32* mine = (u32*)(lds + L_LS + wv * 1024);
    *(u32x4*)(mine + lane * 4) = st4;
    __builtin_amdgcn_wave_barrier();
#pragma unroll
    for (int i = 0; i < 3; i++) {
      const int k = tid - 256 + 256 * i;
      if (k < CH && b * CH + k < E) {
        const u16* pd = (const u16*)(lds) + ed[i] * 3;
        const u16* ps = (const u16*)(lds) + es[i] * 3;
        const float dx = rbf(bf2f(pd[0]) - bf2f(ps[0])), dy = rbf(bf2f(pd[1]) - bf2f(ps[1])), dz = rbf(bf2f(pd[2]) - bf2f(ps[2]));
        const float s2 = rbf(dx * dx) + rbf(dy * dy) + rbf(dz * dz);
        const u32x4 rc = {es[i] << 8, (ed[i] - ebk[i] * NPB) | (ee[i] << 16), pk_bf16(dx, dy), pk_bf16(dz, s2)};
        *(u32x4*)(lds + L_RS + (mine[ebk[i]] + rank[i]) * 16) = rc;
      }
    }
#pragma unroll
    for (int t = 0; t < 3; t++)
#pragma unroll
      for (int s = 0; s < 4; s++) hb[t][s] = *(const u32x4*)(lds + L_HS + tile_node(t, c16) * 272 + g4 * 16 + s * 64);
  }
  STAMP(2);
  __syncthreads();
  STAMP(13);
  {
    const u32x4 r0 = *(const u32x4*)(lds + L_RS + tid * 16), b0 = *(const u32x4*)(lds + L_BS + (tid >> 4) * 272 + (tid & 15) * 16);
    const u32x4 r1 = *(const u32x4*)(lds + L_RS + (NT + (tid & 127)) * 16);
    const u32x4 b1 = *(const u32x4*)(lds + L_BS + (32 + ((tid & 127) >> 4)) * 272 + (tid & 15) * 16);
    __builtin_amdgcn_raw_buffer_store_b128(r0, r_rec, (b * CH + tid) * 16, 0, SC1);
    __builtin_amdgcn_raw_buffer_store_b128(b0, r_bt, nb0 * 256 + tid * 16, 0, SC1);
    if (wv < 2) {
      __builtin_amdgcn_raw_buffer_store_b128(r1, r_rec, (b * CH + NT + tid) * 16, 0, SC1);
      __builtin_amdgcn_raw_buffer_store_b128(b1, r_bt, nb0 * 256 + (NT + tid) * 16, 0, SC1);
    }
    const u32x4 z = {0, 0, 0, 0};
#pragma unroll
    for (int i = 0; i < (L_SRC + 8191) / 8192; i++) { const int o = i * 8192 + tid * 16; if (o < L_SRC) *(u32x4*)(lds + o) = z; }
    *(u32x2*)(lds + L_SRC + tid * 8) = u32x2{OOB, OOB};
    if (tid < 16) *(u32*)(lds + L_SRC + 4096 + tid * 4) = OOB;
  }
  STAMP(14);
  asm volatile("s_waitcnt vmcnt(0)" ::: "memory");
  STAMP(15);
  __syncthreads();
  const u64 tag = (epoch + 1) << 20;
  if (wv == 4) {
    gu64* gp = (gu64*)(ws + WS_GR) + (size_t)b * MAXG + lane * 4;
#pragma unroll
    for (int i = 0; i < 4; i++) __hip_atomic_store(gp + i, tag | (st4[i] << 10) | c4[i], RLX_AGENT);
  }
  STAMP(3);
  {
    f32x4 acc[3][2];
#pragma unroll
    for (int t = 0; t < 3; t++) { acc[t][0] = f32x4{0, 0, 0, 0}; acc[t][1] = f32x4{0, 0, 0, 0}; }
    if (!role1) {
#pragma unroll
      for (int s = 0; s < 4; s++) {
        u32x4 fa, fb; frag_w(wB, s, fa, fb);
#pragma unroll
        for (int t = 0; t < 3; t++) { acc[t][0] = mfma16(fa, hb[t][s], acc[t][0]); acc[t][1] = mfma16(fb, hb[t][s], acc[t][1]); }
      }
#pragma unroll
      for (int t = 0; t < 3; t++) {
        *(f32x4*)(lds + L_CP + ((t * 4 + u) * 64 + lane) * 32) = acc[t][0] * LOG2E;
        *(f32x4*)(lds + L_CP + ((t * 4 + u) * 64 + lane) * 32 + 16) = acc[t][1] * LOG2E;
      }
    } else {
#pragma unroll
      for (int s = 0; s < 4; s++) {
        u32x4 fa, fb; frag_w(wA, s, fa, fb);
#pragma unroll
        for (int t = 0; t < 3; t++) { acc[t][0] = mfma16(hb[t][s], fa, acc[t][0]); acc[t][1] = mfma16(hb[t][s], fb, acc[t][1]); }
      }
#pragma unroll
      for (int tb = 0; tb < 2; tb++) {
        char* kb = lds + L_KB + (32 * u + 2 * c16 + tb) * 16;
#pragma unroll
        for (int t = 0; t < 2; t++) {
          const f32x4 a = acc[t][tb] * LOG2E;
          *(u32x2*)(kb + (4 * t + g4) * 2048) = u32x2{pk_bf16(a[0], a[1]), pk_bf16(a[2], a[3])};
        }
        if (g4 < 2) {
#pragma unroll
          for (int i = 0; i < 4; i++) *(u16*)(kb + (4 * g4 + i) * 2048 + 8) = (u16)pk_bf16(acc[2][tb][i] * LOG2E, 0.f);
        }
      }
#pragma unroll
      for (int s = 0; s < 4; s++) {
        u32x4 fa, fb; frag_w(wB, s, fa, fb);
        *(u32x4*)(lds + L_WF + ((u * 2) * 4 + s) * 1024 + lane * 16) = fa;
        *(u32x4*)(lds + L_WF + ((u * 2 + 1) * 4 + s) * 1024 + lane * 16) = fb;
      }
    }
    const float wd = bf2f(wdv) * LOG2E, wdh = rbf(wd);
    const u32 wdp = pk_bf16(wdh, wd - wdh), wap = pk_bf16(bf2f(wav) * LOG2E, 0.f);
#pragma unroll
    for (int i = 0; i < 2; i++) {
      char* kb = lds + L_KB + (i * NT + tid) * 16;
      *(u32*)(kb + 12) = wdp;
      *(u16*)(kb + 10) = (u16)wap;
    }
    if (tid < D) *(float*)(lds + L_WX + tid * 4) = bf2f(wxv);
  }
  STAMP(4);
  {
    const int cs = wv * 32 + (lane & 31);
    const bool on = lane < 32 && cs < G;
    gu64* gp = (gu64*)(ws + WS_GR) + (size_t)(cs < G ? cs : G - 1) * MAXG + bk;
    u64 gv;
    for (u32 spins = 0;; spins++) {
      gv = __hip_atomic_load(gp, RLX_AGENT);
      if (__all((gv >> 20) == (tag >> 20)) || spins > (1u << 20)) break;
      __builtin_amdgcn_s_sleep(1);
    }
    STAMP(5);
    const u32 cnt = on ? ((u32)gv & 1023u) : 0u, st = ((u32)gv >> 10) & 1023u;
    if (on) *(u32*)(lds + L_BC + cs * 4) = (u32)gv & 0xfffffu;
    u32 inc = cnt;
#pragma unroll
    for (int o = 1; o < 32; o <<= 1) { const u32 t = __shfl_up(inc, o, 64); if (lane >= o) inc += t; }
    const u32 total = __builtin_amdgcn_readlane(inc, 31);
    u32* list = (u32*)(lds + L_LS + wv * 1024);
    const u32 base = (cs * CH + st) * 16;
    for (u32 done = 0; done < total; done += 128) {
      if (*(volatile u32*)(lds + L_CNT + 32)) break;
      const u32 p0 = inc - cnt;
      u32 k = p0 < done ? done - p0 : 0u;
      for (; k < cnt && p0 + k < done + 128; k++) list[p0 + k - done] = base + k * 16;
      __builtin_amdgcn_wave_barrier();
      const u32 a0 = list[lane], a1 = list[64 + lane];
      const bool v0 = done + lane < total, v1 = done + 64 + lane < total;
      const u32x4 r0 = __builtin_amdgcn_raw_buffer_load_b128(r_rec, v0 ? a0 : OOB, 0, SC1);
      const u32x4 r1 = __builtin_amdgcn_raw_buffer_load_b128(r_rec, v1 ? a1 : OOB, 0, SC1);
      if (v0) insert_rec(lds, r0);
      if (v1) insert_rec(lds, r1);
      __builtin_amdgcn_wave_barrier();
    }
  }
  STAMP(6);
  __syncthreads();
  STAMP(8);
  if (b == 0 && tid == 0) __hip_atomic_store((gu64*)(ws + WS_EP), epoch + 1, RLX_AGENT);
  const u32* wcnt = (const u32*)(lds + L_CNT);
  const int ovf = __builtin_amdgcn_readfirstlane(wcnt[8]);
  const int r = lane & 31, hh = lane >> 5;
  if (!ovf) {
    int win;
    {
      const u32x4 ca = *(const u32x4*)(wcnt), cb = *(const u32x4*)(wcnt + 4);
      const u32 mine = wcnt[lane & 7] * 8 + (lane & 7);
      u32 rk = 0;
#pragma unroll
      for (int j = 0; j < 4; j++) { rk += (ca[j] * 8 + j) > mine; rk += (cb[j] * 8 + 4 + j) > mine; }
      const u64 m = __ballot(rk == (u32)(wv < 4 ? wv : 11 - wv));
      win = __builtin_ctzll(m);
    }
    const int cntw = __builtin_amdgcn_readfirstlane(wcnt[win]);
    const int nsteps = (cntw + 15) >> 4;
    const u32 lo = r * 4;
    const u32 src0 = L_SRC + win * CAPW * 4 + hh * 32, srcz = L_SRC + 4096 + hh * 32;
    u32 G0[16], G1[16];
    auto gather = [&](u32 (&Gx)[16], u32 a) {
      const u32x4 i0 = *(const u32x4*)(lds + a), i1 = *(const u32x4*)(lds + a + 16);
#pragma unroll
      for (int j = 0; j < 8; j++) {
        const u32 o = (j < 4 ? i0[j & 3] : i1[j & 3]) + lo;
        Gx[j] = __builtin_amdgcn_raw_buffer_load_b32(r_bt, o, 0, SC1);
        Gx[8 + j] = __builtin_amdgcn_raw_buffer_load_b32(r_bt, o + 128, 0, SC1);
      }
    };
    f32x16 aA0, aA1, aB0, aB1, zero;
#pragma unroll
    for (int i = 0; i < 16; i++) { aA0[i] = 0.f; aA1[i] = 0.f; aB0[i] = 0.f; aB1[i] = 0.f; zero[i] = 0.f; }
    const int rh = (r >> 2) & 1, rp = r >> 4, rj = (r & 3) | (((r >> 3) & 1) << 2);
    u32x4 sel0, sel1;
    {
      const u32 one = rh == hh ? 0x3f80u << (rp * 16) : 0u;
#pragma unroll
      for (int q = 0; q < 4; q++) { sel0[q] = (rj == q) ? one : 0u; sel1[q] = (rj == 4 + q) ? one : 0u; }
    }
    const u32x4 kbA = *(const u32x4*)(lds + L_KB + win * 2048 + (2 * r + hh) * 16);
    const u32x4 kbB = *(const u32x4*)(lds + L_KB + win * 2048 + (64 + 2 * r + hh) * 16);
    const bool cfon = rp == hh;
    u32 cfa = cfon ? (L_CF + (win * CAPW + 8 * rh + rj) * 16) : L_ZERO;
    const u32 cfs = cfon ? 256u : 0u;
    u32 sma = L_SM + win * 8 * 640 + r * 32 + hh * 16;

    if (nsteps > 0) {
      gather(G0, src0);
      SB();
      gather(G1, src0 + 64);
      SB();
      STAMP(7);
      u32x4 cf = *(const u32x4*)(lds + cfa), sf = {0, 0, 0, 0}, mB0 = {0, 0, 0, 0}, mB1 = {0, 0, 0, 0}, mA0, mA1;
      f32x16 yA = mfma32(cf, kbA, zero), yB;
      yA = mfma32(sel0, u32x4{G0[0], G0[1], G0[2], G0[3]}, yA);
      yA = mfma32(sel1, u32x4{G0[4], G0[5], G0[6], G0[7]}, yA);
      auto step = [&](u32 (&Gc)[16], u32 (&Gn)[16], int s) {
        SB();
        yB = mfma32(cf, kbB, zero);
        SB();
        mA0[0] = silu2(yA[0], yA[1]); mA0[1] = silu2(yA[2], yA[3]);
        SB();
        yB = mfma32(sel0, u32x4{Gc[8], Gc[9], Gc[10], Gc[11]}, yB);
        cfa += cfs;
        cf = *(const u32x4*)(lds + cfa);
        SB();
        mA0[2] = silu2(yA[4], yA[5]); mA0[3] = silu2(yA[6], yA[7]);
        SB();
        yB = mfma32(sel1, u32x4{Gc[12], Gc[13], Gc[14], Gc[15]}, yB);
        SB();
        mA1[0] = silu2(yA[8], yA[9]);
        SB();
        aB0 = mfma32(mB0, sf, aB0);
        gather(Gc, s + 2 < nsteps ? src0 + (s + 2) * 64 : srcz);
        SB();
        mA1[1] = silu2(yA[10], yA[11]); mA1[2] = silu2(yA[12], yA[13]);
        SB();
        aB1 = mfma32(mB1, sf, aB1);
        sf = *(const u32x4*)(lds + sma);
        sma += 640;
        SB();
        mA1[3] = silu2(yA[14], yA[15]);
        SB();
        yA = mfma32(cf, kbA, zero);
        SB();
        mB0[0] = silu2(yB[0], yB[1]); mB0[1] = silu2(yB[2], yB[3]);
        SB();
        yA = mfma32(sel0, u32x4{Gn[0], Gn[1], Gn[2], Gn[3]}, yA);
        SB();
        mB0[2] = silu2(yB[4], yB[5]); mB0[3] = silu2(yB[6], yB[7]);
        SB();
        yA = mfma32(sel1, u32x4{Gn[4], Gn[5], Gn[6], Gn[7]}, yA);
        SB();
        mB1[0] = silu2(yB[8], yB[9]);
        SB();
        aA0 = mfma32(mA0, sf, aA0);
        SB();
        mB1[1] = silu2(yB[10], yB[11]); mB1[2] = silu2(yB[12], yB[13]);
        SB();
        aA1 = mfma32(mA1, sf, aA1);
        SB();
        mB1[3] = silu2(yB[14], yB[15]);
        SB();
      };
      int s = 0;
      for (; s + 1 < nsteps; s += 2) { step(G0, G1, s); step(G1, G0, s + 1); }
      if (s < nsteps) step(G0, G1, s);
      aB0 = mfma32(mB0, sf, aB0);
      aB1 = mfma32(mB1, sf, aB1);
    }
    STAMP(9);
    float t = 0.f;
#pragma unroll
    for (int fh = 0; fh < 2; fh++) {
      const f32x16& a0 = fh ? aB0 : aA0;
      const f32x16& a1 = fh ? aB1 : aA1;
#pragma unroll
      for (int q4 = 0; q4 < 4; q4++) {
        const f32x4 wa = *(const f32x4*)(lds + L_WX + (64 * fh + 16 * q4 + 8 * hh) * 4);
        const f32x4 wb = *(const f32x4*)(lds + L_WX + (64 * fh + 16 * q4 + 8 * hh) * 4 + 16);
        t += a0[4 * q4] * wa[0] + a1[4 * q4] * wa[1] + a0[4 * q4 + 1] * wa[2] + a1[4 * q4 + 1] * wa[3]
           + a0[4 * q4 + 2] * wb[0] + a1[4 * q4 + 2] * wb[1] + a0[4 * q4 + 3] * wb[2] + a1[4 * q4 + 3] * wb[3];
        if (r < 5) {
          u32x4 v;
#pragma unroll
          for (int q = 0; q < 4; q++) v[q] = pk_bf16(a0[4 * q4 + q], a1[4 * q4 + q]);
          *(u32x4*)(lds + L_KB + win * 2048 + r * 272 + (64 * fh + 16 * q4 + 8 * hh) * 2) = v;
        }
      }
    }
    if (r >= 5 && r < 20) {
      const int kind = r / 5, slot = r - kind * 5;
      *(float*)(lds + L_PC + (slot * 8 + win) * 32 + (kind - 1) * 8 + hh * 4) = t;
    }
  } else {
    float* macc = (float*)(lds + L_SM);
    float* pacc = (float*)(lds + L_SM + NPB * D * 4);
    __syncthreads();
    for (int i = tid; i < NPB * D + NPB * 4; i += NT) macc[i] = 0.f;
    __syncthreads();
    const float wx0 = *(const float*)(lds + L_WX + lane * 8), wx1 = *(const float*)(lds + L_WX + lane * 8 + 4);
    const u32x4 kc0 = *(const u32x4*)(lds + L_KB + (2 * lane) * 16), kc1 = *(const u32x4*)(lds + L_KB + (2 * lane + 1) * 16);
    const float wd0 = bf2f(kc0[3] & 0xffffu) + bf2f(kc0[3] >> 16), wd1 = bf2f(kc1[3] & 0xffffu) + bf2f(kc1[3] >> 16);
    const float wa0 = bf2f(kc0[2] >> 16), wa1 = bf2f(kc1[2] >> 16);
    u32 rot = 0;
    for (int c = 0; c < G; c++) {
      const u32 oo = __builtin_amdgcn_readfirstlane(*(const u32*)(lds + L_BC + c * 4));
      const u32 s0 = oo >> 10, n = oo & 1023u;
      for (u32 k = (wv - rot) & 7u; k < n; k += 8) {
        const u32x4 rc = __builtin_amdgcn_raw_buffer_load_b128(r_rec, (c * CH + s0 + k) * 16, 0, SC1);
        const u32 nl = rc[1] & 0x3fu, slot = nl >> 3, wn = nl & 7u;
        const u32 bv = __builtin_amdgcn_raw_buffer_load_b32(r_bt, rc[0] + lane * 4, 0, SC1);
        const float d2 = bf2f(rc[3] >> 16), eav = bf2f(rc[1] >> 16);
        const float a0 = bf2f(*(const u16*)(lds + L_KB + wn * 2048 + (2 * lane) * 16 + slot * 2));
        const float a1 = bf2f(*(const u16*)(lds + L_KB + wn * 2048 + (2 * lane + 1) * 16 + slot * 2));
        const float y0 = a0 + bf2f(bv & 0xffffu) + d2 * wd0 + eav * wa0, y1 = a1 + bf2f(bv >> 16) + d2 * wd1 + eav * wa1;
        const u32 mp = silu2(y0, y1);
        const float m0 = bf2f(mp & 0xffffu), m1 = bf2f(mp >> 16);
        atomicAdd(&macc[nl * D + 2 * lane], m0);
        atomicAdd(&macc[nl * D + 2 * lane + 1], m1);
        float csv = m0 * wx0 + m1 * wx1;
#pragma unroll
        for (int o = 32; o > 0; o >>= 1) csv += __shfl_xor(csv, o, 64);
        if (lane < 3) atomicAdd(&pacc[nl * 4 + lane], csv * bf2f(lane == 0 ? (rc[2] & 0xffffu) : lane == 1 ? (rc[2] >> 16) : (rc[3] & 0xffffu)));
      }
      rot += n;
    }
    __syncthreads();
    for (int i = tid; i < NPB * D / 2; i += NT) {
      const int n = i >> 6, f = (i & 63) * 2;
      *(u32*)(lds + mi_row(n) + f * 2) = pk_bf16(macc[n * D + f], macc[n * D + f + 1]);
    }
    if (tid < NPB * 3) { const int n = tid / 3, c = tid - n * 3; *(f32x2*)(lds + L_PC + n * 32 + c * 8) = f32x2{pacc[n * 4 + c], 0.f}; }
  }
  __syncthreads();
  STAMP(10);
  {
    u32x4 w2[8];
#pragma unroll
    for (int q = 0; q < 8; q++) w2[q] = *(const u32x4*)(lds + L_WF + (u * 8 + q) * 1024 + lane * 16);
    auto tile = [&](int t) {
      const int nl = tile_node(t, c16);
      f32x4 a0 = *(const f32x4*)(lds + L_CP + ((t * 4 + u) * 64 + lane) * 32), a1 = *(const f32x4*)(lds + L_CP + ((t * 4 + u) * 64 + lane) * 32 + 16);
      const u32x4 hr = *(const u32x4*)(lds + L_HS + nl * 272 + u * 64 + g4 * 16);
#pragma unroll
      for (int s = 0; s < 4; s++) {
        const u32x4 mb = *(const u32x4*)(lds + mi_row(nl) + s * 64 + g4 * 16);
        a0 = mfma16(w2[s], mb, a0);
        a1 = mfma16(w2[4 + s], mb, a1);
      }
      u32x4 ov;
#pragma unroll
      for (int i = 0; i < 4; i++) {
        const float y0 = a0[i], y1 = a1[i];
        ov[i] = pk_bf16(bf2f(hr[i] & 0xffffu) + LN2 * y0 * __builtin_amdgcn_rcpf(1.f + __builtin_amdgcn_exp2f(-y0)),
                        bf2f(hr[i] >> 16) + LN2 * y1 * __builtin_amdgcn_rcpf(1.f + __builtin_amdgcn_exp2f(-y1)));
      }
      const int node = nb0 + nl;
      if ((t < 2 || c16 < 8) && node < N) *(u32x4*)(out + (size_t)node * D + u * 32 + g4 * 8) = ov;
    };
    tile(role1 ? 1 : 0);
    if (!role1) tile(2);
    else if (tid - 256 < NPB * 3) {
      const int i = tid - 256, n = i / 3, cc = i - n * 3;
      const f32x2 p = *(const f32x2*)(lds + L_PC + n * 32 + cc * 8);
      const float p0 = bf2f(*(const u16*)(lds + L_PD + i * 2));
      if (nb0 + n < N) out[(size_t)N * D + (size_t)nb0 * 3 + i] = (u16)pk_bf16(p0 + LN2 * (p[0] + p[1]), 0.f);
    }
  }
  STAMP(11);
}

extern "C" void kernel_launch(void* const* d_in, const int* in_sizes, int n_in, void* d_out, int out_size, void* d_ws,
                              size_t ws_size, hipStream_t stream) {
  const int N = in_sizes[0] / D, E = in_sizes[3];
  const int NB = (N + NPB - 1) / NPB, C = (E + CH - 1) / CH;
  const int G = NB > C ? NB : C;
  k_egnn<<<G, NT, 0, stream>>>((const u16*)d_in[0], (const u16*)d_in[1], (const u16*)d_in[2], (const int*)d_in[3],
                               (const int*)d_in[4], (const u16*)d_in[5], (const u16*)d_in[6], (const u16*)d_in[7],
                               (char*)d_ws, (u16*)d_out, N, E, G);
}
```

```cpp
#include <hip/hip_runtime.h>
#include <hip/hip_bf16.h>

typedef unsigned int u32;
typedef unsigned short u16;
typedef unsigned long long u64;
typedef u32 u32x2 __attribute__((ext_vector_type(2)));
typedef u32 u32x4 __attribute__((ext_vector_type(4)));
typedef short s16x4 __attribute__((ext_vector_type(4)));
typedef short bf16x8 __attribute__((ext_vector_type(8)));
typedef float f32x2 __attribute__((ext_vector_type(2)));
typedef float f32x4 __attribute__((ext_vector_type(4)));
typedef float f32x16 __attribute__((ext_vector_type(16)));
typedef __bf16 bf16x2_t __attribute__((ext_vector_type(2)));
typedef __attribute__((address_space(1))) u64 gu64;
typedef __attribute__((address_space(3))) s16x4 lds_s16x4;
typedef __attribute__((address_space(3))) char lds_char;
#define DI __device__ __forceinline__
#define RLX_AGENT __ATOMIC_RELAXED, __HIP_MEMORY_SCOPE_AGENT
#define SC1 16
#define SB() __builtin_amdgcn_sched_barrier(0)
#define PIN(x) asm volatile("" : "+v"(x))

constexpr int D = 128;
constexpr int NT = 512;
constexpr int NPB = 40;
constexpr int CH = 640;
constexpr int MAXG = 256;
constexpr int CAPW = 128;
constexpr int MAXPOS = 61440;
constexpr float LOG2E = 1.4426950408889634f;
constexpr float LN2 = 0.6931471805599453f;
constexpr u32 OOB = 0xfffff000u;

constexpr size_t WS_EP = 0;
constexpr size_t WS_SLOT0 = (size_t)2 << 20, WS_SLOT = (size_t)6 << 20;
constexpr size_t WS_GR = 0;
constexpr size_t WS_BT = WS_GR + (size_t)MAXG * MAXG * 8;
constexpr size_t WS_REC = WS_BT + (size_t)MAXG * NPB * 256;
static_assert(WS_REC + (size_t)MAXG * CH * 16 <= WS_SLOT, "slot");

#define STAMP(k) do { } while (0)

DI float bf2f(u32 b) { return __builtin_bit_cast(float, b << 16); }
DI float bfhi(u32 b) { return __builtin_bit_cast(float, b & 0xffff0000u); }
DI u32 pk_bf16(float lo, float hi) {
  f32x2 v = {lo, hi};
  bf16x2_t r = __builtin_convertvector(v, bf16x2_t);
  return __builtin_bit_cast(u32, r);
}
DI f32x4 mfma16(u32x4 a, u32x4 b, f32x4 c) {
  return __builtin_amdgcn_mfma_f32_16x16x32_bf16(__builtin_bit_cast(bf16x8, a), __builtin_bit_cast(bf16x8, b), c, 0, 0, 0);
}
DI f32x16 mfma32(u32x4 a, u32x4 b, f32x16 c) {
  return __builtin_amdgcn_mfma_f32_32x32x16_bf16(__builtin_bit_cast(bf16x8, a), __builtin_bit_cast(bf16x8, b), c, 0, 0, 0);
}
DI __amdgpu_buffer_rsrc_t rsrc(const void* p, u32 bytes) { return __builtin_amdgcn_make_buffer_rsrc((void*)p, 0, bytes, 0x00020000); }
DI u32x4 srd(const void* p, u32 bytes) { const u64 a = (u64)p; return u32x4{(u32)a, (u32)(a >> 32) & 0xffffu, bytes, 0x00020000u}; }
DI void dma16(u32x4 sd, u32 voff, u32 soff, u32 ldsa) {
  u32 keep;
  asm volatile("s_mov_b32 %0, m0\n\ts_mov_b32 m0, %1\n\ts_nop 0\n\tbuffer_load_dwordx4 %2, %3, %4 offen lds\n\ts_mov_b32 m0, %0"
               : "=&s"(keep) : "s"(ldsa), "v"(voff), "s"(sd), "s"(soff) : "memory");
}
DI u32 scan64(u32 x) {
  x += __builtin_amdgcn_update_dpp(0u, x, 0x111, 0xf, 0xf, false);
  x += __builtin_amdgcn_update_dpp(0u, x, 0x112, 0xf, 0xf, false);
  x += __builtin_amdgcn_update_dpp(0u, x, 0x114, 0xf, 0xf, false);
  x += __builtin_amdgcn_update_dpp(0u, x, 0x118, 0xf, 0xf, false);
  x += __builtin_amdgcn_update_dpp(0u, x, 0x142, 0xa, 0xf, false);
  x += __builtin_amdgcn_update_dpp(0u, x, 0x143, 0xc, 0xf, false);
  return x;
}

constexpr int L_SM = 0;
constexpr int L_CF = L_SM + 8 * 8 * 640;
constexpr int L_ZERO = L_CF + 8 * CAPW * 16;
constexpr int L_CNT = L_ZERO + 16;
constexpr int L_SRC = L_CNT + 48;
constexpr int L_KB = L_SRC + 8 * CAPW * 4 + 64;
constexpr int L_W0 = L_KB + 8 * D * 16;
constexpr int L_W1 = L_W0 + 32768;
constexpr int L_HS = L_W1 + 32768;
constexpr int L_WX = L_HS + NPB * 256;
constexpr int L_PD = L_WX + 256;
constexpr int L_BC = L_PD + 256;
constexpr int L_LS = L_BC + 1024;
constexpr int L_DUMP = L_LS + 4096;
constexpr int L_END = L_DUMP + 16;
constexpr int L_RS = L_W1;
static_assert(L_END <= 160 * 1024, "lds");
static_assert(MAXPOS <= L_KB, "pos table");
static_assert((L_CF % 16) == 0 && (L_SRC % 16) == 0 && (L_KB % 16) == 0 && (L_W0 % 16) == 0 && (L_HS % 16) == 0 && (L_WX % 16) == 0 && (L_BC % 16) == 0 && (L_LS % 16) == 0, "align");

DI int tile_node(int t, int c) { return t < 2 ? ((c & 3) * 8 + 4 * t + (c >> 2)) : (32 + (c & 7)); }
DI int mi_row(int nl) { return L_KB + (nl & 7) * 2048 + (nl & 3) * 64 + (nl >> 3) * 272; }
DI int wswz(int k) { return ((k & 3) << 2) | ((k >> 2) & 3); }
DI int hswz(int n) { return ((n & 3) << 2) | ((n >> 3) & 3); }
DI u32x4 tr_frag(const char* img, int s, int lane, int ch, int hf) {
  const int k0 = 8 * (lane >> 4) + ((lane & 15) >> 2), k1 = k0 + 4;
  const s16x4 a = __builtin_amdgcn_ds_read_tr16_b64_v4i16((lds_s16x4*)(img + (32 * s + k0) * 256 + 16 * (ch ^ wswz(k0)) + 8 * hf));
  const s16x4 b = __builtin_amdgcn_ds_read_tr16_b64_v4i16((lds_s16x4*)(img + (32 * s + k1) * 256 + 16 * (ch ^ wswz(k1)) + 8 * hf));
  const u32x2 x = __builtin_bit_cast(u32x2, a), y = __builtin_bit_cast(u32x2, b);
  return u32x4{x[0], x[1], y[0], y[1]};
}
DI u32x4 h_frag(const char* lds, int t, int s, int lane) {
  const int n = tile_node(t, lane & 15);
  return *(const u32x4*)(lds + L_HS + n * 256 + 16 * ((4 * s + (lane >> 4)) ^ hswz(n)));
}
DI void insert_rec(char* lds, u32x4 rc) {
  u32* wcnt = (u32*)(lds + L_CNT);
  const u32 win = rc[1] & 7u, slot = (rc[1] >> 3) & 7u;
  const u32 rank = atomicAdd(&wcnt[win], 1u);
  const u32 one = 0x3f80u << ((slot & 1u) * 16);
  const u32x4 cf = {slot < 2 ? one : 0u, (slot >> 1) == 1 ? one : 0u, (slot == 4 ? 0x3f80u : 0u) | (rc[1] & 0xffff0000u),
                    (rc[3] >> 16) | (rc[3] & 0xffff0000u)};
  if (rank < CAPW) {
    const u32 k = ((rank & 1u) << 3) + ((rank & 15u) >> 1), sp = win * CAPW + (rank & ~15u) + k;
    *(u32*)(lds + L_SRC + sp * 4) = rc[0];
    *(u32x4*)(lds + L_CF + sp * 16) = cf;
    char* sm = lds + L_SM + (sp >> 4) * 640 + (rank & 1u) * 320 + slot * 16 + (rank & 14u);
    *(u16*)(sm) = (u16)0x3f80u;
    *(u16*)(sm + 80) = (u16)rc[2];
    *(u16*)(sm + 160) = (u16)(rc[2] >> 16);
    *(u16*)(sm + 240) = (u16)rc[3];
  } else wcnt[8] = 1;
}
DI u32 silu2(float a, float b) {
  f32x2 yy = {a, b};
  f32x2 e = {__builtin_amdgcn_exp2f(-a), __builtin_amdgcn_exp2f(-b)};
  e += 1.0f;
  f32x2 rc = {__builtin_amdgcn_rcpf(e[0]), __builtin_amdgcn_rcpf(e[1])};
  yy *= rc;
  return pk_bf16(yy[0], yy[1]);
}

__global__ __launch_bounds__(NT, 2) void k_egnn(const u16* __restrict__ h, const u16* __restrict__ pos,
                                                const u16* __restrict__ ea, const int* __restrict__ src,
                                                const int* __restrict__ dst, const u16* __restrict__ w_e,
                                                const u16* __restrict__ w_x, const u16* __restrict__ w_h,
                                                char* __restrict__ ws, u16* __restrict__ out, int N, int E, int G) {
  __shared__ __attribute__((aligned(16))) char lds[L_END];
  const int tid = threadIdx.x, lane = tid & 63, b = blockIdx.x;
  const int wv = __builtin_amdgcn_readfirstlane(tid >> 6);
  const int u = wv & 3, c16 = lane & 15, g4 = lane >> 4;
  const bool role1 = wv >= 4;
  const u32 lb = (u32)(size_t)(lds_char*)lds;
  STAMP(0);
  u32 ed[3], es[3], ee[3];
  const int ne = min(CH, E - b * CH);
  {
    const __amdgpu_buffer_rsrc_t r_d = rsrc(dst + b * CH, ne * 4), r_s = rsrc(src + b * CH, ne * 4), r_e = rsrc(ea + b * CH, ne * 2);
    const u32 k = role1 ? (u32)(tid - 256) : 0x20000000u;
#pragma unroll
    for (int i = 0; i < 3; i++) {
      ed[i] = __builtin_amdgcn_raw_buffer_load_b32(r_d, k * 4 + i * 1024, 0, 0);
      es[i] = __builtin_amdgcn_raw_buffer_load_b32(r_s, k * 4 + i * 1024, 0, 0);
      ee[i] = __builtin_amdgcn_raw_buffer_load_b16(r_e, k * 2 + i * 512, 0, 0);
    }
  }
  const u64 epoch = __hip_atomic_load((gu64*)(ws + WS_EP), RLX_AGENT);
  SB();
  int bk;
  { const int q = G >> 3, r = G & 7, x = b & 7, i = b >> 3;
    bk = (x < r ? x * (q + 1) : r * (q + 1) + (x - r) * q) + i; }
  const int nb0 = bk * NPB;
  const u32 wvo = g4 * 256 + 16 * (c16 ^ ((g4 << 2) | u));
  {
    const u32x4 s_pos = srd(pos, N * 6), s_h = srd(h, N * 256), s_we = srd(w_e, 258 * 256);
    if (!role1) {
#pragma unroll
      for (int i = 0; i < 15; i++) dma16(s_pos, lane * 16, (u + 4 * i) * 1024, lb + (u + 4 * i) * 1024);
#pragma unroll
      for (int i = 0; i < 3; i++)
        if (i < 2 || u < 2) dma16(s_h, g4 * 256 + 16 * (c16 ^ ((g4 << 2) | (((u + 4 * i) >> 1) & 3))), nb0 * 256 + (u + 4 * i) * 1024, lb + L_HS + (u + 4 * i) * 1024);
#pragma unroll
      for (int i = 0; i < 8; i++) dma16(s_we, wvo, 32768 + (u + 4 * i) * 1024, lb + L_W0 + (u + 4 * i) * 1024);
    }
  }
  u32 wdv = w_e[256 * D + (tid & 127)], wav = w_e[257 * D + (tid & 127)], wxv = w_x[tid & 127];
  u32 pf = __builtin_amdgcn_raw_buffer_load_b32(rsrc(wv < 2 ? (const void*)w_e : (const void*)w_h, wv < 2 ? 32768 : 65536),
                                                (wv < 6 && lane < 4) ? (((b >> 3) & 31) * 4 + lane + (wv < 2 ? wv : wv - 2) * 128) * 128 : OOB, 0, 0);
  if (tid < MAXG) *(u32*)(lds + L_BC + tid * 4) = 0;
  __syncthreads();
  PIN(ed[0]); PIN(ed[1]); PIN(ed[2]);
  u32 rank[3], ebk[3];
  if (role1) {
#pragma unroll
    for (int i = 0; i < 3; i++) {
      ebk[i] = ed[i] / NPB;
      rank[i] = 0;
      if (tid - 256 + 256 * i < ne) rank[i] = atomicAdd((u32*)(lds + L_BC) + ebk[i], 1u);
    }
  }
  const u32 ep_lo = __builtin_amdgcn_readfirstlane((u32)epoch), ep_hi = __builtin_amdgcn_readfirstlane((u32)(epoch >> 32));
  const u64 ep = ((u64)ep_hi << 32) | ep_lo;
  char* const slot = ws + WS_SLOT0 + (size_t)(ep_lo & 7u) * WS_SLOT;
  const __amdgpu_buffer_rsrc_t r_bt = rsrc(slot + WS_BT, N * 256), r_rec = rsrc(slot + WS_REC, G * CH * 16);
  STAMP(1);
  asm volatile("s_waitcnt vmcnt(0)" ::: "memory");
  PIN(wdv); PIN(wav); PIN(wxv); PIN(pf); PIN(ee[0]); PIN(ee[1]); PIN(ee[2]);
  __syncthreads();
  STAMP(12);
  if (!role1) {
    f32x4 acc[3][2];
#pragma unroll
    for (int t = 0; t < 3; t++) { acc[t][0] = f32x4{0, 0, 0, 0}; acc[t][1] = f32x4{0, 0, 0, 0}; }
#pragma unroll
    for (int s = 0; s < 4; s++) {
      const u32x4 fa = tr_frag(lds + L_W0, s, lane, 4 * u + (lane & 3), 0), fb = tr_frag(lds + L_W0, s, lane, 4 * u + (lane & 3), 1);
#pragma unroll
      for (int t = 0; t < 3; t++) {
        const u32x4 hb = h_frag(lds, t, s, lane);
        acc[t][0] = mfma16(fa, hb, acc[t][0]); acc[t][1] = mfma16(fb, hb, acc[t][1]);
      }
    }
#pragma unroll
    for (int t = 0; t < 3; t++) {
      const f32x4 x = acc[t][0] * LOG2E, y = acc[t][1] * LOG2E;
      const u32x4 o = {pk_bf16(x[0], x[1]), pk_bf16(x[2], x[3]), pk_bf16(y[0], y[1]), pk_bf16(y[2], y[3])};
      __builtin_amdgcn_raw_buffer_store_b128(o, r_bt, (t < 2 || c16 < 8) ? (nb0 + tile_node(t, c16)) * 256 + u * 64 + g4 * 16 : OOB, 0, SC1);
    }
    if (tid < NPB * 3) *(u16*)(lds + L_PD + tid * 2) = *(const u16*)(lds + min((nb0 * 3 + tid) * 2, MAXPOS - 2));
  } else {
    const u32x4 c4 = *(const u32x4*)(lds + L_BC + lane * 16);
    u32x2 pd[3], ps[3];
#pragma unroll
    for (int i = 0; i < 3; i++) {
      const u32* qd = (const u32*)(lds + ((ed[i] * 6) & ~3u));
      const u32* qs = (const u32*)(lds + ((es[i] * 6) & ~3u));
      pd[i] = u32x2{qd[0], qd[1]}; ps[i] = u32x2{qs[0], qs[1]};
    }
    const u32 tot = c4[0] + c4[1] + c4[2] + c4[3];
    const u32 ex = scan64(tot) - tot;
    u32* mine = (u32*)(lds + L_LS + u * 1024);
    *(u32x4*)(mine + lane * 4) = u32x4{ex, ex + c4[0], ex + c4[0] + c4[1], ex + c4[0] + c4[1] + c4[2]};
    __builtin_amdgcn_wave_barrier();
    u32 at[3];
#pragma unroll
    for (int i = 0; i < 3; i++) at[i] = mine[ebk[i]];
#pragma unroll
    for (int i = 0; i < 3; i++) {
      const u32 shd = (ed[i] & 1u) * 16, shs = (es[i] & 1u) * 16;
      const u32 dxy = __builtin_amdgcn_alignbit(pd[i][1], pd[i][0], shd), sxy = __builtin_amdgcn_alignbit(ps[i][1], ps[i][0], shs);
      const u32 xy = pk_bf16(bf2f(dxy) - bf2f(sxy), bfhi(dxy) - bfhi(sxy));
      const u32 zz = pk_bf16(bf2f(pd[i][1] >> shd) - bf2f(ps[i][1] >> shs), 0.f);
      const float dx = bf2f(xy & 0xffffu), dy = bfhi(xy), dz = bf2f(zz);
      const u32 q0 = pk_bf16(dx * dx, dy * dy), q1 = pk_bf16(dz * dz, 0.f);
      const float s2 = bf2f(q0 & 0xffffu) + bfhi(q0) + bf2f(q1);
      const u32x4 rc = {es[i] << 8, (ed[i] - ebk[i] * NPB) | (ee[i] << 16), xy, pk_bf16(dz, s2)};
      *(u32x4*)(lds + (tid - 256 + 256 * i < ne ? L_RS + (at[i] + rank[i]) * 16 : L_DUMP)) = rc;
    }
  }
  STAMP(2);
  __syncthreads();
  STAMP(13);
  u32 gword;
  {
    const u32x4 r0 = *(const u32x4*)(lds + L_RS + tid * 16);
    const u32x4 r1 = *(const u32x4*)(lds + L_RS + (NT + (tid & 127)) * 16);
    gword = (*(const u32*)(lds + L_LS + (tid & 255) * 4) << 10) | *(const u32*)(lds + L_BC + (tid & 255) * 4);
    __builtin_amdgcn_raw_buffer_store_b128(r0, r_rec, (b * CH + tid) * 16, 0, SC1);
    __builtin_amdgcn_raw_buffer_store_b128(r1, r_rec, tid < 128 ? (b * CH + NT + tid) * 16 : OOB, 0, SC1);
    SB();
    const u32x4 s_we = srd(w_e, 258 * 256);
#pragma unroll
    for (int i = 0; i < 4; i++) dma16(s_we, wvo, (wv + 8 * i) * 1024, lb + L_W0 + (wv + 8 * i) * 1024);
    SB();
    const u32x4 z = {0, 0, 0, 0};
#pragma unroll
    for (int i = 0; i < (L_SRC + 8191) / 8192; i++) { const int o = i * 8192 + tid * 16; if (o < L_SRC) *(u32x4*)(lds + o) = z; }
    *(u32x2*)(lds + L_SRC + tid * 8) = u32x2{OOB, OOB};
    if (tid < 16) *(u32*)(lds + L_SRC + 4096 + tid * 4) = OOB;
    const float wd = bf2f(wdv) * LOG2E;
    const u32 wdh = pk_bf16(wd, 0.f);
    const u32 wdp = wdh | (pk_bf16(wd - bf2f(wdh), 0.f) << 16), wap = pk_bf16(bf2f(wav) * LOG2E, 0.f);
#pragma unroll
    for (int i = 0; i < 2; i++) {
      char* kb = lds + L_KB + (i * NT + tid) * 16;
      *(u32*)(kb + 12) = wdp;
      *(u16*)(kb + 10) = (u16)wap;
    }
    if (tid < D) *(u16*)(lds + L_WX + tid * 2) = (u16)wxv;
  }
  STAMP(14);
  asm volatile("s_waitcnt vmcnt(4)" ::: "memory");
  STAMP(15);
  __syncthreads();
  const u64 tag = (ep + 1) << 20;
  if (role1) __hip_atomic_store((gu64*)(slot + WS_GR) + (size_t)b * MAXG + (tid & 255), tag | gword, RLX_AGENT);
  STAMP(3);
  const int cs = wv * 32 + (lane & 31), csc = cs < G ? cs : G - 1;
  gu64* const gp = (gu64*)(slot + WS_GR) + (size_t)csc * MAXG + bk;
  if (role1) asm volatile("s_waitcnt vmcnt(1)" ::: "memory");
  else asm volatile("s_waitcnt vmcnt(0)" ::: "memory");
  __syncthreads();
  u64 gv = __hip_atomic_load(gp, RLX_AGENT);
  {
    f32x4 acc[3] = {f32x4{0, 0, 0, 0}, f32x4{0, 0, 0, 0}, f32x4{0, 0, 0, 0}};
#pragma unroll
    for (int s = 0; s < 4; s++) {
      const u32x4 fw = tr_frag(lds + L_W0, s, lane, 2 * wv + ((lane & 3) >> 1), lane & 1);
#pragma unroll
      for (int t = 0; t < 3; t++) acc[t] = mfma16(h_frag(lds, t, s, lane), fw, acc[t]);
    }
    char* kb = lds + L_KB + (16 * wv + c16) * 16;
#pragma unroll
    for (int t = 0; t < 2; t++) {
      const f32x4 a = acc[t] * LOG2E;
      *(u32x2*)(kb + (4 * t + g4) * 2048) = u32x2{pk_bf16(a[0], a[1]), pk_bf16(a[2], a[3])};
    }
    if (g4 < 2) {
#pragma unroll
      for (int i = 0; i < 4; i++) *(u16*)(kb + (4 * g4 + i) * 2048 + 8) = (u16)pk_bf16(acc[2][i] * LOG2E, 0.f);
    }
  }
  STAMP(4);
  {
    for (u32 spins = 0; !__all((gv >> 20) == (tag >> 20)) && spins < (1u << 20); spins++) {
      __builtin_amdgcn_s_sleep(1);
      gv = __hip_atomic_load(gp, RLX_AGENT);
    }
    STAMP(5);
    const bool on = lane < 32 && cs < G;
    const u32 cnt = on ? ((u32)gv & 1023u) : 0u, st = ((u32)gv >> 10) & 1023u;
    if (on) *(u32*)(lds + L_BC + cs * 4) = (u32)gv & 0xfffffu;
    const u32 inc = scan64(cnt);
    const u32 total = __builtin_amdgcn_readlane(inc, 63);
    u32* list = (u32*)(lds + L_LS + wv * 512);
    const u32 base = (cs * CH + st) * 16;
    const u32 p0 = inc - cnt;
#pragma unroll 1
    for (u32 k = 0; k < cnt && p0 + k < 128; k++) list[p0 + k] = base + k * 16;
    __builtin_amdgcn_wave_barrier();
    {
      const u32 a0 = list[lane], a1 = list[64 + lane];
      const bool v0 = lane < total, v1 = 64 + lane < total;
      u32x4 r0 = __builtin_amdgcn_raw_buffer_load_b128(r_rec, v0 ? a0 : OOB, 0, SC1);
      u32x4 r1 = __builtin_amdgcn_raw_buffer_load_b128(r_rec, v1 ? a1 : OOB, 0, SC1);
      SB();
      PIN(r0);
      if (v0) insert_rec(lds, r0);
      PIN(r1);
      if (v1) insert_rec(lds, r1);
    }
    if (total > 128) {
#pragma unroll 1
      for (u32 done = 128; done < total; done += 64) {
        if (__hip_atomic_load((u32*)(lds + L_CNT + 32), __ATOMIC_RELAXED, __HIP_MEMORY_SCOPE_WORKGROUP)) break;
        __builtin_amdgcn_wave_barrier();
#pragma unroll 1
        for (u32 k = p0 < done ? done - p0 : 0u; k < cnt && p0 + k < done + 64; k++) list[p0 + k - done] = base + k * 16;
        __builtin_amdgcn_wave_barrier();
        const bool v0 = done + lane < total;
        u32x4 r0 = __builtin_amdgcn_raw_buffer_load_b128(r_rec, v0 ? list[lane] : OOB, 0, SC1);
        PIN(r0);
        if (v0) insert_rec(lds, r0);
      }
    }
  }
  STAMP(6);
  __syncthreads();
  STAMP(8);
  if (b == 0 && tid == 0) __hip_atomic_store((gu64*)(ws + WS_EP), ep + 1, RLX_AGENT);
  const u32* wcnt = (const u32*)(lds + L_CNT);
  const int ovf = __builtin_amdgcn_readfirstlane(wcnt[8]);
  const int r = lane & 31, hh = lane >> 5;
  auto load_wh = [&]() {
    const u32x4 s_wh = srd(w_h, 65536);
#pragma unroll
    for (int i = 0; i < 4; i++) dma16(s_wh, wvo, (wv + 8 * i) * 1024, lb + L_W1 + (wv + 8 * i) * 1024);
#pragma unroll
    for (int i = 0; i < 4; i++) dma16(s_wh, wvo, 32768 + (wv + 8 * i) * 1024, lb + L_W0 + (wv + 8 * i) * 1024);
  };
  if (!ovf) {
    int win;
    {
      const u32x4 ca = *(const u32x4*)(wcnt), cb = *(const u32x4*)(wcnt + 4);
      const u32 mine = wcnt[lane & 7] * 8 + (lane & 7);
      u32 rk = 0;
#pragma unroll
      for (int j = 0; j < 4; j++) { rk += (ca[j] * 8 + j) > mine; rk += (cb[j] * 8 + 4 + j) > mine; }
      const u64 m = __ballot(rk == (u32)(wv < 4 ? wv : 11 - wv));
      win = __builtin_ctzll(m);
    }
    const int cntw = __builtin_amdgcn_readfirstlane(wcnt[win]);
    const int nsteps = (cntw + 15) >> 4;
    const u32 lo = r * 4;
    u32 srca = L_SRC + win * CAPW * 4 + hh * 32;
    u32 GA[8], GB[8], off[8];
    auto idx = [&]() {
      const u32x4 i0 = *(const u32x4*)(lds + srca), i1 = *(const u32x4*)(lds + srca + 16);
      srca += 64;
#pragma unroll
      for (int j = 0; j < 4; j++) { off[j] = i0[j] + lo; off[4 + j] = i1[j] + lo; }
    };
    auto ldA = [&]() {
#pragma unroll
      for (int j = 0; j < 8; j++) GA[j] = __builtin_amdgcn_raw_buffer_load_b32(r_bt, off[j], 0, SC1);
    };
    auto ldB = [&]() {
#pragma unroll
      for (int j = 0; j < 8; j++) GB[j] = __builtin_amdgcn_raw_buffer_load_b32(r_bt, off[j] + 128, 0, SC1);
    };
    idx(); ldA(); SB(); ldB(); SB();
    STAMP(7);
    f32x16 aA0, aA1, aB0, aB1, zero;
#pragma unroll
    for (int i = 0; i < 16; i++) { aA0[i] = 0.f; aA1[i] = 0.f; aB0[i] = 0.f; aB1[i] = 0.f; zero[i] = 0.f; }
    const int rh = (r >> 2) & 1, rp = r >> 4, rj = (r & 3) | (((r >> 3) & 1) << 2);
    u32x4 sel0, sel1;
    {
      const u32 one = rh == hh ? 0x3f80u << (rp * 16) : 0u;
#pragma unroll
      for (int q = 0; q < 4; q++) { sel0[q] = (rj == q) ? one : 0u; sel1[q] = (rj == 4 + q) ? one : 0u; }
    }
    const u32x4 kbA = *(const u32x4*)(lds + L_KB + win * 2048 + (2 * r + hh) * 16);
    const u32x4 kbB = *(const u32x4*)(lds + L_KB + win * 2048 + (64 + 2 * r + hh) * 16);
    const bool cfon = rp == hh;
    u32 cfa = cfon ? (L_CF + (win * CAPW + 8 * rh + rj) * 16) : L_ZERO;
    const u32 cfs = cfon ? 256u : 0u;
    u32 sma = L_SM + win * 8 * 640 + hh * 320 + r * 16;

    u32x4 cf = *(const u32x4*)(lds + cfa), sf = {0, 0, 0, 0}, mB0 = {0, 0, 0, 0}, mB1 = {0, 0, 0, 0}, mA0, mA1;
    f32x16 yA = mfma32(cf, kbA, zero), yB;
    idx();
    yA = mfma32(sel0, u32x4{GA[0], GA[1], GA[2], GA[3]}, yA);
    yA = mfma32(sel1, u32x4{GA[4], GA[5], GA[6], GA[7]}, yA);
    PIN(yA);
    SB();
    ldA();
    SB();
#define MF(d, a, bb, c) do { SB(); d = mfma32(a, bb, c); PIN(d); SB(); } while (0)
#define SA(y, i, j) do { p_ = f32x2{__builtin_amdgcn_exp2f(-y[i]), __builtin_amdgcn_exp2f(-y[(i) + 1])}; \
                         q_ = f32x2{__builtin_amdgcn_exp2f(-y[j]), __builtin_amdgcn_exp2f(-y[(j) + 1])}; SB(); p_ += 1.0f; q_ += 1.0f; \
                         asm volatile("" : "+v"(p_), "+v"(q_)); SB(); } while (0)
#define SBB(y, i, j, o0, o1) do { p_ = f32x2{__builtin_amdgcn_rcpf(p_[0]), __builtin_amdgcn_rcpf(p_[1])}; \
                         q_ = f32x2{__builtin_amdgcn_rcpf(q_[0]), __builtin_amdgcn_rcpf(q_[1])}; SB(); \
                         p_ *= f32x2{y[i], y[(i) + 1]}; q_ *= f32x2{y[j], y[(j) + 1]}; SB(); \
                         u32 t0_ = pk_bf16(p_[0], p_[1]), t1_ = pk_bf16(q_[0], q_[1]); \
                         asm volatile("" : "+v"(t0_), "+v"(t1_)); o0 = t0_; o1 = t1_; SB(); } while (0)
#define S4(y, i, j, o0, o1) do { SA(y, i, j); SBB(y, i, j, o0, o1); } while (0)
#define LD2(Gx, j, add) do { Gx[j] = __builtin_amdgcn_raw_buffer_load_b32(r_bt, off[j] + (add), 0, SC1); \
                             Gx[(j) + 1] = __builtin_amdgcn_raw_buffer_load_b32(r_bt, off[(j) + 1] + (add), 0, SC1); SB(); } while (0)
    f32x2 p_, q_;
    for (int s = 1; s < nsteps; s++) {
      if (s == 2) load_wh();
      MF(yB, cf, kbB, zero);
      S4(yA, 0, 2, mA0[0], mA0[1]);
      MF(yB, sel0, (u32x4{GB[0], GB[1], GB[2], GB[3]}), yB);
      cfa += cfs;
      cf = *(const u32x4*)(lds + cfa);
      SA(yA, 4, 6);
      LD2(GB, 0, 128);
      SBB(yA, 4, 6, mA0[2], mA0[3]);
      LD2(GB, 2, 128);
      MF(yB, sel1, (u32x4{GB[4], GB[5], GB[6], GB[7]}), yB);
      SA(yA, 8, 10);
      LD2(GB, 4, 128);
      MF(aB0, mB0, sf, aB0);
      SBB(yA, 8, 10, mA1[0], mA1[1]);
      LD2(GB, 6, 128);
      const u32x4 i0 = *(const u32x4*)(lds + srca), i1 = *(const u32x4*)(lds + srca + 16);
      srca += 64;
      SA(yA, 12, 14);
      MF(aB1, mB1, sf, aB1);
      sf = *(const u32x4*)(lds + sma);
      sma += 640;
      SBB(yA, 12, 14, mA1[2], mA1[3]);
      MF(yA, cf, kbA, zero);
      S4(yB, 0, 2, mB0[0], mB0[1]);
      MF(yA, sel0, (u32x4{GA[0], GA[1], GA[2], GA[3]}), yA);
#pragma unroll
      for (int j = 0; j < 4; j++) { off[j] = i0[j] + lo; off[4 + j] = i1[j] + lo; }
      SA(yB, 4, 6);
      LD2(GA, 0, 0);
      SBB(yB, 4, 6, mB0[2], mB0[3]);
      LD2(GA, 2, 0);
      MF(yA, sel1, (u32x4{GA[4], GA[5], GA[6], GA[7]}), yA);
      SA(yB, 8, 10);
      LD2(GA, 4, 0);
      MF(aA0, mA0, sf, aA0);
      SBB(yB, 8, 10, mB1[0], mB1[1]);
      LD2(GA, 6, 0);
      SA(yB, 12, 14);
      MF(aA1, mA1, sf, aA1);
      SBB(yB, 12, 14, mB1[2], mB1[3]);
    }
    {
      const int rem = cntw - 16 * (nsteps - 1);
      if (nsteps <= 2) load_wh();
      MF(yB, cf, kbB, zero);
      mA0 = u32x4{0, 0, 0, 0}; mA1 = u32x4{0, 0, 0, 0};
      S4(yA, 0, 8, mA0[0], mA1[0]);
      MF(yB, sel0, (u32x4{GB[0], GB[1], GB[2], GB[3]}), yB);
      if (rem > 4) S4(yA, 2, 10, mA0[1], mA1[1]);
      MF(yB, sel1, (u32x4{GB[4], GB[5], GB[6], GB[7]}), yB);
      if (rem > 8) S4(yA, 4, 12, mA0[2], mA1[2]);
      MF(aB0, mB0, sf, aB0);
      if (rem > 12) S4(yA, 6, 14, mA0[3], mA1[3]);
      MF(aB1, mB1, sf, aB1);
      sf = *(const u32x4*)(lds + sma);
      mB0 = u32x4{0, 0, 0, 0}; mB1 = u32x4{0, 0, 0, 0};
      S4(yB, 0, 8, mB0[0], mB1[0]);
      MF(aA0, mA0, sf, aA0);
      if (rem > 4) S4(yB, 2, 10, mB0[1], mB1[1]);
      MF(aA1, mA1, sf, aA1);
      if (rem > 8) S4(yB, 4, 12, mB0[2], mB1[2]);
      if (rem > 12) S4(yB, 6, 14, mB0[3], mB1[3]);
      MF(aB0, mB0, sf, aB0);
      MF(aB1, mB1, sf, aB1);
    }
    STAMP(9);
    f32x16 z = zero;
    const int mia = r < 5 ? mi_row(r * 8 + win) + 16 * hh : L_DUMP;
    const int mis = r < 5 ? 32 : 0;
#pragma unroll
    for (int fh = 0; fh < 2; fh++) {
      const f32x16& a0 = fh ? aB0 : aA0;
      const f32x16& a1 = fh ? aB1 : aA1;
#pragma unroll
      for (int q4 = 0; q4 < 4; q4++) {
        u32x4 v;
#pragma unroll
        for (int q = 0; q < 4; q++) v[q] = pk_bf16(a0[4 * q4 + q], a1[4 * q4 + q]);
        z = mfma32(*(const u32x4*)(lds + L_WX + (64 * fh + 16 * q4 + 8 * hh) * 2), v, z);
        *(u32x4*)(lds + mia + mis * (4 * fh + q4)) = v;
      }
    }
    {
      const int kind = (lane * 13) >> 6, nl = (lane - kind * 5) * 8 + win, i = nl * 3 + kind - 1;
      const bool pon = lane >= 5 && lane < 20;
      const float p0 = bf2f(*(const u16*)(lds + L_PD + (pon ? i : 0) * 2));
      __builtin_amdgcn_raw_buffer_store_b16((u16)pk_bf16(p0 + LN2 * z[0], 0.f), rsrc(out + (size_t)N * D, N * 6), pon ? (nb0 * 3 + i) * 2 : OOB, 0, 0);
    }
  } else {
    float* macc = (float*)(lds + L_SM);
    float* pacc = (float*)(lds + L_SM + NPB * D * 4);
    load_wh();
    __syncthreads();
    for (int i = tid; i < NPB * D + NPB * 4; i += NT) macc[i] = 0.f;
    __syncthreads();
    const u32 wxp = *(const u32*)(lds + L_WX + lane * 4);
    const float wx0 = bf2f(wxp & 0xffffu), wx1 = bfhi(wxp);
    const u32x4 kc0 = *(const u32x4*)(lds + L_KB + (2 * lane) * 16), kc1 = *(const u32x4*)(lds + L_KB + (2 * lane + 1) * 16);
    const float wd0 = bf2f(kc0[3] & 0xffffu) + bfhi(kc0[3]), wd1 = bf2f(kc1[3] & 0xffffu) + bfhi(kc1[3]);
    const float wa0 = bfhi(kc0[2]), wa1 = bfhi(kc1[2]);
    u32 rot = 0;
    for (int c = 0; c < G; c++) {
      const u32 oo = __builtin_amdgcn_readfirstlane(*(const u32*)(lds + L_BC + c * 4));
      const u32 s0 = oo >> 10, n = oo & 1023u;
      for (u32 k = (wv - rot) & 7u; k < n; k += 8) {
        const u32x4 rc = __builtin_amdgcn_raw_buffer_load_b128(r_rec, (c * CH + s0 + k) * 16, 0, SC1);
        const u32 nl = rc[1] & 0x3fu, sl = nl >> 3, wn = nl & 7u;
        const u32 bv = __builtin_amdgcn_raw_buffer_load_b32(r_bt, rc[0] + lane * 4, 0, SC1);
        const float d2 = bfhi(rc[3]), eav = bfhi(rc[1]);
        const float a0 = bf2f(*(const u16*)(lds + L_KB + wn * 2048 + (2 * lane) * 16 + sl * 2));
        const float a1 = bf2f(*(const u16*)(lds + L_KB + wn * 2048 + (2 * lane + 1) * 16 + sl * 2));
        const float y0 = a0 + bf2f(bv & 0xffffu) + d2 * wd0 + eav * wa0, y1 = a1 + bfhi(bv) + d2 * wd1 + eav * wa1;
        const u32 mp = silu2(y0, y1);
        const float m0 = bf2f(mp & 0xffffu), m1 = bfhi(mp);
        atomicAdd(&macc[nl * D + 2 * lane], m0);
        atomicAdd(&macc[nl * D + 2 * lane + 1], m1);
        float csv = m0 * wx0 + m1 * wx1;
#pragma unroll
        for (int o = 32; o > 0; o >>= 1) csv += __shfl_xor(csv, o, 64);
        if (lane < 3) atomicAdd(&pacc[nl * 4 + lane], csv * bf2f(lane == 0 ? (rc[2] & 0xffffu) : lane == 1 ? (rc[2] >> 16) : (rc[3] & 0xffffu)));
      }
      rot += n;
    }
    __syncthreads();
    for (int i = tid; i < NPB * D / 2; i += NT) {
      const int n = i >> 6, f = (i & 63) * 2;
      *(u32*)(lds + mi_row(n) + f * 2) = pk_bf16(macc[n * D + f], macc[n * D + f + 1]);
    }
    if (tid < NPB * 3) {
      const int n = tid / 3, c = tid - n * 3;
      if (nb0 + n < N) out[(size_t)N * D + (size_t)nb0 * 3 + tid] = (u16)pk_bf16(bf2f(*(const u16*)(lds + L_PD + tid * 2)) + LN2 * pacc[n * 4 + c], 0.f);
    }
  }
  asm volatile("s_waitcnt vmcnt(0)" ::: "memory");
  __syncthreads();
  STAMP(10);
  {
    f32x4 cp[3] = {f32x4{0, 0, 0, 0}, f32x4{0, 0, 0, 0}, f32x4{0, 0, 0, 0}}, cm[3] = {f32x4{0, 0, 0, 0}, f32x4{0, 0, 0, 0}, f32x4{0, 0, 0, 0}};
    u32x2 hr[3];
#pragma unroll
    for (int s = 0; s < 4; s++) {
      const u32x4 f2 = tr_frag(lds + L_W0, s, lane, 2 * wv + ((lane & 3) >> 1), lane & 1);
      const u32x4 f1 = tr_frag(lds + L_W1, s, lane, 2 * wv + ((lane & 3) >> 1), lane & 1);
#pragma unroll
      for (int t = 0; t < 3; t++) {
        cm[t] = mfma16(f2, *(const u32x4*)(lds + mi_row(tile_node(t, c16)) + s * 64 + g4 * 16), cm[t]);
        cp[t] = mfma16(f1, h_frag(lds, t, s, lane), cp[t]);
      }
    }
#pragma unroll
    for (int t = 0; t < 3; t++) {
      const int n = tile_node(t, c16);
      hr[t] = *(const u32x2*)(lds + L_HS + n * 256 + 16 * ((2 * wv + (g4 >> 1)) ^ hswz(n)) + 8 * (g4 & 1));
    }
    const __amdgpu_buffer_rsrc_t r_out = rsrc(out, N * 256);
#pragma unroll
    for (int t = 0; t < 3; t++) {
      const f32x4 y = cp[t] * LOG2E + cm[t];
      float o[4];
#pragma unroll
      for (int i = 0; i < 4; i++)
        o[i] = ((i & 1) ? bfhi(hr[t][i >> 1]) : bf2f(hr[t][i >> 1] & 0xffffu)) + LN2 * y[i] * __builtin_amdgcn_rcpf(1.f + __builtin_amdgcn_exp2f(-y[i]));
      __builtin_amdgcn_raw_buffer_store_b64(u32x2{pk_bf16(o[0], o[1]), pk_bf16(o[2], o[3])}, r_out,
                                            (t < 2 || c16 < 8) ? (nb0 + tile_node(t, c16)) * 256 + wv * 32 + g4 * 8 : OOB, 0, 0);
    }
  }
  STAMP(11);
}

extern "C" void kernel_launch(void* const* d_in, const int* in_sizes, int n_in, void* d_out, int out_size, void* d_ws,
                              size_t ws_size, hipStream_t stream) {
  const int N = in_sizes[0] / D, E = in_sizes[3];
  const int NB = (N + NPB - 1) / NPB, C = (E + CH - 1) / CH;
  const int G = NB > C ? NB : C;
  k_egnn<<<G, NT, 0, stream>>>((const u16*)d_in[0], (const u16*)d_in[1], (const u16*)d_in[2], (const int*)d_in[3],
                               (const int*)d_in[4], (const u16*)d_in[5], (const u16*)d_in[6], (const u16*)d_in[7],
                               (char*)d_ws, (u16*)d_out, N, E, G);
}
```

```cpp
#include <hip/hip_runtime.h>
#include <hip/hip_bf16.h>

typedef unsigned int u32;
typedef unsigned short u16;
typedef unsigned long long u64;
typedef u32 u32x2 __attribute__((ext_vector_type(2)));
typedef u32 u32x4 __attribute__((ext_vector_type(4)));
typedef short s16x4 __attribute__((ext_vector_type(4)));
typedef short bf16x8 __attribute__((ext_vector_type(8)));
typedef float f32x2 __attribute__((ext_vector_type(2)));
typedef float f32x4 __attribute__((ext_vector_type(4)));
typedef float f32x16 __attribute__((ext_vector_type(16)));
typedef __bf16 bf16x2_t __attribute__((ext_vector_type(2)));
typedef __attribute__((address_space(1))) u64 gu64;
typedef __attribute__((address_space(3))) s16x4 lds_s16x4;
typedef __attribute__((address_space(3))) char lds_char;
#define DI __device__ __forceinline__
#define RLX_AGENT __ATOMIC_RELAXED, __HIP_MEMORY_SCOPE_AGENT
#define SC1 16
#define SB() __builtin_amdgcn_sched_barrier(0)
#define PIN(x) asm volatile("" : "+v"(x))

constexpr int D = 128;
constexpr int NT = 512;
constexpr int NPB = 40;
constexpr int CH = 640;
constexpr int MAXG = 256;
constexpr int CAPW = 128;
constexpr int MAXPOS = 61440;
#define ZSPLIT 3
constexpr u32 CODE_BYTES = 19456;
constexpr float LOG2E = 1.4426950408889634f;
constexpr float LN2 = 0.6931471805599453f;
constexpr u32 OOB = 0xfffff000u;

constexpr size_t WS_EP = 0;
constexpr size_t WS_SLOT0 = (size_t)2 << 20, WS_SLOT = (size_t)6 << 20;
constexpr size_t WS_GR = 0;
constexpr size_t WS_BT = WS_GR + (size_t)MAXG * MAXG * 8;
constexpr size_t WS_REC = WS_BT + (size_t)MAXG * NPB * 256;
constexpr size_t WS_FL = WS_REC + (size_t)MAXG * CH * 16;
static_assert(WS_FL + MAXG * 8 <= WS_SLOT, "slot");

#define STAMP(k) do { } while (0)

DI float bf2f(u32 b) { return __builtin_bit_cast(float, b << 16); }
DI float bfhi(u32 b) { return __builtin_bit_cast(float, b & 0xffff0000u); }
DI u32 pk_bf16(float lo, float hi) {
  f32x2 v = {lo, hi};
  bf16x2_t r = __builtin_convertvector(v, bf16x2_t);
  return __builtin_bit_cast(u32, r);
}
DI f32x4 mfma16(u32x4 a, u32x4 b, f32x4 c) {
  return __builtin_amdgcn_mfma_f32_16x16x32_bf16(__builtin_bit_cast(bf16x8, a), __builtin_bit_cast(bf16x8, b), c, 0, 0, 0);
}
DI f32x16 mfma32(u32x4 a, u32x4 b, f32x16 c) {
  return __builtin_amdgcn_mfma_f32_32x32x16_bf16(__builtin_bit_cast(bf16x8, a), __builtin_bit_cast(bf16x8, b), c, 0, 0, 0);
}
DI __amdgpu_buffer_rsrc_t rsrc(const void* p, u32 bytes) { return __builtin_amdgcn_make_buffer_rsrc((void*)p, 0, bytes, 0x00020000); }
DI u32x4 srd(const void* p, u32 bytes) { const u64 a = (u64)p; return u32x4{(u32)a, (u32)(a >> 32) & 0xffffu, bytes, 0x00020000u}; }
DI void dma16(u32x4 sd, u32 voff, u32 soff, u32 ldsa) {
  u32 keep;
  asm volatile("s_mov_b32 %0, m0\n\ts_mov_b32 m0, %1\n\ts_nop 0\n\tbuffer_load_dwordx4 %2, %3, %4 offen lds\n\ts_mov_b32 m0, %0"
               : "=&s"(keep) : "s"(__builtin_amdgcn_readfirstlane(ldsa)), "v"(voff), "s"(sd), "s"(soff) : "memory");
}
DI u32 scan64(u32 x) {
  x += __builtin_amdgcn_update_dpp(0u, x, 0x111, 0xf, 0xf, false);
  x += __builtin_amdgcn_update_dpp(0u, x, 0x112, 0xf, 0xf, false);
  x += __builtin_amdgcn_update_dpp(0u, x, 0x114, 0xf, 0xf, false);
  x += __builtin_amdgcn_update_dpp(0u, x, 0x118, 0xf, 0xf, false);
  x += __builtin_amdgcn_update_dpp(0u, x, 0x142, 0xa, 0xf, false);
  x += __builtin_amdgcn_update_dpp(0u, x, 0x143, 0xc, 0xf, false);
  return x;
}

constexpr int L_SM = 0;
constexpr int L_CF = L_SM + 8 * 8 * 640;
constexpr int L_ZERO = L_CF + 8 * CAPW * 16;
constexpr int L_CNT = L_ZERO + 16;
constexpr int L_SRC = L_CNT + 48;
constexpr int L_KB = L_SRC + 8 * CAPW * 4 + 64;
constexpr int L_W1 = L_KB + 8 * D * 16;
constexpr int L_W0 = L_W1 + 32768;
constexpr int L_HS = L_W0 + 32768;
constexpr int L_WX = L_HS + NPB * 256;
constexpr int L_PD = L_WX + 256;
constexpr int L_BC = L_PD + 256;
constexpr int L_SY = L_BC + 1024;
constexpr int L_LS = L_SY + 16;
constexpr int L_END = L_LS + 8 * 768;
constexpr int L_RS = L_W0 - CH * 16;
constexpr int L_POS = L_RS - MAXPOS;
static_assert(L_END <= 160 * 1024, "lds");
static_assert(L_POS >= 38912 && L_POS <= L_CF && (L_POS % 16) == 0, "pos table");
static_assert((L_CF % 16) == 0 && (L_SRC % 16) == 0 && (L_KB % 16) == 0 && (L_W0 % 16) == 0 && (L_HS % 16) == 0 && (L_WX % 16) == 0 && (L_BC % 16) == 0 && (L_LS % 16) == 0, "align");

DI int tile_node(int t, int c) { return t < 2 ? ((c & 3) * 8 + 4 * t + (c >> 2)) : (32 + (c & 7)); }
DI int mi_row(int nl) { return L_KB + (nl & 7) * 2048 + (nl & 3) * 64 + (nl >> 3) * 272; }
DI int wswz(int k) { return ((k & 3) << 2) | ((k >> 2) & 3); }
DI int hswz(int n) { return ((n & 3) << 2) | ((n >> 3) & 3); }
DI u32x4 tr_frag(const char* img, int s, int lane, int ch, int hf) {
  const int k0 = 8 * (lane >> 4) + ((lane & 15) >> 2), k1 = k0 + 4;
  const s16x4 a = __builtin_amdgcn_ds_read_tr16_b64_v4i16((lds_s16x4*)(img + (32 * s + k0) * 256 + 16 * (ch ^ wswz(k0)) + 8 * hf));
  const s16x4 b = __builtin_amdgcn_ds_read_tr16_b64_v4i16((lds_s16x4*)(img + (32 * s + k1) * 256 + 16 * (ch ^ wswz(k1)) + 8 * hf));
  const u32x2 x = __builtin_bit_cast(u32x2, a), y = __builtin_bit_cast(u32x2, b);
  return u32x4{x[0], x[1], y[0], y[1]};
}
DI u32x4 h_frag(const char* lds, int t, int s, int lane) {
  const int n = tile_node(t, lane & 15);
  return *(const u32x4*)(lds + L_HS + n * 256 + 16 * ((4 * s + (lane >> 4)) ^ hswz(n)));
}
DI u32 dist2(u32 w2, u32 w3) {
  const float dx = bfhi(w2), dy = bf2f(w3), dz = bfhi(w3);
  const u32 q0 = pk_bf16(dx * dx, dy * dy), q1 = pk_bf16(dz * dz, 0.f);
  const float s2 = bf2f(q0) + bfhi(q0) + bf2f(q1);
  return pk_bf16(s2, s2);
}
DI u32 take_rank(char* lds, u32x4 rc) { return atomicAdd((u32*)(lds + L_CNT) + ((rc[0] >> 14) & 7u), 1u); }
DI void put_rec(char* lds, u32x4 rc, u32 rank) {
  const u32 win = (rc[0] >> 14) & 7u, slot = (rc[0] >> 17) & 7u;
  const u32 one = 0x3f80u << ((slot & 1u) * 16);
  const u32x4 cf = {slot < 2 ? one : 0u, (slot >> 1) == 1 ? one : 0u, (slot == 4 ? 0x3f80u : 0u) | (rc[2] << 16), dist2(rc[2], rc[3])};
  if (rank < CAPW) {
    const u32 k = ((rank & 1u) << 3) + ((rank & 15u) >> 1), sp = win * CAPW + (rank & ~15u) + k;
    *(u32*)(lds + L_SRC + sp * 4) = (rc[0] & 0x3fffu) << 8;
    *(u32x4*)(lds + L_CF + sp * 16) = cf;
    char* sm = lds + L_SM + (sp >> 4) * 640 + (rank & 1u) * 320 + slot * 16 + (rank & 14u);
    *(u16*)(sm) = (u16)0x3f80u;
    *(u16*)(sm + 80) = (u16)(rc[2] >> 16);
    *(u16*)(sm + 160) = (u16)rc[3];
    *(u16*)(sm + 240) = (u16)(rc[3] >> 16);
  } else *(u32*)(lds + L_CNT + 32) = 1;
}
DI u32x4 settle(u32x4 rc, bool on, __amdgpu_buffer_rsrc_t r, u32 off, u32 tag0, u32 tag1) {
  for (u32 spins = 0; __any(on && ((rc[0] >> 20) != tag0 || rc[1] != tag1)) && spins < (1u << 20); spins++)
    rc = __builtin_amdgcn_raw_buffer_load_b128(r, on ? off : OOB, 0, SC1);
  return rc;
}
DI void arrive(char* lds, int i, int lane) {
  asm volatile("" ::: "memory");
  if (lane == 0) __hip_atomic_fetch_add((u32*)(lds + L_SY) + i, 1u, __ATOMIC_RELAXED, __HIP_MEMORY_SCOPE_WORKGROUP);
}
DI void await(char* lds, int i, u32 n) {
  while ((u32)__builtin_amdgcn_readfirstlane(__hip_atomic_load((u32*)(lds + L_SY) + i, __ATOMIC_RELAXED, __HIP_MEMORY_SCOPE_WORKGROUP)) < n)
    __builtin_amdgcn_s_sleep(0);
  asm volatile("" ::: "memory");
}
DI u32 silu2(float a, float b) {
  f32x2 yy = {a, b};
  f32x2 e = {__builtin_amdgcn_exp2f(-a), __builtin_amdgcn_exp2f(-b)};
  e += 1.0f;
  f32x2 rc = {__builtin_amdgcn_rcpf(e[0]), __builtin_amdgcn_rcpf(e[1])};
  yy *= rc;
  return pk_bf16(yy[0], yy[1]);
}

__global__ __launch_bounds__(NT, 2) void k_egnn(const u16* __restrict__ h, const u16* __restrict__ pos,
                                                const u16* __restrict__ ea, const int* __restrict__ src,
                                                const int* __restrict__ dst, const u16* __restrict__ w_e,
                                                const u16* __restrict__ w_x, const u16* __restrict__ w_h,
                                                char* __restrict__ ws, u16* __restrict__ out, int N, int E, int G) {
  __shared__ __attribute__((aligned(16))) char lds[L_END];
  const int tid = threadIdx.x, lane = tid & 63, b = blockIdx.x;
  const int wv = __builtin_amdgcn_readfirstlane(tid >> 6);
  const int u = wv & 3, c16 = lane & 15, g4 = lane >> 4;
  const bool role1 = wv >= 4;
  const u32 lb = (u32)(size_t)(lds_char*)lds;
  STAMP(0);
  const int ne = min(CH, E - b * CH);
  u32 ed[3], es[3], ee[3], pfc, pf1, pf2, wdv, wav, wxv, pf3 = 0, pf4 = 0;
#define NT2 "0"
#define W13 "13"
  u32x2 epoch;
  {
    const u32x4 s_d = srd(dst + b * CH, ne * 4), s_s = srd(src + b * CH, ne * 4), s_e = srd(ea + b * CH, ne * 2);
    const u32 k4 = role1 ? (u32)(tid - 256) * 4 : 0x80000000u;
    asm volatile("buffer_load_dword %0, %9, %11, 0 offen\n\tbuffer_load_dword %1, %9, %11, 0 offen offset:1024\n\t"
                 "buffer_load_dword %2, %9, %11, 0 offen offset:2048\n\tbuffer_load_dword %3, %9, %12, 0 offen\n\t"
                 "buffer_load_dword %4, %9, %12, 0 offen offset:1024\n\tbuffer_load_dword %5, %9, %12, 0 offen offset:2048\n\t"
                 "buffer_load_ushort %6, %10, %13, 0 offen\n\tbuffer_load_ushort %7, %10, %13, 0 offen offset:512\n\t"
                 "buffer_load_ushort %8, %10, %13, 0 offen offset:1024"
                 : "=&v"(ed[0]), "=&v"(ed[1]), "=&v"(ed[2]), "=&v"(es[0]), "=&v"(es[1]), "=&v"(es[2]), "=&v"(ee[0]), "=&v"(ee[1]), "=&v"(ee[2])
                 : "v"(k4), "v"(k4 >> 1), "s"(s_d), "s"(s_s), "s"(s_e) : "memory");
    const u32x4 s_c = srd((const void*)__builtin_amdgcn_s_getpc(), CODE_BYTES), s_we = srd(w_e, 258 * 256), s_wh = srd(w_h, 65536), s_wx = srd(w_x, 256);
    const u32 f2 = role1 ? 0x80000000u : (u32)(tid & 127) * 2, xi = (b >> 3) & 31;
    asm volatile("global_load_dwordx2 %0, %7, %8 sc1\n\tbuffer_load_dword %1, %9, %10, 0 offen\n\t"
                 "buffer_load_ushort %2, %11, %12, %13 offen\n\tbuffer_load_ushort %3, %11, %12, %13 offen offset:256\n\t"
                 "buffer_load_ushort %4, %11, %14, 0 offen\n\t"
                 "buffer_load_dword %5, %15, %12, 0 offen\n\tbuffer_load_dword %6, %16, %17, 0 offen"
                 : "=&v"(epoch), "=&v"(pfc), "=&v"(wdv), "=&v"(wav), "=&v"(wxv), "=&v"(pf1), "=&v"(pf2)
                 : "v"(0u), "s"(ws + WS_EP), "v"(tid < 6 ? (xi * 6 + tid) * 128 : OOB), "s"(s_c), "v"(f2), "s"(s_we), "s"(65536u), "s"(s_wx),
                   "v"(tid - 64 < 8u ? (xi * 8 + tid - 64) * 128 : OOB), "v"(tid - 128 < 16u ? (xi * 16 + tid - 128) * 128 : OOB), "s"(s_wh) : "memory");
  }
  if (tid < MAXG + 4) *(u32*)(lds + L_BC + tid * 4) = 0;
  __syncthreads();
  int bk;
  { const int q = G >> 3, r = G & 7, x = b & 7, i = b >> 3;
    bk = (x < r ? x * (q + 1) : r * (q + 1) + (x - r) * q) + i; }
  const int nb0 = bk * NPB;
  const u32 wvo = g4 * 256 + 16 * (c16 ^ ((g4 << 2) | u));
  u64 ep;
  char* slot;
  u32x4 hb[3][4];
  if (!role1) {
    const u32x4 s_pos = srd(pos, N * 6), s_h = srd(h, N * 256), s_we = srd(w_e, 258 * 256);
#pragma unroll
    for (int i = 0; i < 15; i++) dma16(s_pos, lane * 16, (u + 4 * i) * 1024, lb + L_POS + (u + 4 * i) * 1024);
#pragma unroll
    for (int i = 0; i < 3; i++)
      dma16(s_h, g4 * 256 + 16 * (c16 ^ ((g4 << 2) | ((((i < 2 || u < 2 ? u + 4 * i : u) >> 1) & 3)))), nb0 * 256 + (i < 2 || u < 2 ? u + 4 * i : u) * 1024,
            lb + L_HS + (i < 2 || u < 2 ? u + 4 * i : u) * 1024);
#pragma unroll
    for (int i = 0; i < 8; i++) dma16(s_we, wvo, 32768 + (u + 4 * i) * 1024, lb + L_W0 + (u + 4 * i) * 1024);
    u32 ep_lo, ep_hi;
    u32 wd2, wa2, wx2;
    asm volatile("s_waitcnt vmcnt(11)\n\tv_readfirstlane_b32 %0, %5\n\tv_readfirstlane_b32 %1, %6\n\t"
                 "v_mov_b32 %2, %7\n\tv_mov_b32 %3, %8\n\tv_mov_b32 %4, %9\n\ts_nop 1"
                 : "=s"(ep_lo), "=s"(ep_hi), "=&v"(wd2), "=&v"(wa2), "=&v"(wx2)
                 : "v"(epoch[0]), "v"(epoch[1]), "v"(wdv), "v"(wav), "v"(wxv), "v"(pfc), "v"(pf1), "v"(pf2), "v"(ed[0]), "v"(ed[1]), "v"(ed[2]),
                   "v"(es[0]), "v"(es[1]), "v"(es[2]), "v"(ee[0]), "v"(ee[1]), "v"(ee[2]), "v"(pf3), "v"(pf4) : "memory");
    arrive(lds, 0, lane);
    STAMP(1);
    asm volatile("s_waitcnt vmcnt(0)" ::: "memory");
    arrive(lds, 1, lane);
    ep = ((u64)ep_hi << 32) | ep_lo;
    slot = ws + WS_SLOT0 + (size_t)(ep_lo & 3u) * WS_SLOT;
    await(lds, 1, 4);
    STAMP(12);
#pragma unroll
    for (int s = 0; s < 4; s++)
#pragma unroll
      for (int t = 0; t < 3; t++) hb[t][s] = h_frag(lds, t, s, lane);
    f32x4 acc[3][2];
#pragma unroll
    for (int t = 0; t < 3; t++) { acc[t][0] = f32x4{0, 0, 0, 0}; acc[t][1] = f32x4{0, 0, 0, 0}; }
#pragma unroll
    for (int s = 0; s < 4; s++) {
      const u32x4 fa = tr_frag(lds + L_W0, s, lane, 4 * u + (lane & 3), 0), fb = tr_frag(lds + L_W0, s, lane, 4 * u + (lane & 3), 1);
#pragma unroll
      for (int t = 0; t < 3; t++) { acc[t][0] = mfma16(fa, hb[t][s], acc[t][0]); acc[t][1] = mfma16(fb, hb[t][s], acc[t][1]); }
    }
    const __amdgpu_buffer_rsrc_t r_bw = rsrc(slot + WS_BT, N * 256);
#pragma unroll
    for (int t = 0; t < 3; t++) {
      const f32x4 x = acc[t][0] * LOG2E, y = acc[t][1] * LOG2E;
      const u32x4 o = {pk_bf16(x[0], x[1]), pk_bf16(x[2], x[3]), pk_bf16(y[0], y[1]), pk_bf16(y[2], y[3])};
      __builtin_amdgcn_raw_buffer_store_b128(o, r_bw, (t < 2 || c16 < 8) ? (nb0 + tile_node(t, c16)) * 256 + u * 64 + g4 * 16 : OOB, 0, SC1);
    }
    STAMP(2);
    await(lds, 2, 4);
    const u32x4 z = {0, 0, 0, 0};
#pragma unroll
    for (int i = 0; i < 5; i++) { const int o = L_POS + i * 4096 + tid * 16; if (o < L_SRC) *(u32x4*)(lds + o) = z; }
    *(u32x4*)(lds + L_SRC + tid * 16) = u32x4{OOB, OOB, OOB, OOB};
    if (tid < 16) *(u32*)(lds + L_SRC + 4096 + tid * 4) = OOB;
    const float wd = bf2f(wd2) * LOG2E;
    const u32 wdh = pk_bf16(wd, 0.f);
    const u32 wdp = wdh | (pk_bf16(wd - bf2f(wdh), 0.f) << 16), wap = pk_bf16(bf2f(wa2) * LOG2E, 0.f);
#pragma unroll
    for (int i = 0; i < 4; i++) {
      char* kb = lds + L_KB + (i * 256 + tid) * 16;
      *(u32*)(kb + 12) = wdp;
      *(u16*)(kb + 10) = (u16)wap;
    }
    if (tid < D) *(u16*)(lds + L_WX + tid * 2) = (u16)wx2;
  } else {
    const int t1 = tid - 256;
    const u32x4 z = {0, 0, 0, 0};
#pragma unroll
    for (int i = 0; i < 10; i++) if (i * 4096 + t1 * 16 < L_POS) *(u32x4*)(lds + i * 4096 + t1 * 16) = z;
    SB();
    asm volatile("s_waitcnt vmcnt(" W13 ")" : "+v"(ed[0]), "+v"(ed[1]), "+v"(ed[2]) :: "memory");
    u32 rank[3], ebk[3];
#pragma unroll
    for (int i = 0; i < 3; i++) {
      ebk[i] = ed[i] / NPB;
      rank[i] = 0;
      if (t1 + 256 * i < ne) rank[i] = atomicAdd((u32*)(lds + L_BC) + ebk[i], 1u);
    }
    arrive(lds, 0, lane);
    u32 ep_lo, ep_hi;
    asm volatile("s_waitcnt vmcnt(" NT2 ")\n\tv_readfirstlane_b32 %0, %8\n\tv_readfirstlane_b32 %1, %9\n\ts_nop 4"
                 : "=s"(ep_lo), "=s"(ep_hi), "+v"(es[0]), "+v"(es[1]), "+v"(es[2]), "+v"(ee[0]), "+v"(ee[1]), "+v"(ee[2])
                 : "v"(epoch[0]), "v"(epoch[1]), "v"(pfc), "v"(wdv), "v"(wav), "v"(wxv), "v"(pf1), "v"(pf2) : "memory");
    ep = ((u64)ep_hi << 32) | ep_lo;
    slot = ws + WS_SLOT0 + (size_t)(ep_lo & 3u) * WS_SLOT;
    STAMP(14);
    await(lds, 0, 8);
    STAMP(13);
    const u32x4 c4 = *(const u32x4*)(lds + L_BC + lane * 16);
    u32x2 pd[3], ps[3];
#pragma unroll
    for (int i = 0; i < 3; i++) {
      const u32* qd = (const u32*)(lds + L_POS + ((ed[i] * 6) & ~3u));
      const u32* qs = (const u32*)(lds + L_POS + ((es[i] * 6) & ~3u));
      pd[i] = u32x2{qd[0], qd[1]}; ps[i] = u32x2{qs[0], qs[1]};
    }
    const u32 pown = *(const u16*)(lds + L_POS + min((nb0 * 3 + t1) * 2, MAXPOS - 2));
    const u32 tot = c4[0] + c4[1] + c4[2] + c4[3];
    const u32 ex = scan64(tot) - tot;
    u32* mine = (u32*)(lds + L_LS + u * 1024);
    *(u32x4*)(mine + lane * 4) = u32x4{ex, ex + c4[0], ex + c4[0] + c4[1], ex + c4[0] + c4[1] + c4[2]};
    __builtin_amdgcn_wave_barrier();
    u32 at[3];
#pragma unroll
    for (int i = 0; i < 3; i++) at[i] = mine[ebk[i]];
    const u32 gword = (mine[t1] << 10) | *(const u32*)(lds + L_BC + t1 * 4);
#pragma unroll
    for (int i = 0; i < 3; i++) {
      const u32 shd = (ed[i] & 1u) * 16, shs = (es[i] & 1u) * 16;
      const u32 dxy = __builtin_amdgcn_alignbit(pd[i][1], pd[i][0], shd), sxy = __builtin_amdgcn_alignbit(ps[i][1], ps[i][0], shs);
      const u32 ex2 = pk_bf16(0.f, bf2f(dxy) - bf2f(sxy));
      const u32 yz = pk_bf16(bfhi(dxy) - bfhi(sxy), bf2f(pd[i][1] >> shd) - bf2f(ps[i][1] >> shs));
      u32x4 rc = {es[i] | ((ed[i] - ebk[i] * NPB) << 14) | ((u32)(ep + 1) << 20), (u32)((ep + 1) >> 12), ee[i] | ex2, yz};
      PIN(rc);
      if (t1 + 256 * i < ne) *(u32x4*)(lds + L_RS + (at[i] + rank[i]) * 16) = rc;
    }
    if (t1 < NPB * 3) *(u16*)(lds + L_PD + t1 * 2) = (u16)pown;
    arrive(lds, 2, lane);
    asm volatile("s_waitcnt vmcnt(0)" :: "v"(pf3), "v"(pf4) : "memory");
    await(lds, 2, 4);
    const __amdgpu_buffer_rsrc_t r_rw = rsrc(slot + WS_REC + (size_t)b * CH * 16, CH * 16);
    u32x4 rv[3];
#pragma unroll
    for (int i = 0; i < 3; i++) rv[i] = *(const u32x4*)(lds + L_RS + min(t1 + 256 * i, CH - 1) * 16);
#pragma unroll
    for (int i = 0; i < 3; i++) __builtin_amdgcn_raw_buffer_store_b128(rv[i], r_rw, t1 * 16, i * 4096, SC1);
    __hip_atomic_store((gu64*)(slot + WS_GR) + (size_t)b * MAXG + t1, ((ep + 1) << 20) | gword, RLX_AGENT);
    STAMP(2);
    await(lds, 1, 4);
#pragma unroll
    for (int s = 0; s < 4; s++)
#pragma unroll
      for (int t = 0; t < 3; t++) hb[t][s] = h_frag(lds, t, s, lane);
  }
  STAMP(15);
  __syncthreads();
  const u64 tag = (ep + 1) & 0xfffffffffffull;
  STAMP(3);
  const __amdgpu_buffer_rsrc_t r_bt = rsrc(slot + WS_BT, N * 256), r_rec = rsrc(slot + WS_REC, G * CH * 16);
  const int cs = wv * 32 + (lane & 31), csc = cs < G ? cs : G - 1;
  gu64* const gp = (gu64*)(slot + WS_GR) + (size_t)csc * MAXG + bk;
  gu64* const fp = (gu64*)(slot + WS_FL) + csc;
  const u32 tag0 = (u32)tag & 0xfffu, tag1 = (u32)(tag >> 12);
  u64 gv = __hip_atomic_load(gp, RLX_AGENT);
  SB();
  {
    const u32x4 s_we = srd(w_e, 258 * 256), s_wh = srd(w_h, 65536);
#pragma unroll
    for (int i = 0; i < 4; i++) dma16(s_we, wvo + wv * 1024, i * 8192, lb + L_W0 + (wv + 8 * i) * 1024);
#pragma unroll
    for (int i = 0; i < 4; i++) dma16(s_wh, wvo + wv * 1024, i * 8192, lb + L_W1 + (wv + 8 * i) * 1024);
  }
  const int r = lane & 31, hh = lane >> 5;
  f32x16 aA0, aA1, aB0, aB1, zero;
#pragma unroll
  for (int i = 0; i < 16; i++) { aA0[i] = 0.f; aA1[i] = 0.f; aB0[i] = 0.f; aB1[i] = 0.f; zero[i] = 0.f; }
  const int rh = (r >> 2) & 1, rp = r >> 4, rj = (r & 3) | (((r >> 3) & 1) << 2);
  u32x4 sel0, sel1;
  {
    const u32 one = rh == hh ? 0x3f80u << (rp * 16) : 0u;
#pragma unroll
    for (int q = 0; q < 4; q++) { sel0[q] = (rj == q) ? one : 0u; sel1[q] = (rj == 4 + q) ? one : 0u; }
  }
  PIN(aA0); PIN(aA1); PIN(aB0); PIN(aB1); PIN(sel0); PIN(sel1);
  {
    STAMP(4);
    if (!role1) {
      asm volatile("s_waitcnt vmcnt(0)" ::: "memory");
      if (lane == 0 && __hip_atomic_fetch_add((u32*)(lds + L_SY) + 3, 1u, __ATOMIC_RELAXED, __HIP_MEMORY_SCOPE_WORKGROUP) == 3)
        __hip_atomic_store((gu64*)(slot + WS_FL) + b, tag, RLX_AGENT);
    }
    for (u32 spins = 0; !__all((gv >> 20) == tag) && spins < (1u << 20); spins++) {
      __builtin_amdgcn_s_sleep(1);
      gv = __hip_atomic_load(gp, RLX_AGENT);
    }
    STAMP(5);
    const bool on = lane < 32 && cs < G;
    const u32 cnt = on ? ((u32)gv & 1023u) : 0u, st = ((u32)gv >> 10) & 1023u;
    if (on) *(u32*)(lds + L_BC + cs * 4) = (u32)gv & 0xfffffu;
    const u32 inc = scan64(cnt);
    const u32 total = __builtin_amdgcn_readlane(inc, 63);
    u32* list = (u32*)(lds + L_LS + wv * 768);
    const u32 base = (cs * CH + st) * 16;
    const u32 p0 = inc - cnt;
#pragma unroll
    for (u32 k = 0; k < 6; k++) list[(k < cnt && p0 + k < 128) ? p0 + k : 128 + lane] = base + k * 16;
    if (__any(cnt > 6)) {
#pragma unroll 1
      for (u32 k = 6; k < cnt && p0 + k < 128; k++) list[p0 + k] = base + k * 16;
    }
    __builtin_amdgcn_wave_barrier();
    {
      const u32 a0 = list[lane], a1 = list[64 + lane];
      const bool v0 = lane < total, v1 = 64 + lane < total;
      u32x4 r0 = __builtin_amdgcn_raw_buffer_load_b128(r_rec, v0 ? a0 : OOB, 0, SC1);
      u32x4 r1 = __builtin_amdgcn_raw_buffer_load_b128(r_rec, v1 ? a1 : OOB, 0, SC1);
      u64 fv = __hip_atomic_load(fp, RLX_AGENT);
      SB();
      __syncthreads();
      {
        f32x4 acc[3] = {f32x4{0, 0, 0, 0}, f32x4{0, 0, 0, 0}, f32x4{0, 0, 0, 0}};
#pragma unroll
        for (int s = 0; s < 4; s++) {
          const u32x4 fw = tr_frag(lds + L_W0, s, lane, 2 * wv + ((lane & 3) >> 1), lane & 1);
#pragma unroll
          for (int t = 0; t < 3; t++) acc[t] = mfma16(hb[t][s], fw, acc[t]);
        }
        char* kb = lds + L_KB + (16 * wv + c16) * 16;
#pragma unroll
        for (int t = 0; t < 2; t++) {
          const f32x4 a = acc[t] * LOG2E;
          *(u32x2*)(kb + (4 * t + g4) * 2048) = u32x2{pk_bf16(a[0], a[1]), pk_bf16(a[2], a[3])};
        }
        if (g4 < 2) {
#pragma unroll
          for (int i = 0; i < 4; i++) *(u16*)(kb + (4 * g4 + i) * 2048 + 8) = (u16)pk_bf16(acc[2][i] * LOG2E, 0.f);
        }
      }
      SB();
      r0 = settle(r0, v0, r_rec, a0, tag0, tag1);
      r1 = settle(r1, v1, r_rec, a1, tag0, tag1);
      u32 k0 = 0, k1 = 0;
      if (v0) k0 = take_rank(lds, r0);
      if (v1) k1 = take_rank(lds, r1);
      if (v0) put_rec(lds, r0, k0);
      if (v1) put_rec(lds, r1, k1);
      for (u32 spins = 0; !__all(fv == tag) && spins < (1u << 20); spins++) {
        __builtin_amdgcn_s_sleep(1);
        fv = __hip_atomic_load(fp, RLX_AGENT);
      }
    }
    if (total > 128) {
#pragma unroll 1
      for (u32 done = 128; done < total; done += 64) {
        if (__hip_atomic_load((u32*)(lds + L_CNT + 32), __ATOMIC_RELAXED, __HIP_MEMORY_SCOPE_WORKGROUP)) break;
        __builtin_amdgcn_wave_barrier();
#pragma unroll 1
        for (u32 k = p0 < done ? done - p0 : 0u; k < cnt && p0 + k < done + 64; k++) list[p0 + k - done] = base + k * 16;
        __builtin_amdgcn_wave_barrier();
        const bool v0 = done + lane < total;
        const u32 a0 = list[lane];
        u32x4 r0 = __builtin_amdgcn_raw_buffer_load_b128(r_rec, v0 ? a0 : OOB, 0, SC1);
        r0 = settle(r0, v0, r_rec, a0, tag0, tag1);
        if (v0) put_rec(lds, r0, take_rank(lds, r0));
      }
    }
  }
  asm volatile("s_waitcnt vmcnt(0)" ::: "memory");
  STAMP(6);
  __syncthreads();
  STAMP(8);
  if (b == 0 && tid == 0) __hip_atomic_store((gu64*)(ws + WS_EP), ep + 1, RLX_AGENT);
  const u32* wcnt = (const u32*)(lds + L_CNT);
  const int ovf = __builtin_amdgcn_readfirstlane(wcnt[8]);
  auto load_wh2 = [&]() {
    const u32x4 s_wh = srd(w_h, 65536);
#pragma unroll
    for (int i = 0; i < 4; i++) dma16(s_wh, wvo + wv * 1024, 32768 + i * 8192, lb + L_W0 + (wv + 8 * i) * 1024);
  };
  f32x4 cp[3];
  auto cpre = [&]() {
#pragma unroll
    for (int t = 0; t < 3; t++) cp[t] = f32x4{0, 0, 0, 0};
#pragma unroll
    for (int s = 0; s < 4; s++) {
      const u32x4 f1 = tr_frag(lds + L_W1, s, lane, 2 * wv + ((lane & 3) >> 1), lane & 1);
#pragma unroll
      for (int t = 0; t < 3; t++) cp[t] = mfma16(f1, hb[t][s], cp[t]);
    }
#pragma unroll
    for (int t = 0; t < 3; t++) cp[t] *= LOG2E;
  };
  if (!ovf) {
    int win;
    {
      const u32x4 ca = *(const u32x4*)(wcnt), cb = *(const u32x4*)(wcnt + 4);
      const u32 mine = wcnt[lane & 7] * 8 + (lane & 7);
      u32 rk = 0;
#pragma unroll
      for (int j = 0; j < 4; j++) { rk += (ca[j] * 8 + j) > mine; rk += (cb[j] * 8 + 4 + j) > mine; }
      const u64 m = __ballot(rk == (u32)(wv < 4 ? wv : 11 - wv));
      win = __builtin_ctzll(m);
    }
    const int cntw = __builtin_amdgcn_readfirstlane(wcnt[win]);
    const int nsteps = (cntw + 15) >> 4;
    const u32 lo = r * 4;
    u32 srca = L_SRC + win * CAPW * 4 + hh * 32;
    u32 GA[8], GB[8], off[8];
    auto idx = [&]() {
      const u32x4 i0 = *(const u32x4*)(lds + srca), i1 = *(const u32x4*)(lds + srca + 16);
      srca += 64;
#pragma unroll
      for (int j = 0; j < 4; j++) { off[j] = i0[j] + lo; off[4 + j] = i1[j] + lo; }
    };
    auto ldA = [&]() {
#pragma unroll
      for (int j = 0; j < 8; j++) GA[j] = __builtin_amdgcn_raw_buffer_load_b32(r_bt, off[j], 0, SC1);
    };
    auto ldB = [&]() {
#pragma unroll
      for (int j = 0; j < 8; j++) GB[j] = __builtin_amdgcn_raw_buffer_load_b32(r_bt, off[j] + 128, 0, SC1);
    };
    idx(); ldA(); SB(); ldB(); SB();
    STAMP(7);
    cpre();
    SB();
    const u32x4 kbA = *(const u32x4*)(lds + L_KB + win * 2048 + (2 * r + hh) * 16);
    const u32x4 kbB = *(const u32x4*)(lds + L_KB + win * 2048 + (64 + 2 * r + hh) * 16);
    const bool cfon = rp == hh;
    u32 cfa = cfon ? (L_CF + (win * CAPW + 8 * rh + rj) * 16) : L_ZERO;
    const u32 cfs = cfon ? 256u : 0u;
    u32 sma = L_SM + win * 8 * 640 + hh * 320 + r * 16;

    u32x4 cf = *(const u32x4*)(lds + cfa), sf = {0, 0, 0, 0}, mB0 = {0, 0, 0, 0}, mB1 = {0, 0, 0, 0}, mA0, mA1;
    f32x16 yA = mfma32(cf, kbA, zero), yB;
    idx();
    yA = mfma32(sel0, u32x4{GA[0], GA[1], GA[2], GA[3]}, yA);
    yA = mfma32(sel1, u32x4{GA[4], GA[5], GA[6], GA[7]}, yA);
    PIN(yA);
    SB();
    ldA();
    SB();
#define MF(d, a, bb, c) do { SB(); d = mfma32(a, bb, c); PIN(d); SB(); } while (0)
#define SA(y, i, j) do { p_ = f32x2{__builtin_amdgcn_exp2f(-y[i]), __builtin_amdgcn_exp2f(-y[(i) + 1])}; \
                         q_ = f32x2{__builtin_amdgcn_exp2f(-y[j]), __builtin_amdgcn_exp2f(-y[(j) + 1])}; SB(); p_ += 1.0f; q_ += 1.0f; \
                         asm volatile("" : "+v"(p_), "+v"(q_)); SB(); } while (0)
#define SBB(y, i, j, o0, o1) do { p_ = f32x2{__builtin_amdgcn_rcpf(p_[0]), __builtin_amdgcn_rcpf(p_[1])}; \
                         q_ = f32x2{__builtin_amdgcn_rcpf(q_[0]), __builtin_amdgcn_rcpf(q_[1])}; SB(); \
                         p_ *= f32x2{y[i], y[(i) + 1]}; q_ *= f32x2{y[j], y[(j) + 1]}; SB(); \
                         u32 t0_ = pk_bf16(p_[0], p_[1]), t1_ = pk_bf16(q_[0], q_[1]); \
                         asm volatile("" : "+v"(t0_), "+v"(t1_)); o0 = t0_; o1 = t1_; SB(); } while (0)
#define S4(y, i, j, o0, o1) do { SA(y, i, j); SBB(y, i, j, o0, o1); } while (0)
#define LD2(Gx, j, add) do { Gx[j] = __builtin_amdgcn_raw_buffer_load_b32(r_bt, off[j] + (add), 0, SC1); \
                             Gx[(j) + 1] = __builtin_amdgcn_raw_buffer_load_b32(r_bt, off[(j) + 1] + (add), 0, SC1); SB(); } while (0)
    f32x2 p_, q_;
    for (int s = 1; s < nsteps; s++) {
      MF(yB, cf, kbB, zero);
      S4(yA, 0, 2, mA0[0], mA0[1]);
      MF(yB, sel0, (u32x4{GB[0], GB[1], GB[2], GB[3]}), yB);
      cfa += cfs;
      cf = *(const u32x4*)(lds + cfa);
      SA(yA, 4, 6);
      LD2(GB, 0, 128);
      SBB(yA, 4, 6, mA0[2], mA0[3]);
      LD2(GB, 2, 128);
      MF(yB, sel1, (u32x4{GB[4], GB[5], GB[6], GB[7]}), yB);
      SA(yA, 8, 10);
      LD2(GB, 4, 128);
      MF(aB0, mB0, sf, aB0);
      SBB(yA, 8, 10, mA1[0], mA1[1]);
      LD2(GB, 6, 128);
      const u32x4 i0 = *(const u32x4*)(lds + srca), i1 = *(const u32x4*)(lds + srca + 16);
      srca += 64;
      SA(yA, 12, 14);
      MF(aB1, mB1, sf, aB1);
      sf = *(const u32x4*)(lds + sma);
      sma += 640;
      SBB(yA, 12, 14, mA1[2], mA1[3]);
      MF(yA, cf, kbA, zero);
      S4(yB, 0, 2, mB0[0], mB0[1]);
      MF(yA, sel0, (u32x4{GA[0], GA[1], GA[2], GA[3]}), yA);
#pragma unroll
      for (int j = 0; j < 4; j++) { off[j] = i0[j] + lo; off[4 + j] = i1[j] + lo; }
      SA(yB, 4, 6);
      LD2(GA, 0, 0);
      SBB(yB, 4, 6, mB0[2], mB0[3]);
      LD2(GA, 2, 0);
      MF(yA, sel1, (u32x4{GA[4], GA[5], GA[6], GA[7]}), yA);
      SA(yB, 8, 10);
      LD2(GA, 4, 0);
      MF(aA0, mA0, sf, aA0);
      SBB(yB, 8, 10, mB1[0], mB1[1]);
      LD2(GA, 6, 0);
      SA(yB, 12, 14);
      MF(aA1, mA1, sf, aA1);
      SBB(yB, 12, 14, mB1[2], mB1[3]);
    }
    {
      const int rem = cntw - 16 * (nsteps - 1);
      load_wh2();
      MF(yB, cf, kbB, zero);
      mA0 = u32x4{0, 0, 0, 0}; mA1 = u32x4{0, 0, 0, 0};
      S4(yA, 0, 8, mA0[0], mA1[0]);
      MF(yB, sel0, (u32x4{GB[0], GB[1], GB[2], GB[3]}), yB);
      if (rem > 4) S4(yA, 2, 10, mA0[1], mA1[1]);
      MF(yB, sel1, (u32x4{GB[4], GB[5], GB[6], GB[7]}), yB);
      if (rem > 8) S4(yA, 4, 12, mA0[2], mA1[2]);
      MF(aB0, mB0, sf, aB0);
      if (rem > 12) S4(yA, 6, 14, mA0[3], mA1[3]);
      MF(aB1, mB1, sf, aB1);
      sf = *(const u32x4*)(lds + sma);
      mB0 = u32x4{0, 0, 0, 0}; mB1 = u32x4{0, 0, 0, 0};
      S4(yB, 0, 8, mB0[0], mB1[0]);
      MF(aA0, mA0, sf, aA0);
      if (rem > 4) S4(yB, 2, 10, mB0[1], mB1[1]);
      MF(aA1, mA1, sf, aA1);
      if (rem > 8) S4(yB, 4, 12, mB0[2], mB1[2]);
      if (rem > 12) S4(yB, 6, 14, mB0[3], mB1[3]);
      MF(aB0, mB0, sf, aB0);
      MF(aB1, mB1, sf, aB1);
    }
    STAMP(9);
    f32x16 z = zero;
    u32x4 v[8];
#pragma unroll
    for (int q8 = 0; q8 < 8; q8++) {
      const f32x16& a0 = q8 < 4 ? aA0 : aB0;
      const f32x16& a1 = q8 < 4 ? aA1 : aB1;
#pragma unroll
      for (int q = 0; q < 4; q++) v[q8][q] = pk_bf16(a0[4 * (q8 & 3) + q], a1[4 * (q8 & 3) + q]);
    }
    if (r < 5) {
      char* mi = lds + mi_row(r * 8 + win) + 16 * hh;
#pragma unroll
      for (int q8 = 0; q8 < 8; q8++) *(u32x4*)(mi + 32 * q8) = v[q8];
    }
#pragma unroll
    for (int q8 = 0; q8 < 8; q8++) z = mfma32(*(const u32x4*)(lds + L_WX + (16 * q8 + 8 * hh) * 2), v[q8], z);
    {
      const int kind = (lane * 13) >> 6, nl = (lane - kind * 5) * 8 + win, i = nl * 3 + kind - 1;
      const bool pon = lane >= 5 && lane < 20;
      const float p0 = bf2f(*(const u16*)(lds + L_PD + (pon ? i : 0) * 2));
      __builtin_amdgcn_raw_buffer_store_b16((u16)pk_bf16(p0 + LN2 * z[0], 0.f), rsrc(out + (size_t)N * D, N * 6), pon ? (nb0 * 3 + i) * 2 : OOB, 0, 0);
    }
  } else {
    float* macc = (float*)(lds + L_SM);
    float* pacc = (float*)(lds + L_SM + NPB * D * 4);
    load_wh2();
    cpre();
    __syncthreads();
    for (int i = tid; i < NPB * D + NPB * 4; i += NT) macc[i] = 0.f;
    __syncthreads();
    const u32 wxp = *(const u32*)(lds + L_WX + lane * 4);
    const float wx0 = bf2f(wxp & 0xffffu), wx1 = bfhi(wxp);
    const u32x4 kc0 = *(const u32x4*)(lds + L_KB + (2 * lane) * 16), kc1 = *(const u32x4*)(lds + L_KB + (2 * lane + 1) * 16);
    const float wd0 = bf2f(kc0[3] & 0xffffu) + bfhi(kc0[3]), wd1 = bf2f(kc1[3] & 0xffffu) + bfhi(kc1[3]);
    const float wa0 = bfhi(kc0[2]), wa1 = bfhi(kc1[2]);
    u32 rot = 0;
    for (int c = 0; c < G; c++) {
      const u32 oo = __builtin_amdgcn_readfirstlane(*(const u32*)(lds + L_BC + c * 4));
      const u32 s0 = oo >> 10, n = oo & 1023u;
      for (u32 k = (wv - rot) & 7u; k < n; k += 8) {
        u32x4 rr = __builtin_amdgcn_raw_buffer_load_b128(r_rec, (c * CH + s0 + k) * 16, 0, SC1);
        rr = settle(rr, true, r_rec, (c * CH + s0 + k) * 16, tag0, tag1);
        const u32 nl = (rr[0] >> 14) & 0x3fu, sl = nl >> 3, wn = nl & 7u;
        const u32 bv = __builtin_amdgcn_raw_buffer_load_b32(r_bt, ((rr[0] & 0x3fffu) << 8) + lane * 4, 0, SC1);
        const float d2 = bfhi(dist2(rr[2], rr[3])), eav = bf2f(rr[2]);
        const float a0 = bf2f(*(const u16*)(lds + L_KB + wn * 2048 + (2 * lane) * 16 + sl * 2));
        const float a1 = bf2f(*(const u16*)(lds + L_KB + wn * 2048 + (2 * lane + 1) * 16 + sl * 2));
        const float y0 = a0 + bf2f(bv) + d2 * wd0 + eav * wa0, y1 = a1 + bfhi(bv) + d2 * wd1 + eav * wa1;
        const u32 mp = silu2(y0, y1);
        const float m0 = bf2f(mp), m1 = bfhi(mp);
        atomicAdd(&macc[nl * D + 2 * lane], m0);
        atomicAdd(&macc[nl * D + 2 * lane + 1], m1);
        float csv = m0 * wx0 + m1 * wx1;
#pragma unroll
        for (int o = 32; o > 0; o >>= 1) csv += __shfl_xor(csv, o, 64);
        if (lane < 3) atomicAdd(&pacc[nl * 4 + lane], csv * (lane == 0 ? bfhi(rr[2]) : lane == 1 ? bf2f(rr[3]) : bfhi(rr[3])));
      }
      rot += n;
    }
    __syncthreads();
    for (int i = tid; i < NPB * D / 2; i += NT) {
      const int n = i >> 6, f = (i & 63) * 2;
      *(u32*)(lds + mi_row(n) + f * 2) = pk_bf16(macc[n * D + f], macc[n * D + f + 1]);
    }
    if (tid < NPB * 3) {
      const int n = tid / 3, c = tid - n * 3;
      if (nb0 + n < N) out[(size_t)N * D + (size_t)nb0 * 3 + tid] = (u16)pk_bf16(bf2f(*(const u16*)(lds + L_PD + tid * 2)) + LN2 * pacc[n * 4 + c], 0.f);
    }
  }
  u32x2 hr[3];
#pragma unroll
  for (int t = 0; t < 3; t++) {
    const int n = tile_node(t, c16);
    hr[t] = *(const u32x2*)(lds + L_HS + n * 256 + 16 * ((2 * wv + (g4 >> 1)) ^ hswz(n)) + 8 * (g4 & 1));
  }
  asm volatile("s_waitcnt vmcnt(0)" ::: "memory");
  __syncthreads();
  STAMP(10);
  {
#pragma unroll
    for (int s = 0; s < 4; s++) {
      const u32x4 f2 = tr_frag(lds + L_W0, s, lane, 2 * wv + ((lane & 3) >> 1), lane & 1);
#pragma unroll
      for (int t = 0; t < 3; t++) cp[t] = mfma16(f2, *(const u32x4*)(lds + mi_row(tile_node(t, c16)) + s * 64 + g4 * 16), cp[t]);
    }
#pragma unroll
    for (int t = 0; t < 3; t++) {
      const f32x4 y = cp[t];
      float o[4];
#pragma unroll
      for (int i = 0; i < 4; i++)
        o[i] = ((i & 1) ? bfhi(hr[t][i >> 1]) : bf2f(hr[t][i >> 1])) + LN2 * y[i] * __builtin_amdgcn_rcpf(1.f + __builtin_amdgcn_exp2f(-y[i]));
      u32x2 ov = {pk_bf16(o[0], o[1]), pk_bf16(o[2], o[3])};
      *(u32x2*)(lds + L_SM + tile_node(t, c16) * 272 + wv * 32 + g4 * 8) = ov;
    }
    __syncthreads();
    const __amdgpu_buffer_rsrc_t r_out = rsrc(out, N * 256);
    const u32x4 o0 = *(const u32x4*)(lds + L_SM + (tid >> 4) * 272 + (tid & 15) * 16);
    __builtin_amdgcn_raw_buffer_store_b128(o0, r_out, nb0 * 256 + tid * 16, 0, 0);
    if (wv < 2) {
      const u32x4 o1 = *(const u32x4*)(lds + L_SM + (32 + (tid >> 4)) * 272 + (tid & 15) * 16);
      __builtin_amdgcn_raw_buffer_store_b128(o1, r_out, nb0 * 256 + tid * 16, 8192, 0);
    }
  }
  STAMP(11);
}

extern "C" void kernel_launch(void* const* d_in, const int* in_sizes, int n_in, void* d_out, int out_size, void* d_ws,
                              size_t ws_size, hipStream_t stream) {
  const int N = in_sizes[0] / D, E = in_sizes[3];
  const int NB = (N + NPB - 1) / NPB, C = (E + CH - 1) / CH;
  const int G = NB > C ? NB : C;
  k_egnn<<<G, NT, 0, stream>>>((const u16*)d_in[0], (const u16*)d_in[1], (const u16*)d_in[2], (const int*)d_in[3],
                               (const int*)d_in[4], (const u16*)d_in[5], (const u16*)d_in[6], (const u16*)d_in[7],
                               (char*)d_ws, (u16*)d_out, N, E, G);
}
```

```cpp
#include <hip/hip_runtime.h>
#include <hip/hip_bf16.h>

typedef unsigned int u32;
typedef unsigned short u16;
typedef unsigned long long u64;
typedef u32 u32x2 __attribute__((ext_vector_type(2)));
typedef u32 u32x4 __attribute__((ext_vector_type(4)));
typedef short s16x4 __attribute__((ext_vector_type(4)));
typedef short bf16x8 __attribute__((ext_vector_type(8)));
typedef float f32x2 __attribute__((ext_vector_type(2)));
typedef float f32x4 __attribute__((ext_vector_type(4)));
typedef float f32x16 __attribute__((ext_vector_type(16)));
typedef __bf16 bf16x2_t __attribute__((ext_vector_type(2)));
typedef __attribute__((address_space(1))) u64 gu64;
typedef __attribute__((address_space(3))) s16x4 lds_s16x4;
typedef __attribute__((address_space(3))) char lds_char;
#define DI __device__ __forceinline__
#define RLX_AGENT __ATOMIC_RELAXED, __HIP_MEMORY_SCOPE_AGENT
#define SC1 16
#define SB() __builtin_amdgcn_sched_barrier(0)
#define PIN(x) asm volatile("" : "+v"(x))
#define RFL(x) __builtin_amdgcn_readfirstlane(x)

constexpr int D = 128;
constexpr int NT = 512;
constexpr int NPB = 40;
constexpr int CH = 640;
constexpr int MAXG = 256;
constexpr int CAPW = 128;
constexpr int MAXPOS = 61440;
#define PFR 16
constexpr float LOG2E = 1.4426950408889634f;
constexpr float LN2 = 0.6931471805599453f;
constexpr u32 OOB = 0xfffff000u;

constexpr size_t WS_EP = 0;
constexpr size_t WS_SLOT0 = (size_t)2 << 20, WS_SLOT = (size_t)6 << 20;
constexpr size_t WS_GR = 0;
constexpr size_t WS_BT = WS_GR + (size_t)MAXG * MAXG * 8;
constexpr size_t WS_REC = WS_BT + (size_t)MAXG * NPB * 256;
constexpr size_t WS_FL = WS_REC + (size_t)MAXG * CH * 16;
static_assert(WS_FL + MAXG * 8 <= WS_SLOT, "slot");

#define STAMP(k) do { } while (0)

DI float bf2f(u32 b) { return __builtin_bit_cast(float, b << 16); }
DI float bfhi(u32 b) { return __builtin_bit_cast(float, b & 0xffff0000u); }
DI u32 pk_bf16(float lo, float hi) {
  f32x2 v = {lo, hi};
  bf16x2_t r = __builtin_convertvector(v, bf16x2_t);
  return __builtin_bit_cast(u32, r);
}
DI f32x4 mfma16(u32x4 a, u32x4 b, f32x4 c) {
  return __builtin_amdgcn_mfma_f32_16x16x32_bf16(__builtin_bit_cast(bf16x8, a), __builtin_bit_cast(bf16x8, b), c, 0, 0, 0);
}
DI f32x16 mfma32(u32x4 a, u32x4 b, f32x16 c) {
  return __builtin_amdgcn_mfma_f32_32x32x16_bf16(__builtin_bit_cast(bf16x8, a), __builtin_bit_cast(bf16x8, b), c, 0, 0, 0);
}
DI __amdgpu_buffer_rsrc_t rsrc(const void* p, u32 bytes) { return __builtin_amdgcn_make_buffer_rsrc((void*)p, 0, bytes, 0x00020000); }
DI u32x4 srd(const void* p, u32 bytes) { const u64 a = (u64)p; return u32x4{(u32)a, (u32)(a >> 32) & 0xffffu, bytes, 0x00020000u}; }
DI void dma4(u32x4 sd, u32 v0, u32 v1, u32 soff, u32 ldsa) {
  u32 keep;
  asm volatile("s_mov_b32 %0, m0\n\ts_mov_b32 m0, %1\n\ts_nop 0\n\t"
               "buffer_load_dwordx4 %2, %4, %5 offen lds\n\tbuffer_load_dwordx4 %2, %4, %5 offen offset:1024 lds\n\t"
               "buffer_load_dwordx4 %3, %4, %5 offen offset:2048 lds\n\tbuffer_load_dwordx4 %3, %4, %5 offen offset:3072 lds\n\t"
               "s_mov_b32 m0, %0"
               : "=&s"(keep) : "s"(RFL(ldsa)), "v"(v0), "v"(v1), "s"(sd), "s"(soff) : "memory");
}
DI void dma2(u32x4 sd, u32 v0, u32 soff, u32 ldsa) {
  u32 keep;
  asm volatile("s_mov_b32 %0, m0\n\ts_mov_b32 m0, %1\n\ts_nop 0\n\t"
               "buffer_load_dwordx4 %2, %3, %4 offen lds\n\tbuffer_load_dwordx4 %2, %3, %4 offen offset:1024 lds\n\t"
               "s_mov_b32 m0, %0"
               : "=&s"(keep) : "s"(RFL(ldsa)), "v"(v0), "s"(sd), "s"(soff) : "memory");
}
DI u32 scan64(u32 x) {
  x += __builtin_amdgcn_update_dpp(0u, x, 0x111, 0xf, 0xf, false);
  x += __builtin_amdgcn_update_dpp(0u, x, 0x112, 0xf, 0xf, false);
  x += __builtin_amdgcn_update_dpp(0u, x, 0x114, 0xf, 0xf, false);
  x += __builtin_amdgcn_update_dpp(0u, x, 0x118, 0xf, 0xf, false);
  x += __builtin_amdgcn_update_dpp(0u, x, 0x142, 0xa, 0xf, false);
  x += __builtin_amdgcn_update_dpp(0u, x, 0x143, 0xc, 0xf, false);
  return x;
}

constexpr int L_ZERO = 0;
constexpr int L_CNT = L_ZERO + 16;
constexpr int L_BC = L_CNT + 48;
constexpr int L_SY = L_BC + 1024;
constexpr int L_SM = L_SY + 32;
constexpr int L_CF = L_SM + 8 * 8 * 640;
constexpr int L_SRC = L_CF + 8 * CAPW * 16;
constexpr int L_KB = L_SRC + 8 * CAPW * 4 + 64;
constexpr int L_W1 = L_KB + 8 * D * 16;
constexpr int L_W0 = L_W1 + 32768;
constexpr int L_HS = L_W0 + 32768;
constexpr int L_WX = L_HS + NPB * 256;
constexpr int L_PD = L_WX + 256;
constexpr int L_LS = L_PD + 256;
constexpr int L_END = L_LS + 8 * 768;
constexpr int L_RS = L_W0 - CH * 16;
constexpr int L_POS = L_RS - MAXPOS;
static_assert(L_END <= 160 * 1024, "lds");
static_assert(L_POS >= L_SM + 36864 && L_POS <= L_CF && (L_POS % 16) == 0, "pos table");
static_assert((L_SM % 16) == 0 && (L_BC % 16) == 0 && (L_SRC % 16) == 0 && (L_KB % 16) == 0 && (L_W0 % 16) == 0 && (L_HS % 16) == 0 && (L_WX % 16) == 0 && (L_LS % 16) == 0, "align");

DI int tile_node(int t, int c) { return t < 2 ? ((c & 3) * 8 + 4 * t + (c >> 2)) : (32 + (c & 7)); }
DI int mi_row(int nl) { return L_KB + (nl & 7) * 2048 + (nl & 3) * 64 + (nl >> 3) * 272; }
DI int wswz(int k) { return ((k & 3) << 2) | ((k >> 2) & 2); }
DI int hswz(int n) { return ((n & 3) << 2) | ((n >> 3) & 3); }
DI u32x4 tr_frag(const char* img, int s, int lane, int ch, int hf) {
  const int k0 = 8 * (lane >> 4) + ((lane & 15) >> 2);
  const char* p = img + (32 * s + k0) * 256 + 16 * (ch ^ wswz(k0)) + 8 * hf;
  const s16x4 a = __builtin_amdgcn_ds_read_tr16_b64_v4i16((lds_s16x4*)p);
  const s16x4 b = __builtin_amdgcn_ds_read_tr16_b64_v4i16((lds_s16x4*)(p + 1024));
  const u32x2 x = __builtin_bit_cast(u32x2, a), y = __builtin_bit_cast(u32x2, b);
  return u32x4{x[0], x[1], y[0], y[1]};
}
DI u32x4 s_frag(const char* p) {
  const u32x2 x = __builtin_bit_cast(u32x2, __builtin_amdgcn_ds_read_tr16_b64_v4i16((lds_s16x4*)p));
  const u32x2 y = __builtin_bit_cast(u32x2, __builtin_amdgcn_ds_read_tr16_b64_v4i16((lds_s16x4*)(p + 160)));
  return u32x4{x[0], x[1], y[0], y[1]};
}
DI u32x4 h_frag(const char* lds, int t, int s, int lane) {
  const int n = tile_node(t, lane & 15);
  return *(const u32x4*)(lds + L_HS + n * 256 + 16 * ((4 * s + (lane >> 4)) ^ hswz(n)));
}
DI u32 dist2(u32 w2, u32 w3) {
  const float dx = bfhi(w2), dy = bf2f(w3), dz = bfhi(w3);
  const u32 q0 = pk_bf16(dx * dx, dy * dy), q1 = pk_bf16(dz * dz, 0.f);
  const float s2 = bf2f(q0) + bfhi(q0) + bf2f(q1);
  return pk_bf16(s2, s2);
}
DI u32 take_rank(char* lds, u32x4 rc) { return atomicAdd((u32*)(lds + L_CNT) + ((rc[0] >> 14) & 7u), 1u); }
DI void put_rec(char* lds, u32x4 rc, u32 rank) {
  const u32 win = (rc[0] >> 14) & 7u, slot = (rc[0] >> 17) & 7u;
  const u64 oh = 0x3f80ull << (slot * 16);
  const u32x4 cf = {slot < 4 ? (u32)oh : 0u, slot < 4 ? (u32)(oh >> 32) : 0u, (slot == 4 ? 0x3f80u : 0u) | (rc[2] << 16), dist2(rc[2], rc[3])};
  if (rank < CAPW) {
    const u32 k = ((rank & 1u) << 3) + ((rank & 15u) >> 1), sp = win * CAPW + (rank & ~15u) + k;
    *(u32*)(lds + L_SRC + sp * 4) = (rc[0] & 0x3fffu) << 8;
    *(u32x4*)(lds + L_CF + sp * 16) = cf;
    *(u32x2*)(lds + L_SM + sp * 40 + slot * 8) = u32x2{0x3f80u | (rc[2] & 0xffff0000u), rc[3]};
  } else *(u32*)(lds + L_CNT + 32) = 1;
}
DI u32x4 settle(u32x4 rc, bool on, __amdgpu_buffer_rsrc_t r, u32 off, u32 tag0, u32 tag1) {
  for (u32 spins = 0; __any(on && ((rc[0] >> 20) != tag0 || rc[1] != tag1)) && spins < (1u << 20); spins++) {
    __builtin_amdgcn_s_sleep(4);
    if (on && ((rc[0] >> 20) != tag0 || rc[1] != tag1)) rc = __builtin_amdgcn_raw_buffer_load_b128(r, off, 0, SC1);
  }
  return rc;
}
DI void arrive(u32 lb, int i) {
  asm volatile("s_mov_b64 exec, 1\n\tds_add_u32 %0, %1 offset:%2\n\ts_mov_b64 exec, -1" :: "v"(lb), "v"(1u), "i"(L_SY + 4 * i) : "memory");
}
DI void await(char* lds, int i, u32 n) {
  asm volatile("" ::: "memory");
  while ((u32)RFL(__hip_atomic_load((u32*)(lds + L_SY) + i, __ATOMIC_RELAXED, __HIP_MEMORY_SCOPE_WORKGROUP)) < n) { }
  asm volatile("" ::: "memory");
}
DI u32 silu2(float a, float b) {
  f32x2 yy = {a, b};
  f32x2 e = {__builtin_amdgcn_exp2f(-a), __builtin_amdgcn_exp2f(-b)};
  e += 1.0f;
  f32x2 rc = {__builtin_amdgcn_rcpf(e[0]), __builtin_amdgcn_rcpf(e[1])};
  yy *= rc;
  return pk_bf16(yy[0], yy[1]);
}

__global__ __launch_bounds__(NT, 2) void k_egnn(const u16* __restrict__ h, const u16* __restrict__ pos,
                                                const u16* __restrict__ ea, const int* __restrict__ src,
                                                const int* __restrict__ dst, const u16* __restrict__ w_e,
                                                const u16* __restrict__ w_x, const u16* __restrict__ w_h,
                                                char* __restrict__ ws, u16* __restrict__ out, int N, int E, int G) {
  __shared__ __attribute__((aligned(16))) char lds[L_END];
  const int tid = threadIdx.x, lane = tid & 63, b = blockIdx.x;
  const int wv = RFL(tid >> 6);
  const int u = wv & 3, c16 = lane & 15, g4 = lane >> 4;
  const bool role1 = wv >= 4;
  const u32 lb = (u32)(size_t)(lds_char*)lds;
  if (tid < L_SM / 4) *(u32*)(lds + tid * 4) = 0;
  __syncthreads();
  const int ne = min(CH, E - b * CH);
  u32 ed[3], es[3], ee[3];
  u32x2 epoch;
  {
    const u32x4 s_d = srd(dst + b * CH, ne * 4), s_s = srd(src + b * CH, ne * 4), s_e = srd(ea + b * CH, ne * 2);
    asm volatile("s_cmp_lt_u32 %15, 4\n\ts_cbranch_scc1 1f\n\t"
                 "buffer_load_dword %0, %10, %12, 0 offen\n\tbuffer_load_dword %1, %10, %12, 0 offen offset:1024\n\t"
                 "buffer_load_dword %2, %10, %12, 0 offen offset:2048\n\tbuffer_load_dword %3, %10, %13, 0 offen\n\t"
                 "buffer_load_dword %4, %10, %13, 0 offen offset:1024\n\tbuffer_load_dword %5, %10, %13, 0 offen offset:2048\n\t"
                 "buffer_load_ushort %6, %11, %14, 0 offen\n\tbuffer_load_ushort %7, %11, %14, 0 offen offset:512\n\t"
                 "buffer_load_ushort %8, %11, %14, 0 offen offset:1024\n"
                 "1:\n\tglobal_load_dwordx2 %9, %16, %17 sc1"
                 : "=&v"(ed[0]), "=&v"(ed[1]), "=&v"(ed[2]), "=&v"(es[0]), "=&v"(es[1]), "=&v"(es[2]), "=&v"(ee[0]), "=&v"(ee[1]), "=&v"(ee[2]), "=&v"(epoch)
                 : "v"((tid & 255) * 4), "v"((tid & 255) * 2), "s"(s_d), "s"(s_s), "s"(s_e), "s"(wv), "v"(0u), "s"(ws + WS_EP) : "memory", "scc");
  }
  int bk;
  { const int q = G >> 3, r = G & 7, x = b & 7, i = b >> 3;
    bk = (x < r ? x * (q + 1) : r * (q + 1) + (x - r) * q) + i; }
  const int nb0 = bk * NPB;
  const int r = lane & 31, hh = lane >> 5;
  const int rh = (r >> 2) & 1, rp = r >> 4, rj = (r & 3) | (((r >> 3) & 1) << 2);
  f32x16 aA0, aA1, aB0, aB1, zero;
  u32x4 sel0, sel1;
  auto setup = [&]() {
#pragma unroll
    for (int i = 0; i < 16; i++) zero[i] = 0.f;
    u32x4 z4 = {0, 0, 0, 0};
    PIN(z4);
    aA0 = mfma32(z4, z4, zero); aA1 = mfma32(z4, z4, zero); aB0 = mfma32(z4, z4, zero); aB1 = mfma32(z4, z4, zero);
    const u32 one = rh == hh ? 0x3f80u << (rp * 16) : 0u;
#pragma unroll
    for (int q = 0; q < 4; q++) { sel0[q] = (rj == q) ? one : 0u; sel1[q] = (rj == 4 + q) ? one : 0u; }
    PIN(aA0); PIN(aA1); PIN(aB0); PIN(aB1); PIN(sel0); PIN(sel1);
  };
  u32 ep_lo, ep_hi;
  u32x4 hb[3][4];
  const u32 wv0 = g4 * 256 + 16 * (c16 ^ (g4 << 2)) + u * 4096, wv1 = wv0 ^ 32;
  if (!role1) {
    const u32x4 s_pos = srd(pos, N * 6), s_h = srd(h, N * 256), s_we = srd(w_e, 258 * 256), s_wh = srd(w_h, 65536);
#pragma unroll
    for (int i = 0; i < 4; i++)
      if (i < 3 || u < 3) dma4(s_pos, lane * 16 + u * 4096, lane * 16 + u * 4096, i * 16384, lb + L_POS + i * 16384 + u * 4096);
    dma2(s_h, g4 * 256 + 16 * (c16 ^ ((g4 << 2) | u)) + u * 2048, nb0 * 256, lb + L_HS + u * 2048);
    if (u == 0) dma2(s_h, g4 * 256 + 16 * (c16 ^ (g4 << 2)) + 8192, nb0 * 256, lb + L_HS + 8192);
    dma4(s_we, wv0, wv1, 32768, lb + L_W0 + u * 4096);
    dma4(s_we, wv0, wv1, 32768 + 16384, lb + L_W0 + 16384 + u * 4096);
    u32 wdv, wav, wxv, pf1, pf2;
    {
      const u32x4 s_wx = srd(w_x, 256);
      const u32 xi = (b >> 3) & 31;
      asm volatile("buffer_load_ushort %0, %5, %6, %7 offen\n\tbuffer_load_ushort %1, %5, %6, %7 offen offset:256\n\t"
                   "buffer_load_ushort %2, %5, %8, 0 offen\n\tbuffer_load_dword %3, %9, %6, 0 offen\n\t"
                   "buffer_load_dword %4, %10, %11, 0 offen"
                   : "=&v"(wdv), "=&v"(wav), "=&v"(wxv), "=&v"(pf1), "=&v"(pf2)
                   : "v"((tid & 127) * 2), "s"(s_we), "s"(65536u), "s"(s_wx), "v"(tid < 8 ? (xi * 8 + tid) * 128 : OOB),
                     "v"(tid - 64 < 16u ? (xi * 16 + tid - 64) * 128 : OOB), "s"(s_wh) : "memory");
    }
    asm volatile("s_waitcnt vmcnt(15)\n\tv_readfirstlane_b32 %0, %2\n\tv_readfirstlane_b32 %1, %3"
                 : "=s"(ep_lo), "=s"(ep_hi) : "v"(epoch[0]), "v"(epoch[1]) : "memory");
    arrive(lb, 0);
    setup();
    asm volatile("s_waitcnt vmcnt(5)" ::: "memory");
    arrive(lb, 1);
    char* const slot = ws + WS_SLOT0 + (size_t)(RFL(ep_lo) & 3u) * WS_SLOT;
    await(lds, 1, 4);
    STAMP(12);
#pragma unroll
    for (int s = 0; s < 4; s++)
#pragma unroll
      for (int t = 0; t < 3; t++) hb[t][s] = h_frag(lds, t, s, lane);
    f32x4 acc[3][2];
#pragma unroll
    for (int t = 0; t < 3; t++) { acc[t][0] = f32x4{0, 0, 0, 0}; acc[t][1] = f32x4{0, 0, 0, 0}; }
#pragma unroll
    for (int s = 0; s < 4; s++) {
      const u32x4 fa = tr_frag(lds + L_W0, s, lane, 4 * u + (lane & 3), 0), fb = tr_frag(lds + L_W0, s, lane, 4 * u + (lane & 3), 1);
#pragma unroll
      for (int t = 0; t < 3; t++) { acc[t][0] = mfma16(fa, hb[t][s], acc[t][0]); acc[t][1] = mfma16(fb, hb[t][s], acc[t][1]); }
    }
    asm volatile("s_waitcnt vmcnt(0)" : "+v"(wdv), "+v"(wav), "+v"(wxv), "+v"(pf1), "+v"(pf2), "+v"(acc[2][1]) :: "memory");
    arrive(lb, 3);
    const __amdgpu_buffer_rsrc_t r_bw = rsrc(slot + WS_BT, N * 256);
#pragma unroll
    for (int t = 0; t < 3; t++) {
      const f32x4 x = acc[t][0] * LOG2E, y = acc[t][1] * LOG2E;
      const u32x4 o = {pk_bf16(x[0], x[1]), pk_bf16(x[2], x[3]), pk_bf16(y[0], y[1]), pk_bf16(y[2], y[3])};
      __builtin_amdgcn_raw_buffer_store_b128(o, r_bw, (t < 2 || c16 < 8) ? (nb0 + tile_node(t, c16)) * 256 + u * 64 + g4 * 16 : OOB, 0, SC1);
    }
    STAMP(15);
    await(lds, 4, 4);
    dma4(s_we, wv0, wv1, 0, lb + L_W1 + u * 4096);
    dma4(s_we, wv0, wv1, 16384, lb + L_W1 + 16384 + u * 4096);
    await(lds, 3, 4);
    dma4(s_wh, wv0, wv1, 0, lb + L_W0 + u * 4096);
    dma4(s_wh, wv0, wv1, 16384, lb + L_W0 + 16384 + u * 4096);
    {
      const float wd = bf2f(wdv) * LOG2E;
      const u32 wdh = pk_bf16(wd, 0.f);
      const u32 wdp = wdh | (pk_bf16(wd - bf2f(wdh), 0.f) << 16), wap = pk_bf16(bf2f(wav) * LOG2E, 0.f);
#pragma unroll
      for (int i = 0; i < 4; i++) {
        char* kb = lds + L_KB + (i * 256 + tid) * 16;
        *(u32*)(kb + 12) = wdp;
        *(u16*)(kb + 10) = (u16)wap;
      }
      if (tid < D) *(u16*)(lds + L_WX + tid * 2) = (u16)wxv;
    }
    asm volatile("s_waitcnt vmcnt(16)" ::: "memory");
    if (lane == 0 && __hip_atomic_fetch_add((u32*)(lds + L_SY) + 5, 1u, __ATOMIC_RELAXED, __HIP_MEMORY_SCOPE_WORKGROUP) == 3)
      __hip_atomic_store((gu64*)(slot + WS_FL) + b, ((((u64)ep_hi << 32) | ep_lo) + 1) & 0xfffffffffffull, RLX_AGENT);
    STAMP(1);
    asm volatile("s_waitcnt vmcnt(0)" ::: "memory");
  } else {
    const int t1 = tid - 256;
    const u32x4 z = {0, 0, 0, 0};
#pragma unroll
    for (int i = 0; i < 10; i++) if (i < 9 || L_SM + i * 4096 + t1 * 16 < L_POS) *(u32x4*)(lds + L_SM + i * 4096 + t1 * 16) = z;
    setup();
    asm volatile("s_waitcnt vmcnt(7)" : "+v"(ed[0]), "+v"(ed[1]), "+v"(ed[2]) :: "memory");
    u32 rank[3], ebk[3];
#pragma unroll
    for (int i = 0; i < 3; i++) {
      ebk[i] = ed[i] / NPB;
      rank[i] = 0;
      if (t1 + 256 * i < ne) rank[i] = atomicAdd((u32*)(lds + L_BC) + ebk[i], 1u);
    }
    arrive(lb, 0);
    asm volatile("s_waitcnt vmcnt(0)\n\tv_readfirstlane_b32 %0, %8\n\tv_readfirstlane_b32 %1, %9"
                 : "=s"(ep_lo), "=s"(ep_hi), "+v"(es[0]), "+v"(es[1]), "+v"(es[2]), "+v"(ee[0]), "+v"(ee[1]), "+v"(ee[2])
                 : "v"(epoch[0]), "v"(epoch[1]) : "memory");
    ep_lo = RFL(ep_lo); ep_hi = RFL(ep_hi);
    char* const slot = ws + WS_SLOT0 + (size_t)(ep_lo & 3u) * WS_SLOT;
    const u64 tag = ((((u64)ep_hi << 32) | ep_lo) + 1) & 0xfffffffffffull;
    STAMP(14);
    await(lds, 0, 8);
    STAMP(13);
    const u32x4 c4 = *(const u32x4*)(lds + L_BC + lane * 16);
    const u32 cown = *(const u32*)(lds + L_BC + t1 * 4);
    u32x2 pd[3], ps[3];
#pragma unroll
    for (int i = 0; i < 3; i++) {
      const u32* qd = (const u32*)(lds + L_POS + ((ed[i] * 6) & ~3u));
      const u32* qs = (const u32*)(lds + L_POS + ((es[i] * 6) & ~3u));
      pd[i] = u32x2{qd[0], qd[1]}; ps[i] = u32x2{qs[0], qs[1]};
    }
    const u32 pown = *(const u16*)(lds + L_POS + min((nb0 * 3 + t1) * 2, MAXPOS - 2));
    const u32 tot = c4[0] + c4[1] + c4[2] + c4[3];
    const u32 ex = scan64(tot) - tot;
    u32* mine = (u32*)(lds + L_LS + u * 1024);
    *(u32x4*)(mine + lane * 4) = u32x4{ex, ex + c4[0], ex + c4[0] + c4[1], ex + c4[0] + c4[1] + c4[2]};
    __builtin_amdgcn_wave_barrier();
    u32 at[3];
#pragma unroll
    for (int i = 0; i < 3; i++) at[i] = L_RS + (mine[ebk[i]] + rank[i]) * 16;
    const u32 gword = (mine[t1] << 10) | cown;
#pragma unroll
    for (int i = 0; i < 3; i++) {
      const u32 shd = (ed[i] & 1u) * 16, shs = (es[i] & 1u) * 16;
      const u32 dxy = __builtin_amdgcn_alignbit(pd[i][1], pd[i][0], shd), sxy = __builtin_amdgcn_alignbit(ps[i][1], ps[i][0], shs);
      const u32 ex2 = pk_bf16(0.f, bf2f(dxy) - bf2f(sxy));
      const u32 yz = pk_bf16(bfhi(dxy) - bfhi(sxy), bf2f(pd[i][1] >> shd) - bf2f(ps[i][1] >> shs));
      u32x4 rc = {es[i] | ((ed[i] - ebk[i] * NPB) << 14) | ((u32)tag << 20), (u32)(tag >> 12), ee[i] | ex2, yz};
      PIN(rc);
      if (t1 + 256 * i < ne) *(u32x4*)(lds + at[i]) = rc;
    }
    if (t1 < NPB * 3) *(u16*)(lds + L_PD + t1 * 2) = (u16)pown;
    arrive(lb, 2);
    await(lds, 2, 4);
    const __amdgpu_buffer_rsrc_t r_rw = rsrc(slot + WS_REC + (size_t)b * CH * 16, CH * 16);
    u32x4 rv[3];
#pragma unroll
    for (int i = 0; i < 3; i++) rv[i] = *(const u32x4*)(lds + L_RS + (t1 + 256 * i) * 16);
#pragma unroll
    for (int i = 0; i < 3; i++) __builtin_amdgcn_raw_buffer_store_b128(rv[i], r_rw, t1 * 16, i * 4096, SC1);
    arrive(lb, 4);
    __hip_atomic_store((gu64*)(slot + WS_GR) + (size_t)b * MAXG + t1, (tag << 20) | gword, RLX_AGENT);
    STAMP(2);
#pragma unroll
    for (int i = 0; i < 5; i++) { const int o = L_POS + i * 4096 + t1 * 16; if (i < 4 || o < L_SRC) *(u32x4*)(lds + o) = z; }
    *(u32x4*)(lds + L_SRC + t1 * 16) = u32x4{OOB, OOB, OOB, OOB};
    if (t1 < 16) *(u32*)(lds + L_SRC + 4096 + t1 * 4) = OOB;
    await(lds, 1, 4);
#pragma unroll
    for (int s = 0; s < 4; s++)
#pragma unroll
      for (int t = 0; t < 3; t++) hb[t][s] = h_frag(lds, t, s, lane);
  }
  STAMP(3);
  __syncthreads();
  ep_lo = RFL(ep_lo); ep_hi = RFL(ep_hi);
  const u64 ep = ((u64)ep_hi << 32) | ep_lo, tag = (ep + 1) & 0xfffffffffffull;
  char* const slot = ws + WS_SLOT0 + (size_t)(ep_lo & 3u) * WS_SLOT;
  const __amdgpu_buffer_rsrc_t r_bt = rsrc(slot + WS_BT, N * 256), r_rec = rsrc(slot + WS_REC, G * CH * 16);
  const u32 tag0 = (u32)tag & 0xfffu, tag1 = (u32)(tag >> 12);
  f32x4 cp[3];
  u32 pw[4] = {0, 0, 0, 0};
  {
    const int cs = wv * 32 + (lane & 31), csc = cs < G ? cs : G - 1;
    gu64* const gp = (gu64*)(slot + WS_GR) + (size_t)csc * MAXG + bk;
    gu64* const fp = (gu64*)(slot + WS_FL) + csc;
    u64 gv = __hip_atomic_load(gp, RLX_AGENT);
    SB();
    {
      f32x4 acc[3] = {f32x4{0, 0, 0, 0}, f32x4{0, 0, 0, 0}, f32x4{0, 0, 0, 0}};
#pragma unroll
      for (int s = 0; s < 4; s++) {
        const u32x4 fw = tr_frag(lds + L_W1, s, lane, 2 * wv + ((lane & 3) >> 1), lane & 1);
#pragma unroll
        for (int t = 0; t < 3; t++) acc[t] = mfma16(hb[t][s], fw, acc[t]);
      }
      char* kb = lds + L_KB + (16 * wv + c16) * 16;
#pragma unroll
      for (int t = 0; t < 2; t++) {
        const f32x4 a = acc[t] * LOG2E;
        *(u32x2*)(kb + (4 * t + g4) * 2048) = u32x2{pk_bf16(a[0], a[1]), pk_bf16(a[2], a[3])};
      }
      if (g4 < 2) {
#pragma unroll
        for (int i = 0; i < 4; i++) *(u16*)(kb + (4 * g4 + i) * 2048 + 8) = (u16)pk_bf16(acc[2][i] * LOG2E, 0.f);
      }
    }
    SB();
    STAMP(4);
    for (u32 spins = 0; !__all((gv >> 20) == tag) && spins < (1u << 20); spins++) {
      __builtin_amdgcn_s_sleep(4);
      if ((gv >> 20) != tag) gv = __hip_atomic_load(gp, RLX_AGENT);
    }
    STAMP(5);
    const bool on = lane < 32 && cs < G;
    const u32 cnt = on ? ((u32)gv & 1023u) : 0u, st = ((u32)gv >> 10) & 1023u;
    const u32 inc = scan64(cnt);
    const u32 total = __builtin_amdgcn_readlane(inc, 63);
    u32* list = (u32*)(lds + L_LS + wv * 768);
    const u32 base = (cs * CH + st) * 16, p0 = inc - cnt;
    const u32 lim = min(cnt, p0 < 128 ? 128 - p0 : 0u);
#pragma unroll
    for (u32 k = 0; k < 6; k++) list[k < lim ? p0 + k : 128 + lane] = base + k * 16;
    if (__any(lim > 6)) {
#pragma unroll 1
      for (u32 k = 6; k < lim; k++) list[p0 + k] = base + k * 16;
    }
    __builtin_amdgcn_wave_barrier();
    {
      const u32 a0 = list[lane], a1 = list[64 + lane];
      const bool v0 = lane < total, v1 = 64 + lane < total;
      u32x4 r0 = __builtin_amdgcn_raw_buffer_load_b128(r_rec, v0 ? a0 : OOB, 0, SC1);
      u32x4 r1 = __builtin_amdgcn_raw_buffer_load_b128(r_rec, v1 ? a1 : OOB, 0, SC1);
      u64 fv = __hip_atomic_load(fp, RLX_AGENT);
      if (on) *(u32*)(lds + L_BC + cs * 4) = (u32)gv & 0xfffffu;
      SB();
#pragma unroll
      for (int t = 0; t < 3; t++) cp[t] = f32x4{0, 0, 0, 0};
#pragma unroll
      for (int s = 0; s < 4; s++) {
        const u32x4 f1 = tr_frag(lds + L_W0, s, lane, 2 * wv + ((lane & 3) >> 1), lane & 1);
#pragma unroll
        for (int t = 0; t < 3; t++) cp[t] = mfma16(f1, hb[t][s], cp[t]);
      }
#pragma unroll
      for (int t = 0; t < 3; t++) { cp[t] *= LOG2E; PIN(cp[t]); }
      SB();
      STAMP(6);
      r0 = settle(r0, v0, r_rec, a0, tag0, tag1);
      r1 = settle(r1, v1, r_rec, a1, tag0, tag1);
      STAMP(7);
      u32 k0 = 0, k1 = 0;
      if (v0) k0 = take_rank(lds, r0);
      if (v1) k1 = take_rank(lds, r1);
      for (u32 spins = 0; !__all(fv == tag) && spins < (1u << 20); spins++) {
        __builtin_amdgcn_s_sleep(4);
        fv = __hip_atomic_load(fp, RLX_AGENT);
      }
      pw[0] = __builtin_amdgcn_raw_buffer_load_b32(r_bt, v0 && k0 < PFR ? (r0[0] & 0x3fffu) << 8 : OOB, 0, SC1);
      pw[1] = __builtin_amdgcn_raw_buffer_load_b32(r_bt, v0 && k0 < PFR ? ((r0[0] & 0x3fffu) << 8) + 128 : OOB, 0, SC1);
      pw[2] = __builtin_amdgcn_raw_buffer_load_b32(r_bt, v1 && k1 < PFR ? (r1[0] & 0x3fffu) << 8 : OOB, 0, SC1);
      pw[3] = __builtin_amdgcn_raw_buffer_load_b32(r_bt, v1 && k1 < PFR ? ((r1[0] & 0x3fffu) << 8) + 128 : OOB, 0, SC1);
      SB();
      if (v0) put_rec(lds, r0, k0);
      if (v1) put_rec(lds, r1, k1);
    }
    if (__builtin_expect(total > 128, 0)) {
#pragma unroll 1
      for (u32 done = 128; done < total; done += 64) {
        if (__hip_atomic_load((u32*)(lds + L_CNT + 32), __ATOMIC_RELAXED, __HIP_MEMORY_SCOPE_WORKGROUP)) break;
        __builtin_amdgcn_wave_barrier();
#pragma unroll 1
        for (u32 k = p0 < done ? done - p0 : 0u; k < cnt && p0 + k < done + 64; k++) list[p0 + k - done] = base + k * 16;
        __builtin_amdgcn_wave_barrier();
        const bool v0 = done + lane < total;
        const u32 a0 = list[lane];
        u32x4 r0 = __builtin_amdgcn_raw_buffer_load_b128(r_rec, v0 ? a0 : OOB, 0, SC1);
        r0 = settle(r0, v0, r_rec, a0, tag0, tag1);
        if (v0) put_rec(lds, r0, take_rank(lds, r0));
      }
    }
  }
  STAMP(9);
  __syncthreads();
  STAMP(8);
  const u32* wcnt = (const u32*)(lds + L_CNT);
  const u32 ci = wcnt[lane >> 3], cj = wcnt[lane & 7];
  const int ovf = RFL(wcnt[8]);
  auto load_wh2 = [&]() {
    const u32x4 s_wh = srd(w_h, 65536);
    dma4(s_wh, wv0 + (wv >> 2) * 16384, wv1 + (wv >> 2) * 16384, 32768, lb + L_W0 + wv * 4096);
  };
  u32x4 v[8], wxf[8];
  int win = 0;
  if (__builtin_expect(!ovf, 1)) {
    const u64 gt = __ballot(cj * 8 + (lane & 7) > ci * 8 + (lane >> 3));
    const u32 rk = __builtin_popcount((u32)(gt >> (lane & 56)) & 0xffu);
    const int wl = __builtin_ctzll(__ballot(rk == (u32)(wv < 4 ? wv : 11 - wv)));
    win = wl >> 3;
    const int cntw = __builtin_amdgcn_readlane(ci, wl);
    const int nsteps = (cntw + 15) >> 4;
    const u32 lo = r * 4;
    u32 srca = L_SRC + win * CAPW * 4 + hh * 32;
    u32 GA[8], GB[8], off[8];
    const bool cfon = rp == hh;
    u32 cfa = cfon ? (L_CF + (win * CAPW + 8 * rh + rj) * 16) : L_ZERO;
    const u32 cfs = cfon ? 256u : 0u;
    u32 sma = L_SM + (win * CAPW + 8 * hh + (c16 >> 2)) * 40 + (g4 & 1) * 32 + (lane & 3) * 8;
    u32x4 cf, kbA, kbB, sf = {0, 0, 0, 0}, mB0 = {0, 0, 0, 0}, mB1 = {0, 0, 0, 0}, mA0, mA1;
    f32x16 yA, yB;
    {
      const u32x4 i0 = *(const u32x4*)(lds + srca), i1 = *(const u32x4*)(lds + srca + 16);
      const u32x4 i2 = *(const u32x4*)(lds + srca + 64), i3 = *(const u32x4*)(lds + srca + 80);
      cf = *(const u32x4*)(lds + cfa);
      kbA = *(const u32x4*)(lds + L_KB + win * 2048 + (2 * r + hh) * 16);
      kbB = *(const u32x4*)(lds + L_KB + win * 2048 + (64 + 2 * r + hh) * 16);
      srca += 128;
      SB();
#pragma unroll
      for (int j = 0; j < 4; j++) { off[j] = i0[j] + lo; off[4 + j] = i1[j] + lo; }
#pragma unroll
      for (int j = 0; j < 8; j++) GA[j] = __builtin_amdgcn_raw_buffer_load_b32(r_bt, off[j], 0, SC1);
      SB();
#pragma unroll
      for (int j = 0; j < 8; j++) GB[j] = __builtin_amdgcn_raw_buffer_load_b32(r_bt, off[j] + 128, 0, SC1);
      SB();
      asm volatile("" :: "v"(pw[0]), "v"(pw[1]), "v"(pw[2]), "v"(pw[3]));
      yA = mfma32(cf, kbA, zero);
#pragma unroll
      for (int j = 0; j < 4; j++) { off[j] = i2[j] + lo; off[4 + j] = i3[j] + lo; }
      yA = mfma32(sel0, u32x4{GA[0], GA[1], GA[2], GA[3]}, yA);
      yA = mfma32(sel1, u32x4{GA[4], GA[5], GA[6], GA[7]}, yA);
      PIN(yA);
      SB();
#pragma unroll
      for (int j = 0; j < 8; j++) GA[j] = __builtin_amdgcn_raw_buffer_load_b32(r_bt, off[j], 0, SC1);
      SB();
    }
#define MF(d, a, bb, c) do { SB(); d = mfma32(a, bb, c); PIN(d); SB(); } while (0)
#define SA(y, i, j) do { p_ = f32x2{__builtin_amdgcn_exp2f(-y[i]), __builtin_amdgcn_exp2f(-y[(i) + 1])}; \
                         q_ = f32x2{__builtin_amdgcn_exp2f(-y[j]), __builtin_amdgcn_exp2f(-y[(j) + 1])}; SB(); p_ += 1.0f; q_ += 1.0f; SB(); } while (0)
#define SBB(y, i, j, o0, o1) do { p_ = f32x2{__builtin_amdgcn_rcpf(p_[0]), __builtin_amdgcn_rcpf(p_[1])}; \
                         q_ = f32x2{__builtin_amdgcn_rcpf(q_[0]), __builtin_amdgcn_rcpf(q_[1])}; SB(); \
                         p_ *= f32x2{y[i], y[(i) + 1]}; q_ *= f32x2{y[j], y[(j) + 1]}; SB(); \
                         u32 t0_ = pk_bf16(p_[0], p_[1]), t1_ = pk_bf16(q_[0], q_[1]); \
                         asm volatile("" : "+v"(t0_), "+v"(t1_)); o0 = t0_; o1 = t1_; SB(); } while (0)
#define S4(y, i, j, o0, o1) do { SA(y, i, j); SBB(y, i, j, o0, o1); } while (0)
#define LD2(Gx, j, add) do { Gx[j] = __builtin_amdgcn_raw_buffer_load_b32(r_bt, off[j] + (add), 0, SC1); \
                             Gx[(j) + 1] = __builtin_amdgcn_raw_buffer_load_b32(r_bt, off[(j) + 1] + (add), 0, SC1); SB(); } while (0)
    f32x2 p_, q_;
    for (int s = 1; s < nsteps; s++) {
      { const int left = nsteps - s; if (left >= 4) __builtin_amdgcn_s_setprio(3); else if (left == 3) __builtin_amdgcn_s_setprio(2); else if (left == 2) __builtin_amdgcn_s_setprio(1); else __builtin_amdgcn_s_setprio(0); }
      MF(yB, cf, kbB, zero);
      S4(yA, 0, 2, mA0[0], mA0[1]);
      MF(yB, sel0, (u32x4{GB[0], GB[1], GB[2], GB[3]}), yB);
      cfa += cfs;
      cf = *(const u32x4*)(lds + cfa);
      SA(yA, 4, 6);
      LD2(GB, 0, 128);
      SBB(yA, 4, 6, mA0[2], mA0[3]);
      LD2(GB, 2, 128);
      MF(yB, sel1, (u32x4{GB[4], GB[5], GB[6], GB[7]}), yB);
      SA(yA, 8, 10);
      LD2(GB, 4, 128);
      MF(aB0, mB0, sf, aB0);
      SBB(yA, 8, 10, mA1[0], mA1[1]);
      LD2(GB, 6, 128);
      const u32x4 i0 = *(const u32x4*)(lds + srca), i1 = *(const u32x4*)(lds + srca + 16);
      srca += 64;
      SA(yA, 12, 14);
      MF(aB1, mB1, sf, aB1);
      sf = s_frag(lds + sma);
      sma += 640;
      SBB(yA, 12, 14, mA1[2], mA1[3]);
      MF(yA, cf, kbA, zero);
      S4(yB, 0, 2, mB0[0], mB0[1]);
      MF(yA, sel0, (u32x4{GA[0], GA[1], GA[2], GA[3]}), yA);
#pragma unroll
      for (int j = 0; j < 4; j++) { off[j] = i0[j] + lo; off[4 + j] = i1[j] + lo; }
      SA(yB, 4, 6);
      LD2(GA, 0, 0);
      SBB(yB, 4, 6, mB0[2], mB0[3]);
      LD2(GA, 2, 0);
      MF(yA, sel1, (u32x4{GA[4], GA[5], GA[6], GA[7]}), yA);
      SA(yB, 8, 10);
      LD2(GA, 4, 0);
      MF(aA0, mA0, sf, aA0);
      SBB(yB, 8, 10, mB1[0], mB1[1]);
      LD2(GA, 6, 0);
      SA(yB, 12, 14);
      MF(aA1, mA1, sf, aA1);
      SBB(yB, 12, 14, mB1[2], mB1[3]);
    }
    {
      const int rem = cntw - 16 * (nsteps - 1);
      load_wh2();
      MF(yB, cf, kbB, zero);
      mA0 = u32x4{0, 0, 0, 0}; mA1 = u32x4{0, 0, 0, 0};
      S4(yA, 0, 8, mA0[0], mA1[0]);
      MF(yB, sel0, (u32x4{GB[0], GB[1], GB[2], GB[3]}), yB);
      if (rem > 4) S4(yA, 2, 10, mA0[1], mA1[1]);
      MF(yB, sel1, (u32x4{GB[4], GB[5], GB[6], GB[7]}), yB);
      if (rem > 8) S4(yA, 4, 12, mA0[2], mA1[2]);
      MF(aB0, mB0, sf, aB0);
      if (rem > 12) S4(yA, 6, 14, mA0[3], mA1[3]);
      MF(aB1, mB1, sf, aB1);
      sf = s_frag(lds + sma);
      mB0 = u32x4{0, 0, 0, 0}; mB1 = u32x4{0, 0, 0, 0};
      S4(yB, 0, 8, mB0[0], mB1[0]);
      MF(aA0, mA0, sf, aA0);
      if (rem > 4) S4(yB, 2, 10, mB0[1], mB1[1]);
      MF(aA1, mA1, sf, aA1);
      if (rem > 8) S4(yB, 4, 12, mB0[2], mB1[2]);
      if (rem > 12) S4(yB, 6, 14, mB0[3], mB1[3]);
      MF(aB0, mB0, sf, aB0);
      MF(aB1, mB1, sf, aB1);
    }
#pragma unroll
    for (int q8 = 0; q8 < 8; q8++) {
      const f32x16& a0 = q8 < 4 ? aA0 : aB0;
      const f32x16& a1 = q8 < 4 ? aA1 : aB1;
#pragma unroll
      for (int q = 0; q < 4; q++) v[q8][q] = pk_bf16(a0[4 * (q8 & 3) + q], a1[4 * (q8 & 3) + q]);
    }
    if (r < 20 && (r & 3) == 0) {
      char* mi = lds + mi_row(r * 2 + win) + 16 * hh;
#pragma unroll
      for (int q8 = 0; q8 < 8; q8++) *(u32x4*)(mi + 32 * q8) = v[q8];
    }
  } else {
    float* macc = (float*)(lds + L_SM);
    float* pacc = (float*)(lds + L_SM + NPB * D * 4);
    load_wh2();
    asm volatile("" :: "v"(pw[0]), "v"(pw[1]), "v"(pw[2]), "v"(pw[3]));
    __syncthreads();
    for (int i = tid; i < NPB * D + NPB * 4; i += NT) macc[i] = 0.f;
    __syncthreads();
    const u32 wxp = *(const u32*)(lds + L_WX + lane * 4);
    const float wx0 = bf2f(wxp), wx1 = bfhi(wxp);
    const u32x4 kc0 = *(const u32x4*)(lds + L_KB + (2 * lane) * 16), kc1 = *(const u32x4*)(lds + L_KB + (2 * lane + 1) * 16);
    const float wd0 = bf2f(kc0[3]) + bfhi(kc0[3]), wd1 = bf2f(kc1[3]) + bfhi(kc1[3]);
    const float wa0 = bfhi(kc0[2]), wa1 = bfhi(kc1[2]);
    u32 rot = 0;
    for (int c = 0; c < G; c++) {
      const u32 oo = RFL(*(const u32*)(lds + L_BC + c * 4));
      const u32 s0 = oo >> 10, n = oo & 1023u;
      for (u32 k = (wv - rot) & 7u; k < n; k += 8) {
        u32x4 rr = __builtin_amdgcn_raw_buffer_load_b128(r_rec, (c * CH + s0 + k) * 16, 0, SC1);
        rr = settle(rr, true, r_rec, (c * CH + s0 + k) * 16, tag0, tag1);
        const u32 nl = (rr[0] >> 14) & 0x3fu, sl = nl >> 3, wn = nl & 7u;
        const u32 bv = __builtin_amdgcn_raw_buffer_load_b32(r_bt, ((rr[0] & 0x3fffu) << 8) + lane * 4, 0, SC1);
        const float d2 = bfhi(dist2(rr[2], rr[3])), eav = bf2f(rr[2]);
        const float a0 = bf2f(*(const u16*)(lds + L_KB + wn * 2048 + (2 * lane) * 16 + sl * 2));
        const float a1 = bf2f(*(const u16*)(lds + L_KB + wn * 2048 + (2 * lane + 1) * 16 + sl * 2));
        const float y0 = a0 + bf2f(bv) + d2 * wd0 + eav * wa0, y1 = a1 + bfhi(bv) + d2 * wd1 + eav * wa1;
        const u32 mp = silu2(y0, y1);
        const float m0 = bf2f(mp), m1 = bfhi(mp);
        atomicAdd(&macc[nl * D + 2 * lane], m0);
        atomicAdd(&macc[nl * D + 2 * lane + 1], m1);
        float csv = m0 * wx0 + m1 * wx1;
#pragma unroll
        for (int o = 32; o > 0; o >>= 1) csv += __shfl_xor(csv, o, 64);
        if (lane < 3) atomicAdd(&pacc[nl * 4 + lane], csv * (lane == 0 ? bfhi(rr[2]) : lane == 1 ? bf2f(rr[3]) : bfhi(rr[3])));
      }
      rot += n;
    }
    __syncthreads();
    for (int i = tid; i < NPB * D / 2; i += NT) {
      const int n = i >> 6, f = (i & 63) * 2;
      *(u32*)(lds + mi_row(n) + f * 2) = pk_bf16(macc[n * D + f], macc[n * D + f + 1]);
    }
    if (tid < NPB * 3) {
      const int n = tid / 3, c = tid - n * 3;
      if (nb0 + n < N) out[(size_t)N * D + (size_t)nb0 * 3 + tid] = (u16)pk_bf16(bf2f(*(const u16*)(lds + L_PD + tid * 2)) + LN2 * pacc[n * 4 + c], 0.f);
    }
#pragma unroll
    for (int q8 = 0; q8 < 8; q8++) v[q8] = u32x4{0, 0, 0, 0};
  }
  u32x2 hr[3];
  u32x4 f2[4];
#pragma unroll
  for (int t = 0; t < 3; t++) {
    const int n = tile_node(t, c16);
    hr[t] = *(const u32x2*)(lds + L_HS + n * 256 + 16 * ((2 * wv + (g4 >> 1)) ^ hswz(n)) + 8 * (g4 & 1));
  }
#pragma unroll
  for (int q8 = 0; q8 < 8; q8++) wxf[q8] = *(const u32x4*)(lds + L_WX + (16 * q8 + 8 * hh) * 2);
  const int kind = lane & 3, pnl = (lane >> 2) * 8 + win;
  const bool pon = kind != 0 && lane < 20 && !ovf;
  const u32 p0 = *(const u16*)(lds + L_PD + (pon ? pnl * 6 + kind * 2 - 2 : 0));
  const int mia = mi_row(tile_node(0, c16)) + g4 * 16, mib = mi_row(tile_node(2, c16)) + g4 * 16;
  asm volatile("s_waitcnt vmcnt(0)" ::: "memory");
  STAMP(10);
  __syncthreads();
  {
    u32x4 mb[3][4];
#pragma unroll
    for (int s = 0; s < 4; s++) {
      f2[s] = tr_frag(lds + L_W0, s, lane, 2 * wv + ((lane & 3) >> 1), lane & 1);
      mb[0][s] = *(const u32x4*)(lds + mia + s * 64);
      mb[1][s] = *(const u32x4*)(lds + mia + 8192 + s * 64);
      mb[2][s] = *(const u32x4*)(lds + mib + s * 64);
    }
    SB();
#pragma unroll
    for (int s = 0; s < 4; s++)
#pragma unroll
      for (int t = 0; t < 3; t++) cp[t] = mfma16(f2[s], mb[t][s], cp[t]);
    f32x16 z = zero, z1 = zero;
#pragma unroll
    for (int t = 0; t < 3; t++) {
      const f32x4 y = cp[t];
      f32x4 e;
#pragma unroll
      for (int i = 0; i < 4; i++) e[i] = __builtin_amdgcn_exp2f(-y[i]);
      SB();
      if (t & 1) { z1 = mfma32(wxf[3 * t], v[3 * t], z1); PIN(z1); } else { z = mfma32(wxf[3 * t], v[3 * t], z); PIN(z); }
      SB();
      e += 1.0f;
#pragma unroll
      for (int i = 0; i < 4; i++) e[i] = __builtin_amdgcn_rcpf(e[i]);
      SB();
      if (t & 1) { z = mfma32(wxf[3 * t + 1], v[3 * t + 1], z); PIN(z); } else { z1 = mfma32(wxf[3 * t + 1], v[3 * t + 1], z1); PIN(z1); }
      SB();
      const f32x4 o = f32x4{bf2f(hr[t][0]), bfhi(hr[t][0]), bf2f(hr[t][1]), bfhi(hr[t][1])} + (y * LN2) * e;
      *(u32x2*)(lds + L_SM + tile_node(t, c16) * 272 + wv * 32 + g4 * 8) = u32x2{pk_bf16(o[0], o[1]), pk_bf16(o[2], o[3])};
      SB();
      if (t == 0) { z = mfma32(wxf[2], v[2], z); PIN(z); }
      if (t == 1) { z1 = mfma32(wxf[5], v[5], z1); PIN(z1); }
      SB();
    }
    __syncthreads();
    const __amdgpu_buffer_rsrc_t r_out = rsrc(out, N * 262);
    const u32x4 o0 = *(const u32x4*)(lds + L_SM + (tid >> 4) * 272 + (tid & 15) * 16);
    const u32x4 o1 = *(const u32x4*)(lds + L_SM + (32 + ((tid >> 4) & 7)) * 272 + (tid & 15) * 16);
    const u32 oo = nb0 * 256 + tid * 16;
    __builtin_amdgcn_raw_buffer_store_b128(o0, r_out, oo < N * 256 ? oo : OOB, 0, 0);
    __builtin_amdgcn_raw_buffer_store_b128(o1, r_out, tid < 128 && oo + 8192 < N * 256 ? oo + 8192 : OOB, 0, 0);
    __builtin_amdgcn_raw_buffer_store_b16((u16)pk_bf16(bf2f(p0) + LN2 * (z[0] + z1[0]), 0.f), r_out,
                                          pon ? N * 256 + ((nb0 + pnl) * 3 + kind - 1) * 2 : OOB, 0, 0);
  }
  if (b == 0 && tid == 0) __hip_atomic_store((gu64*)(ws + WS_EP), ep + 1, RLX_AGENT);
  STAMP(11);
}

extern "C" void kernel_launch(void* const* d_in, const int* in_sizes, int n_in, void* d_out, int out_size, void* d_ws,
                              size_t ws_size, hipStream_t stream) {
  const int N = in_sizes[0] / D, E = in_sizes[3];
  const int NB = (N + NPB - 1) / NPB, C = (E + CH - 1) / CH;
  const int G = NB > C ? NB : C;
  k_egnn<<<G, NT, 0, stream>>>((const u16*)d_in[0], (const u16*)d_in[1], (const u16*)d_in[2], (const int*)d_in[3],
                               (const int*)d_in[4], (const u16*)d_in[5], (const u16*)d_in[6], (const u16*)d_in[7],
                               (char*)d_ws, (u16*)d_out, N, E, G);
}
```

```cpp
#include <hip/hip_runtime.h>
#include <hip/hip_bf16.h>

typedef unsigned int u32;
typedef unsigned short u16;
typedef unsigned long long u64;
typedef u32 u32x2 __attribute__((ext_vector_type(2)));
typedef u32 u32x4 __attribute__((ext_vector_type(4)));
typedef short s16x4 __attribute__((ext_vector_type(4)));
typedef short bf16x8 __attribute__((ext_vector_type(8)));
typedef float f32x2 __attribute__((ext_vector_type(2)));
typedef float f32x4 __attribute__((ext_vector_type(4)));
typedef float f32x16 __attribute__((ext_vector_type(16)));
typedef __bf16 bf16x2_t __attribute__((ext_vector_type(2)));
typedef __attribute__((address_space(1))) u64 gu64;
typedef __attribute__((address_space(3))) s16x4 lds_s16x4;
typedef __attribute__((address_space(3))) char lds_char;
#define DI __device__ __forceinline__
#define RLX_AGENT __ATOMIC_RELAXED, __HIP_MEMORY_SCOPE_AGENT
#define SC1 16
#define SB() __builtin_amdgcn_sched_barrier(0)
#define PIN(x) asm volatile("" : "+v"(x))
#define RFL(x) __builtin_amdgcn_readfirstlane(x)

constexpr int D = 128;
constexpr int NT = 512;
constexpr int NPB = 40;
constexpr int CH = 640;
constexpr int MAXG = 256;
constexpr int CAPW = 128;
constexpr int MAXPOS = 61440;
#define PD1 100
#define PFR 16
constexpr float LOG2E = 1.4426950408889634f;
constexpr float LN2 = 0.6931471805599453f;
constexpr u32 OOB = 0xfffff000u;

constexpr size_t WS_EP = 0;
constexpr size_t WS_SLOT0 = (size_t)2 << 20, WS_SLOT = (size_t)6 << 20;
constexpr size_t WS_GR = 0;
constexpr size_t WS_BT = WS_GR + (size_t)MAXG * MAXG * 8;
constexpr size_t WS_REC = WS_BT + (size_t)MAXG * NPB * 256;
constexpr size_t WS_FL = WS_REC + (size_t)MAXG * CH * 16;
static_assert(WS_FL + MAXG * 8 <= WS_SLOT, "slot");

#define STAMP(k) do { } while (0)

DI float bf2f(u32 b) { return __builtin_bit_cast(float, b << 16); }
DI float bfhi(u32 b) { return __builtin_bit_cast(float, b & 0xffff0000u); }
DI u32 pk_bf16(float lo, float hi) {
  f32x2 v = {lo, hi};
  bf16x2_t r = __builtin_convertvector(v, bf16x2_t);
  return __builtin_bit_cast(u32, r);
}
DI f32x4 mfma16(u32x4 a, u32x4 b, f32x4 c) {
  return __builtin_amdgcn_mfma_f32_16x16x32_bf16(__builtin_bit_cast(bf16x8, a), __builtin_bit_cast(bf16x8, b), c, 0, 0, 0);
}
DI f32x16 mfma32(u32x4 a, u32x4 b, f32x16 c) {
  return __builtin_amdgcn_mfma_f32_32x32x16_bf16(__builtin_bit_cast(bf16x8, a), __builtin_bit_cast(bf16x8, b), c, 0, 0, 0);
}
DI __amdgpu_buffer_rsrc_t rsrc(const void* p, u32 bytes) { return __builtin_amdgcn_make_buffer_rsrc((void*)p, 0, bytes, 0x00020000); }
DI u32x4 srd(const void* p, u32 bytes) { const u64 a = (u64)p; return u32x4{(u32)a, (u32)(a >> 32) & 0xffffu, bytes, 0x00020000u}; }
DI void dma4(u32x4 sd, u32 v0, u32 v1, u32 soff, u32 ldsa) {
  u32 keep;
  asm volatile("s_mov_b32 %0, m0\n\ts_mov_b32 m0, %1\n\ts_nop 0\n\t"
               "buffer_load_dwordx4 %2, %4, %5 offen lds\n\tbuffer_load_dwordx4 %2, %4, %5 offen offset:1024 lds\n\t"
               "buffer_load_dwordx4 %3, %4, %5 offen offset:2048 lds\n\tbuffer_load_dwordx4 %3, %4, %5 offen offset:3072 lds\n\t"
               "s_mov_b32 m0, %0"
               : "=&s"(keep) : "s"(RFL(ldsa)), "v"(v0), "v"(v1), "s"(sd), "s"(soff) : "memory");
}
DI void dma2(u32x4 sd, u32 v0, u32 soff, u32 ldsa) {
  u32 keep;
  asm volatile("s_mov_b32 %0, m0\n\ts_mov_b32 m0, %1\n\ts_nop 0\n\t"
               "buffer_load_dwordx4 %2, %3, %4 offen lds\n\tbuffer_load_dwordx4 %2, %3, %4 offen offset:1024 lds\n\t"
               "s_mov_b32 m0, %0"
               : "=&s"(keep) : "s"(RFL(ldsa)), "v"(v0), "s"(sd), "s"(soff) : "memory");
}
DI u32 scan64(u32 x) {
  x += __builtin_amdgcn_update_dpp(0u, x, 0x111, 0xf, 0xf, false);
  x += __builtin_amdgcn_update_dpp(0u, x, 0x112, 0xf, 0xf, false);
  x += __builtin_amdgcn_update_dpp(0u, x, 0x114, 0xf, 0xf, false);
  x += __builtin_amdgcn_update_dpp(0u, x, 0x118, 0xf, 0xf, false);
  x += __builtin_amdgcn_update_dpp(0u, x, 0x142, 0xa, 0xf, false);
  x += __builtin_amdgcn_update_dpp(0u, x, 0x143, 0xc, 0xf, false);
  return x;
}

constexpr int L_ZERO = 0;
constexpr int L_CNT = L_ZERO + 16;
constexpr int L_BC = L_CNT + 48;
constexpr int L_SY = L_BC + 1024;
constexpr int L_SM = L_SY + 32;
constexpr int L_CF = L_SM + 8 * 8 * 640;
constexpr int L_SRC = L_CF + 8 * CAPW * 16;
constexpr int L_KB = L_SRC + 8 * CAPW * 4 + 64;
constexpr int L_W1 = L_KB + 8 * D * 16;
constexpr int L_W0 = L_W1 + 32768;
constexpr int L_HS = L_W0 + 32768;
constexpr int L_WX = L_HS + NPB * 256;
constexpr int L_PD = L_WX + 256;
constexpr int L_LS = L_PD + 256;
constexpr int L_LI = L_LS + 4096;
constexpr int L_END = L_LI + 1024;
constexpr int L_RS = L_W0 - CH * 16;
constexpr int L_POS = L_RS - MAXPOS;
static_assert(L_END <= 160 * 1024, "lds");
static_assert(L_POS >= L_SM + 36864 && L_POS <= L_CF && (L_POS % 16) == 0, "pos table");
static_assert((L_SM % 16) == 0 && (L_BC % 16) == 0 && (L_SRC % 16) == 0 && (L_KB % 16) == 0 && (L_W0 % 16) == 0 && (L_HS % 16) == 0 && (L_WX % 16) == 0 && (L_LS % 16) == 0, "align");

DI int tile_node(int t, int c) { return t < 2 ? ((c & 3) * 8 + 4 * t + (c >> 2)) : (32 + (c & 7)); }
DI int mi_row(int nl) { return L_KB + (nl & 7) * 2048 + (nl & 3) * 64 + (nl >> 3) * 272; }
DI int wswz(int k) { return ((k & 3) << 2) | ((k >> 2) & 2); }
DI int hswz(int n) { return ((n & 3) << 2) | ((n >> 3) & 3); }
DI u32x4 tr_frag(const char* img, int s, int lane, int ch, int hf) {
  const int k0 = 8 * (lane >> 4) + ((lane & 15) >> 2);
  const char* p = img + (32 * s + k0) * 256 + 16 * (ch ^ wswz(k0)) + 8 * hf;
  const s16x4 a = __builtin_amdgcn_ds_read_tr16_b64_v4i16((lds_s16x4*)p);
  const s16x4 b = __builtin_amdgcn_ds_read_tr16_b64_v4i16((lds_s16x4*)(p + 1024));
  const u32x2 x = __builtin_bit_cast(u32x2, a), y = __builtin_bit_cast(u32x2, b);
  return u32x4{x[0], x[1], y[0], y[1]};
}
DI u32x4 s_frag(const char* p) {
  const u32x2 x = __builtin_bit_cast(u32x2, __builtin_amdgcn_ds_read_tr16_b64_v4i16((lds_s16x4*)p));
  const u32x2 y = __builtin_bit_cast(u32x2, __builtin_amdgcn_ds_read_tr16_b64_v4i16((lds_s16x4*)(p + 160)));
  return u32x4{x[0], x[1], y[0], y[1]};
}
DI u32x4 h_frag(const char* lds, int t, int s, int lane) {
  const int n = tile_node(t, lane & 15);
  return *(const u32x4*)(lds + L_HS + n * 256 + 16 * ((4 * s + (lane >> 4)) ^ hswz(n)));
}
DI u32 dist2(u32 w2, u32 w3) {
  const float dx = bfhi(w2), dy = bf2f(w3), dz = bfhi(w3);
  const u32 q0 = pk_bf16(dx * dx, dy * dy), q1 = pk_bf16(dz * dz, 0.f);
  const float s2 = bf2f(q0) + bfhi(q0) + bf2f(q1);
  return pk_bf16(s2, s2);
}
DI u32 take_rank(char* lds, u32x4 rc) { return atomicAdd((u32*)(lds + L_CNT) + ((rc[0] >> 14) & 7u), 1u); }
DI void put_rec(char* lds, u32x4 rc, u32 rank) {
  const u32 win = (rc[0] >> 14) & 7u, slot = (rc[0] >> 17) & 7u;
  const u64 oh = 0x3f80ull << (slot * 16);
  const u32x4 cf = {slot < 4 ? (u32)oh : 0u, slot < 4 ? (u32)(oh >> 32) : 0u, (slot == 4 ? 0x3f80u : 0u) | (rc[2] << 16), dist2(rc[2], rc[3])};
  if (rank < CAPW) {
    const u32 k = ((rank & 1u) << 3) + ((rank & 15u) >> 1), sp = win * CAPW + (rank & ~15u) + k;
    *(u32*)(lds + L_SRC + sp * 4) = (rc[0] & 0x3fffu) << 8;
    *(u32x4*)(lds + L_CF + sp * 16) = cf;
    *(u32x2*)(lds + L_SM + sp * 40 + slot * 8) = u32x2{0x3f80u | (rc[2] & 0xffff0000u), rc[3]};
  } else *(u32*)(lds + L_CNT + 32) = 1;
}
DI u32x4 settle(u32x4 rc, bool on, __amdgpu_buffer_rsrc_t r, u32 off, u32 tag0, u32 tag1) {
  for (u32 spins = 0; __any(on && ((rc[0] >> 20) != tag0 || rc[1] != tag1)) && spins < (1u << 20); spins++) {
    __builtin_amdgcn_s_sleep(4);
    if (on && ((rc[0] >> 20) != tag0 || rc[1] != tag1)) rc = __builtin_amdgcn_raw_buffer_load_b128(r, off, 0, SC1);
  }
  return rc;
}
DI void arrive(u32 lb, int i) {
  asm volatile("s_mov_b64 exec, 1\n\tds_add_u32 %0, %1 offset:%2\n\ts_mov_b64 exec, -1" :: "v"(lb), "v"(1u), "i"(L_SY + 4 * i) : "memory");
}
DI void await(char* lds, int i, u32 n) {
  asm volatile("" ::: "memory");
  while ((u32)RFL(__hip_atomic_load((u32*)(lds + L_SY) + i, __ATOMIC_RELAXED, __HIP_MEMORY_SCOPE_WORKGROUP)) < n) { }
  asm volatile("" ::: "memory");
}
DI u32 silu2(float a, float b) {
  f32x2 yy = {a, b};
  f32x2 e = {__builtin_amdgcn_exp2f(-a), __builtin_amdgcn_exp2f(-b)};
  e += 1.0f;
  f32x2 rc = {__builtin_amdgcn_rcpf(e[0]), __builtin_amdgcn_rcpf(e[1])};
  yy *= rc;
  return pk_bf16(yy[0], yy[1]);
}

__global__ __launch_bounds__(NT, 2) void k_egnn(const u16* __restrict__ h, const u16* __restrict__ pos,
                                                const u16* __restrict__ ea, const int* __restrict__ src,
                                                const int* __restrict__ dst, const u16* __restrict__ w_e,
                                                const u16* __restrict__ w_x, const u16* __restrict__ w_h,
                                                char* __restrict__ ws, u16* __restrict__ out, int N, int E, int G) {
  __shared__ __attribute__((aligned(16))) char lds[L_END];
  const int tid = threadIdx.x, lane = tid & 63, b = blockIdx.x;
  const int wv = RFL(tid >> 6);
  const int u = wv & 3, c16 = lane & 15, g4 = lane >> 4;
  const bool role1 = wv >= 4;
  const u32 lb = (u32)(size_t)(lds_char*)lds;
  if (tid < L_SM / 4) *(u32*)(lds + tid * 4) = 0;
  int bk;
  { const int q = G >> 3, r = G & 7, x = b & 7, i = b >> 3;
    bk = (x < r ? x * (q + 1) : r * (q + 1) + (x - r) * q) + i; }
  const int nb0 = bk * NPB;
  const int ne = min(CH, E - b * CH);
  u32 ed[3], es[3], ee[3], pf1, pf2;
  u32x2 epoch;
  const u32x4 s_we = srd(w_e, 258 * 256), s_wh = srd(w_h, 65536);
  {
    const u32x4 s_d = srd(dst + b * CH, ne * 4), s_s = srd(src + b * CH, ne * 4), s_e = srd(ea + b * CH, ne * 2), s_wx = srd(w_x, 256);
    const u32 xi = (b >> 3) & 31;
    asm volatile("s_cmp_lt_u32 %17, 4\n\ts_cbranch_scc1 1f\n\t"
                 "buffer_load_dword %0, %12, %14, 0 offen\n\tbuffer_load_dword %1, %12, %14, 0 offen offset:1024\n\t"
                 "buffer_load_dword %2, %12, %14, 0 offen offset:2048\n\tbuffer_load_dword %3, %12, %15, 0 offen\n\t"
                 "buffer_load_dword %4, %12, %15, 0 offen offset:1024\n\tbuffer_load_dword %5, %12, %15, 0 offen offset:2048\n\t"
                 "buffer_load_ushort %6, %13, %16, 0 offen\n\tbuffer_load_ushort %7, %13, %16, 0 offen offset:512\n\t"
                 "buffer_load_ushort %8, %13, %16, 0 offen offset:1024\n\t"
                 "global_load_dwordx2 %9, %18, %19 sc1\n\t"
                 "buffer_load_dword %10, %23, %20, 0 offen\n\tbuffer_load_dword %11, %24, %21, 0 offen\n\t"
                 "s_branch 2f\n"
                 "1:\n\tglobal_load_dwordx2 %9, %18, %19 sc1\n\t"
                 "buffer_load_ushort %6, %25, %20, %26 offen\n\tbuffer_load_ushort %7, %25, %20, %26 offen offset:256\n\t"
                 "buffer_load_ushort %8, %25, %22, 0 offen\n"
                 "2:"
                 : "=&v"(ed[0]), "=&v"(ed[1]), "=&v"(ed[2]), "=&v"(es[0]), "=&v"(es[1]), "=&v"(es[2]), "=&v"(ee[0]), "=&v"(ee[1]), "=&v"(ee[2]),
                   "=&v"(epoch), "=&v"(pf1), "=&v"(pf2)
                 : "v"((tid & 255) * 4), "v"((tid & 255) * 2), "s"(s_d), "s"(s_s), "s"(s_e), "s"(wv), "v"(0u), "s"(ws + WS_EP),
                   "s"(s_we), "s"(s_wh), "s"(s_wx), "v"(tid - 256 < 8u ? (xi * 8 + tid - 256) * 128 : OOB),
                   "v"(tid - 320 < 16u ? (xi * 16 + tid - 320) * 128 : OOB), "v"((tid & 127) * 2), "s"(65536u) : "memory", "scc");
  }
  __syncthreads();
  const int r = lane & 31, hh = lane >> 5;
  const int rh = (r >> 2) & 1, rp = r >> 4, rj = (r & 3) | (((r >> 3) & 1) << 2);
  f32x16 aA0, aA1, aB0, aB1, zero;
  u32x4 sel0, sel1;
  auto setup = [&]() {
#pragma unroll
    for (int i = 0; i < 16; i++) zero[i] = 0.f;
    u32x4 z4 = {0, 0, 0, 0};
    PIN(z4);
    aA0 = mfma32(z4, z4, zero); aA1 = mfma32(z4, z4, zero); aB0 = mfma32(z4, z4, zero); aB1 = mfma32(z4, z4, zero);
    const u32 one = rh == hh ? 0x3f80u << (rp * 16) : 0u;
#pragma unroll
    for (int q = 0; q < 4; q++) { sel0[q] = (rj == q) ? one : 0u; sel1[q] = (rj == 4 + q) ? one : 0u; }
    PIN(aA0); PIN(aA1); PIN(aB0); PIN(aB1); PIN(sel0); PIN(sel1);
  };
  u32 ep_lo, ep_hi;
  u32x4 hb[3][4];
  const u32 wv0 = g4 * 256 + 16 * (c16 ^ (g4 << 2)) + u * 4096, wv1 = wv0 ^ 32;
  if (!role1) {
    const u32x4 s_pos = srd(pos, N * 6), s_h = srd(h, N * 256);
#pragma unroll
    for (int i = 0; i < 4; i++)
      if (i < 3 || u < 3) dma4(s_pos, lane * 16 + u * 4096, lane * 16 + u * 4096, i * 16384, lb + L_POS + i * 16384 + u * 4096);
    dma2(s_h, g4 * 256 + 16 * (c16 ^ ((g4 << 2) | u)) + u * 2048, nb0 * 256, lb + L_HS + u * 2048);
    if (u == 3) dma2(s_h, g4 * 256 + 16 * (c16 ^ (g4 << 2)) + 8192, nb0 * 256, lb + L_HS + 8192);
    dma4(s_we, wv0, wv1, 32768, lb + L_W0 + u * 4096);
    dma4(s_we, wv0, wv1, 32768 + 16384, lb + L_W0 + 16384 + u * 4096);
    u32 wdv, wav, wxv;
    asm volatile("s_waitcnt vmcnt(10)\n\tv_readfirstlane_b32 %0, %5\n\tv_readfirstlane_b32 %1, %6\n\t"
                 "v_mov_b32 %2, %7\n\tv_mov_b32 %3, %8\n\tv_mov_b32 %4, %9"
                 : "=s"(ep_lo), "=s"(ep_hi), "=&v"(wdv), "=&v"(wav), "=&v"(wxv)
                 : "v"(epoch[0]), "v"(epoch[1]), "v"(ee[0]), "v"(ee[1]), "v"(ee[2]) : "memory");
    arrive(lb, 0);
    setup();
    asm volatile("s_waitcnt vmcnt(0)" ::: "memory");
    arrive(lb, 1);
    char* const slot = ws + WS_SLOT0 + (size_t)(RFL(ep_lo) & 3u) * WS_SLOT;
    await(lds, 1, 4);
    STAMP(12);
#pragma unroll
    for (int s = 0; s < 4; s++)
#pragma unroll
      for (int t = 0; t < 3; t++) hb[t][s] = h_frag(lds, t, s, lane);
    f32x4 acc[3][2];
#pragma unroll
    for (int t = 0; t < 3; t++) { acc[t][0] = f32x4{0, 0, 0, 0}; acc[t][1] = f32x4{0, 0, 0, 0}; }
#pragma unroll
    for (int s = 0; s < 4; s++) {
      const u32x4 fa = tr_frag(lds + L_W0, s, lane, 4 * u + (lane & 3), 0), fb = tr_frag(lds + L_W0, s, lane, 4 * u + (lane & 3), 1);
#pragma unroll
      for (int t = 0; t < 3; t++) { acc[t][0] = mfma16(fa, hb[t][s], acc[t][0]); acc[t][1] = mfma16(fb, hb[t][s], acc[t][1]); }
    }
    PIN(acc[2][1]);
    arrive(lb, 3);
    u32x4 bo[3];
#pragma unroll
    for (int t = 0; t < 3; t++) {
      const f32x4 x = acc[t][0] * LOG2E, y = acc[t][1] * LOG2E;
      bo[t] = u32x4{pk_bf16(x[0], x[1]), pk_bf16(x[2], x[3]), pk_bf16(y[0], y[1]), pk_bf16(y[2], y[3])};
    }
    await(lds, 3, 4);
#pragma unroll
    for (int t = 0; t < 3; t++) *(u32x4*)(lds + L_W0 + tile_node(t, c16) * 272 + u * 64 + g4 * 16) = bo[t];
    arrive(lb, 3);
    await(lds, 3, 8);
#pragma unroll
    for (int i = 0; i < 3; i++) bo[i] = *(const u32x4*)(lds + L_W0 + (i * 16 + (tid >> 4)) * 272 + (tid & 15) * 16);
    PIN(bo[0]); PIN(bo[1]); PIN(bo[2]);
    arrive(lb, 3);
    const __amdgpu_buffer_rsrc_t r_bw = rsrc(slot + WS_BT + (size_t)nb0 * 256, min(NPB, N - nb0) * 256);
    auto bstore = [&]() {
#pragma unroll
      for (int i = 0; i < 3; i++) __builtin_amdgcn_raw_buffer_store_b128(bo[i], r_bw, tid * 16, i * 4096, SC1);
    };
    auto bflag = [&]() {
      if (lane == 0 && __hip_atomic_fetch_add((u32*)(lds + L_SY) + 5, 1u, __ATOMIC_RELAXED, __HIP_MEMORY_SCOPE_WORKGROUP) == 3)
        __hip_atomic_store((gu64*)(slot + WS_FL) + b, ((((u64)ep_hi << 32) | ep_lo) + 1) & 0xfffffffffffull, RLX_AGENT);
    };
    STAMP(15);
    await(lds, 4, 4);
    bstore();
    await(lds, 3, 12);
    dma4(s_wh, wv0, wv1, 0, lb + L_W0 + u * 4096);
    dma4(s_wh, wv0, wv1, 16384, lb + L_W0 + 16384 + u * 4096);
    {
      const float wd = bf2f(wdv) * LOG2E;
      const u32 wdh = pk_bf16(wd, 0.f);
      const u32 wdp = wdh | (pk_bf16(wd - bf2f(wdh), 0.f) << 16), wap = pk_bf16(bf2f(wav) * LOG2E, 0.f);
#pragma unroll
      for (int i = 0; i < 4; i++) {
        char* kb = lds + L_KB + (i * 256 + tid) * 16;
        *(u32*)(kb + 12) = wdp;
        *(u16*)(kb + 10) = (u16)wap;
      }
      if (tid < D) *(u16*)(lds + L_WX + tid * 2) = (u16)wxv;
    }
    asm volatile("s_waitcnt vmcnt(8)" ::: "memory");
    bflag();
    STAMP(1);
    asm volatile("s_waitcnt vmcnt(0)" ::: "memory");
  } else {
    const int t1 = tid - 256;
    const u32x4 z = {0, 0, 0, 0};
#pragma unroll
    for (int i = 0; i < 10; i++) if (i < 9 || L_SM + i * 4096 + t1 * 16 < L_POS) *(u32x4*)(lds + L_SM + i * 4096 + t1 * 16) = z;
    setup();
    asm volatile("s_waitcnt vmcnt(9)" : "+v"(ed[0]), "+v"(ed[1]), "+v"(ed[2]) :: "memory");
    u32 rank[3], ebk[3];
#pragma unroll
    for (int i = 0; i < 3; i++) {
      ebk[i] = ed[i] / NPB;
      rank[i] = 0;
      if (t1 + 256 * i < ne) rank[i] = atomicAdd((u32*)(lds + L_BC) + ebk[i], 1u);
    }
    arrive(lb, 0);
    asm volatile("s_waitcnt vmcnt(2)\n\tv_readfirstlane_b32 %0, %8\n\tv_readfirstlane_b32 %1, %9"
                 : "=s"(ep_lo), "=s"(ep_hi), "+v"(es[0]), "+v"(es[1]), "+v"(es[2]), "+v"(ee[0]), "+v"(ee[1]), "+v"(ee[2])
                 : "v"(epoch[0]), "v"(epoch[1]) : "memory");
    ep_lo = RFL(ep_lo); ep_hi = RFL(ep_hi);
    char* const slot = ws + WS_SLOT0 + (size_t)(ep_lo & 3u) * WS_SLOT;
    const u64 tag = ((((u64)ep_hi << 32) | ep_lo) + 1) & 0xfffffffffffull;
    STAMP(14);
    await(lds, 0, 8);
    STAMP(13);
    const u32x4 c4 = *(const u32x4*)(lds + L_BC + lane * 16);
    const u32 cown = *(const u32*)(lds + L_BC + t1 * 4);
    u32x2 pd[3], ps[3];
#pragma unroll
    for (int i = 0; i < 3; i++) {
      const u32* qd = (const u32*)(lds + L_POS + ((ed[i] * 6) & ~3u));
      const u32* qs = (const u32*)(lds + L_POS + ((es[i] * 6) & ~3u));
      pd[i] = u32x2{qd[0], qd[1]}; ps[i] = u32x2{qs[0], qs[1]};
    }
    const u32 pown = *(const u16*)(lds + L_POS + min((nb0 * 3 + t1) * 2, MAXPOS - 2));
    const u32 tot = c4[0] + c4[1] + c4[2] + c4[3];
    const u32 ex = scan64(tot) - tot;
    u32* mine = (u32*)(lds + L_LS + u * 1024);
    *(u32x4*)(mine + lane * 4) = u32x4{ex, ex + c4[0], ex + c4[0] + c4[1], ex + c4[0] + c4[1] + c4[2]};
    __builtin_amdgcn_wave_barrier();
    u32 at[3];
#pragma unroll
    for (int i = 0; i < 3; i++) at[i] = L_RS + (mine[ebk[i]] + rank[i]) * 16;
    const u32 gword = (mine[t1] << 10) | cown;
#pragma unroll
    for (int i = 0; i < 3; i++) {
      const u32 shd = (ed[i] & 1u) * 16, shs = (es[i] & 1u) * 16;
      const u32 dxy = __builtin_amdgcn_alignbit(pd[i][1], pd[i][0], shd), sxy = __builtin_amdgcn_alignbit(ps[i][1], ps[i][0], shs);
      const u32 ex2 = pk_bf16(0.f, bf2f(dxy) - bf2f(sxy));
      const u32 yz = pk_bf16(bfhi(dxy) - bfhi(sxy), bf2f(pd[i][1] >> shd) - bf2f(ps[i][1] >> shs));
      u32x4 rc = {es[i] | ((ed[i] - ebk[i] * NPB) << 14) | ((u32)tag << 20), (u32)(tag >> 12), ee[i] | ex2, yz};
      PIN(rc);
      if (t1 + 256 * i < ne) *(u32x4*)(lds + at[i]) = rc;
    }
    if (t1 < NPB * 3) *(u16*)(lds + L_PD + t1 * 2) = (u16)pown;
    arrive(lb, 2);
    await(lds, 2, 4);
    const __amdgpu_buffer_rsrc_t r_rw = rsrc(slot + WS_REC + (size_t)b * CH * 16, CH * 16);
    u32x4 rv[3];
#pragma unroll
    for (int i = 0; i < 3; i++) rv[i] = *(const u32x4*)(lds + L_RS + (t1 + 256 * i) * 16);
#pragma unroll
    for (int i = 0; i < 3; i++) __builtin_amdgcn_raw_buffer_store_b128(rv[i], r_rw, t1 * 16, i * 4096, SC1);
    arrive(lb, 4);
    __hip_atomic_store((gu64*)(slot + WS_GR) + (size_t)b * MAXG + t1, (tag << 20) | gword, RLX_AGENT);
    const u64 tpub = __builtin_amdgcn_s_memrealtime();
    STAMP(2);
    await(lds, 4, 4);
    dma4(s_we, wv0, wv1, 0, lb + L_W1 + u * 4096);
    dma4(s_we, wv0, wv1, 16384, lb + L_W1 + 16384 + u * 4096);
#pragma unroll
    for (int i = 0; i < 5; i++) { const int o = L_POS + i * 4096 + t1 * 16; if (i < 4 || o < L_SRC) *(u32x4*)(lds + o) = z; }
    *(u32x4*)(lds + L_SRC + t1 * 16) = u32x4{OOB, OOB, OOB, OOB};
    if (t1 < 16) *(u32*)(lds + L_SRC + 4096 + t1 * 4) = OOB;
    await(lds, 1, 4);
#pragma unroll
    for (int s = 0; s < 4; s++)
#pragma unroll
      for (int t = 0; t < 3; t++) hb[t][s] = h_frag(lds, t, s, lane);
    const int csc = t1 < G ? t1 : G - 1;
    gu64* const gp = (gu64*)(slot + WS_GR) + (size_t)csc * MAXG + bk;
    while (__builtin_amdgcn_s_memrealtime() - tpub < PD1) __builtin_amdgcn_s_sleep(2);
    STAMP(4);
    u64 gv = __hip_atomic_load(gp, RLX_AGENT);
    SB();
    asm volatile("s_waitcnt vmcnt(1)" :: "v"(pf1), "v"(pf2) : "memory");
    arrive(lb, 6);
    await(lds, 6, 4);
    f32x4 acc[3][2];
#pragma unroll
    for (int t = 0; t < 3; t++) { acc[t][0] = f32x4{0, 0, 0, 0}; acc[t][1] = f32x4{0, 0, 0, 0}; }
#pragma unroll
    for (int s = 0; s < 4; s++) {
      const u32x4 fa = tr_frag(lds + L_W1, s, lane, 4 * u + ((lane & 3) >> 1), lane & 1), fb = tr_frag(lds + L_W1, s, lane, 4 * u + 2 + ((lane & 3) >> 1), lane & 1);
#pragma unroll
      for (int t = 0; t < 3; t++) { acc[t][0] = mfma16(hb[t][s], fa, acc[t][0]); acc[t][1] = mfma16(hb[t][s], fb, acc[t][1]); }
    }
#pragma unroll
    for (int tb = 0; tb < 2; tb++) {
      char* kb = lds + L_KB + (32 * u + 16 * tb + c16) * 16;
#pragma unroll
      for (int t = 0; t < 2; t++) {
        const f32x4 x = acc[t][tb] * LOG2E;
        *(u32x2*)(kb + (4 * t + g4) * 2048) = u32x2{pk_bf16(x[0], x[1]), pk_bf16(x[2], x[3])};
      }
      if (g4 < 2) {
#pragma unroll
        for (int i = 0; i < 4; i++) *(u16*)(kb + (4 * g4 + i) * 2048 + 8) = (u16)pk_bf16(acc[2][tb][i] * LOG2E, 0.f);
      }
    }
    SB();
    for (u32 spins = 0; !__all((gv >> 20) == tag) && spins < (1u << 20); spins++) {
      __builtin_amdgcn_s_sleep(4);
      if ((gv >> 20) != tag) gv = __hip_atomic_load(gp, RLX_AGENT);
    }
    STAMP(5);
    const u32 cnt = t1 < G ? ((u32)gv & 1023u) : 0u, st = ((u32)gv >> 10) & 1023u;
    const u32 inc = scan64(cnt);
    u32 wb = 0;
    if (lane == 63) wb = atomicAdd((u32*)(lds + L_SY) + 7, inc);
    *(u32*)(lds + L_BC + t1 * 4) = t1 < G ? (u32)gv & 0xfffffu : 0u;
    const u32 base = (t1 * CH + st) * 16, p0 = __builtin_amdgcn_readlane(wb, 63) + inc - cnt;
    const u32 lim = min(cnt, p0 < 1024 ? 1024 - p0 : 0u);
    u32* list = (u32*)(lds + L_LS);
#pragma unroll
    for (u32 k = 0; k < 6; k++) list[k < lim ? p0 + k : 1024 + t1] = base + k * 16;
    if (__any(lim > 6)) {
#pragma unroll 1
      for (u32 k = 6; k < lim; k++) list[p0 + k] = base + k * 16;
    }
  }
  STAMP(3);
  __syncthreads();
  ep_lo = RFL(ep_lo); ep_hi = RFL(ep_hi);
  const u64 ep = ((u64)ep_hi << 32) | ep_lo, tag = (ep + 1) & 0xfffffffffffull;
  char* const slot = ws + WS_SLOT0 + (size_t)(ep_lo & 3u) * WS_SLOT;
  const __amdgpu_buffer_rsrc_t r_bt = rsrc(slot + WS_BT, N * 256), r_rec = rsrc(slot + WS_REC, G * CH * 16);
  const u32 tag0 = (u32)tag & 0xfffu, tag1 = (u32)(tag >> 12);
  f32x4 cp[3];
  u32 pw[4] = {0, 0, 0, 0};
  {
    const u32 total = *(const u32*)(lds + L_SY + 28);
    const u32 a0 = *(const u32*)(lds + L_LS + tid * 4), a1 = *(const u32*)(lds + L_LS + (NT + tid) * 4);
    const bool v0 = tid < total, v1 = NT + tid < total;
    u32x4 r0 = __builtin_amdgcn_raw_buffer_load_b128(r_rec, v0 ? a0 : OOB, 0, SC1);
    u32x4 r1 = __builtin_amdgcn_raw_buffer_load_b128(r_rec, v1 ? a1 : OOB, 0, SC1);
    gu64* const fp = (gu64*)(slot + WS_FL) + min(wv * 32 + (lane & 31), G - 1);
    u64 fv = __hip_atomic_load(fp, RLX_AGENT);
    if (total > 8 * CAPW) *(u32*)(lds + L_CNT + 32) = 1;
    SB();
#pragma unroll
    for (int t = 0; t < 3; t++) cp[t] = f32x4{0, 0, 0, 0};
#pragma unroll
    for (int s = 0; s < 4; s++) {
      const u32x4 f1 = tr_frag(lds + L_W0, s, lane, 2 * wv + ((lane & 3) >> 1), lane & 1);
#pragma unroll
      for (int t = 0; t < 3; t++) cp[t] = mfma16(f1, hb[t][s], cp[t]);
    }
#pragma unroll
    for (int t = 0; t < 3; t++) { cp[t] *= LOG2E; PIN(cp[t]); }
    SB();
    STAMP(6);
    r0 = settle(r0, v0, r_rec, a0, tag0, tag1);
    r1 = settle(r1, v1, r_rec, a1, tag0, tag1);
    STAMP(7);
    u32 k0 = 0, k1 = 0;
    if (v0) k0 = take_rank(lds, r0);
    if (v1) k1 = take_rank(lds, r1);
    for (u32 spins = 0; !__all(fv == tag) && spins < (1u << 20); spins++) {
      __builtin_amdgcn_s_sleep(4);
      fv = __hip_atomic_load(fp, RLX_AGENT);
    }
    __builtin_amdgcn_s_waitcnt(0x0f70);
    pw[0] = __builtin_amdgcn_raw_buffer_load_b32(r_bt, v0 && k0 < PFR ? (r0[0] & 0x3fffu) << 8 : OOB, 0, SC1);
    pw[1] = __builtin_amdgcn_raw_buffer_load_b32(r_bt, v0 && k0 < PFR ? ((r0[0] & 0x3fffu) << 8) + 128 : OOB, 0, SC1);
    SB();
    if (v0) put_rec(lds, r0, k0);
    if (v1) put_rec(lds, r1, k1);
  }
  STAMP(9);
  __syncthreads();
  STAMP(8);
  const u32* wcnt = (const u32*)(lds + L_CNT);
  const u32 ci = wcnt[lane >> 3], cj = wcnt[lane & 7];
  const int ovf = RFL(wcnt[8]);
  auto load_wh2 = [&]() {
    dma4(s_wh, wv0 + (wv >> 2) * 16384, wv1 + (wv >> 2) * 16384, 32768, lb + L_W0 + wv * 4096);
  };
  u32x4 v[8], wxf[8];
  int win;
  {
    const u64 gt = __ballot(cj * 8 + (lane & 7) > ci * 8 + (lane >> 3));
    const u32 rk = __builtin_popcount((u32)(gt >> (lane & 56)) & 0xffu);
    const int wl = __builtin_ctzll(__ballot(rk == (u32)(wv < 4 ? wv : 11 - wv)));
    win = wl >> 3;
    const int cntw = ovf ? 1 : __builtin_amdgcn_readlane(ci, wl);
    const int nsteps = (cntw + 15) >> 4;
    const u32 lo = r * 4;
    u32 srca = L_SRC + win * CAPW * 4 + hh * 32;
    u32 GA[8], GB[8], off[8];
    const bool cfon = rp == hh;
    u32 cfa = cfon ? (L_CF + (win * CAPW + 8 * rh + rj) * 16) : L_ZERO;
    const u32 cfs = cfon ? 256u : 0u;
    u32 sma = L_SM + (win * CAPW + 8 * hh + (c16 >> 2)) * 40 + (g4 & 1) * 32 + (lane & 3) * 8;
    u32x4 cf, kbA, kbB, sf = {0, 0, 0, 0}, mB0 = {0, 0, 0, 0}, mB1 = {0, 0, 0, 0}, mA0, mA1;
    f32x16 yA, yB;
    {
      const u32x4 i0 = *(const u32x4*)(lds + srca), i1 = *(const u32x4*)(lds + srca + 16);
      const u32x4 i2 = *(const u32x4*)(lds + srca + 64), i3 = *(const u32x4*)(lds + srca + 80);
      cf = *(const u32x4*)(lds + cfa);
      kbA = *(const u32x4*)(lds + L_KB + win * 2048 + (2 * r + hh) * 16);
      kbB = *(const u32x4*)(lds + L_KB + win * 2048 + (64 + 2 * r + hh) * 16);
      srca += 128;
      SB();
#pragma unroll
      for (int j = 0; j < 4; j++) { off[j] = i0[j] + lo; off[4 + j] = i1[j] + lo; }
#pragma unroll
      for (int j = 0; j < 8; j++) GA[j] = __builtin_amdgcn_raw_buffer_load_b32(r_bt, off[j], 0, SC1);
      SB();
#pragma unroll
      for (int j = 0; j < 8; j++) GB[j] = __builtin_amdgcn_raw_buffer_load_b32(r_bt, off[j] + 128, 0, SC1);
      SB();
      asm volatile("" :: "v"(pw[0]), "v"(pw[1]), "v"(pw[2]), "v"(pw[3]));
      yA = mfma32(cf, kbA, zero);
#pragma unroll
      for (int j = 0; j < 4; j++) { off[j] = i2[j] + lo; off[4 + j] = i3[j] + lo; }
      yA = mfma32(sel0, u32x4{GA[0], GA[1], GA[2], GA[3]}, yA);
      yA = mfma32(sel1, u32x4{GA[4], GA[5], GA[6], GA[7]}, yA);
      PIN(yA);
      SB();
#pragma unroll
      for (int j = 0; j < 8; j++) GA[j] = __builtin_amdgcn_raw_buffer_load_b32(r_bt, off[j], 0, SC1);
      SB();
    }
#define MF(d, a, bb, c) do { SB(); d = mfma32(a, bb, c); PIN(d); SB(); } while (0)
#define SA(y, i, j) do { p_ = f32x2{__builtin_amdgcn_exp2f(-y[i]), __builtin_amdgcn_exp2f(-y[(i) + 1])}; \
                         q_ = f32x2{__builtin_amdgcn_exp2f(-y[j]), __builtin_amdgcn_exp2f(-y[(j) + 1])}; SB(); p_ += 1.0f; q_ += 1.0f; SB(); } while (0)
#define SBB(y, i, j, o0, o1) do { p_ = f32x2{__builtin_amdgcn_rcpf(p_[0]), __builtin_amdgcn_rcpf(p_[1])}; \
                         q_ = f32x2{__builtin_amdgcn_rcpf(q_[0]), __builtin_amdgcn_rcpf(q_[1])}; SB(); \
                         p_ *= f32x2{y[i], y[(i) + 1]}; q_ *= f32x2{y[j], y[(j) + 1]}; SB(); \
                         u32 t0_ = pk_bf16(p_[0], p_[1]), t1_ = pk_bf16(q_[0], q_[1]); \
                         asm volatile("" : "+v"(t0_), "+v"(t1_)); o0 = t0_; o1 = t1_; SB(); } while (0)
#define S4(y, i, j, o0, o1) do { SA(y, i, j); SBB(y, i, j, o0, o1); } while (0)
#define LD2(Gx, j, add) do { Gx[j] = __builtin_amdgcn_raw_buffer_load_b32(r_bt, off[j] + (add), 0, SC1); \
                             Gx[(j) + 1] = __builtin_amdgcn_raw_buffer_load_b32(r_bt, off[(j) + 1] + (add), 0, SC1); SB(); } while (0)
    f32x2 p_, q_;
    for (int s = 1; s < nsteps; s++) {
      { const int left = nsteps - s; if (left >= 4) __builtin_amdgcn_s_setprio(3); else if (left == 3) __builtin_amdgcn_s_setprio(2); else if (left == 2) __builtin_amdgcn_s_setprio(1); else __builtin_amdgcn_s_setprio(0); }
      MF(yB, cf, kbB, zero);
      S4(yA, 0, 2, mA0[0], mA0[1]);
      MF(yB, sel0, (u32x4{GB[0], GB[1], GB[2], GB[3]}), yB);
      cfa += cfs;
      cf = *(const u32x4*)(lds + cfa);
      SA(yA, 4, 6);
      LD2(GB, 0, 128);
      SBB(yA, 4, 6, mA0[2], mA0[3]);
      LD2(GB, 2, 128);
      MF(yB, sel1, (u32x4{GB[4], GB[5], GB[6], GB[7]}), yB);
      SA(yA, 8, 10);
      LD2(GB, 4, 128);
      MF(aB0, mB0, sf, aB0);
      SBB(yA, 8, 10, mA1[0], mA1[1]);
      LD2(GB, 6, 128);
      const u32x4 i0 = *(const u32x4*)(lds + srca), i1 = *(const u32x4*)(lds + srca + 16);
      srca += 64;
      SA(yA, 12, 14);
      MF(aB1, mB1, sf, aB1);
      sf = s_frag(lds + sma);
      sma += 640;
      SBB(yA, 12, 14, mA1[2], mA1[3]);
      MF(yA, cf, kbA, zero);
      S4(yB, 0, 2, mB0[0], mB0[1]);
      MF(yA, sel0, (u32x4{GA[0], GA[1], GA[2], GA[3]}), yA);
#pragma unroll
      for (int j = 0; j < 4; j++) { off[j] = i0[j] + lo; off[4 + j] = i1[j] + lo; }
      SA(yB, 4, 6);
      LD2(GA, 0, 0);
      SBB(yB, 4, 6, mB0[2], mB0[3]);
      LD2(GA, 2, 0);
      MF(yA, sel1, (u32x4{GA[4], GA[5], GA[6], GA[7]}), yA);
      SA(yB, 8, 10);
      LD2(GA, 4, 0);
      MF(aA0, mA0, sf, aA0);
      SBB(yB, 8, 10, mB1[0], mB1[1]);
      LD2(GA, 6, 0);
      SA(yB, 12, 14);
      MF(aA1, mA1, sf, aA1);
      SBB(yB, 12, 14, mB1[2], mB1[3]);
    }
    {
      const int rem = cntw - 16 * (nsteps - 1);
      load_wh2();
      MF(yB, cf, kbB, zero);
      mA0 = u32x4{0, 0, 0, 0}; mA1 = u32x4{0, 0, 0, 0};
      S4(yA, 0, 8, mA0[0], mA1[0]);
      MF(yB, sel0, (u32x4{GB[0], GB[1], GB[2], GB[3]}), yB);
      if (rem > 4) S4(yA, 2, 10, mA0[1], mA1[1]);
      MF(yB, sel1, (u32x4{GB[4], GB[5], GB[6], GB[7]}), yB);
      if (rem > 8) S4(yA, 4, 12, mA0[2], mA1[2]);
      MF(aB0, mB0, sf, aB0);
      if (rem > 12) S4(yA, 6, 14, mA0[3], mA1[3]);
      MF(aB1, mB1, sf, aB1);
      sf = s_frag(lds + sma);
      mB0 = u32x4{0, 0, 0, 0}; mB1 = u32x4{0, 0, 0, 0};
      S4(yB, 0, 8, mB0[0], mB1[0]);
      MF(aA0, mA0, sf, aA0);
      if (rem > 4) S4(yB, 2, 10, mB0[1], mB1[1]);
      MF(aA1, mA1, sf, aA1);
      if (rem > 8) S4(yB, 4, 12, mB0[2], mB1[2]);
      if (rem > 12) S4(yB, 6, 14, mB0[3], mB1[3]);
      MF(aB0, mB0, sf, aB0);
      MF(aB1, mB1, sf, aB1);
    }
#pragma unroll
    for (int q8 = 0; q8 < 8; q8++) {
      const f32x16& a0 = q8 < 4 ? aA0 : aB0;
      const f32x16& a1 = q8 < 4 ? aA1 : aB1;
#pragma unroll
      for (int q = 0; q < 4; q++) v[q8][q] = pk_bf16(a0[4 * (q8 & 3) + q], a1[4 * (q8 & 3) + q]);
    }
    if (r < 20 && (r & 3) == 0 && !ovf) {
      char* mi = lds + mi_row(r * 2 + win) + 16 * hh;
#pragma unroll
      for (int q8 = 0; q8 < 8; q8++) *(u32x4*)(mi + 32 * q8) = v[q8];
    }
  }
  if (__builtin_expect(ovf, 0)) {
    float* macc = (float*)(lds + L_SM);
    float* pacc = (float*)(lds + L_SM + NPB * D * 4);
    __syncthreads();
    for (int i = tid; i < NPB * D + NPB * 4; i += NT) macc[i] = 0.f;
    __syncthreads();
    const u32 wxp = *(const u32*)(lds + L_WX + lane * 4);
    const float wx0 = bf2f(wxp), wx1 = bfhi(wxp);
    const u32x4 kc0 = *(const u32x4*)(lds + L_KB + (2 * lane) * 16), kc1 = *(const u32x4*)(lds + L_KB + (2 * lane + 1) * 16);
    const float wd0 = bf2f(kc0[3]) + bfhi(kc0[3]), wd1 = bf2f(kc1[3]) + bfhi(kc1[3]);
    const float wa0 = bfhi(kc0[2]), wa1 = bfhi(kc1[2]);
    u32 rot = 0;
    for (int c = 0; c < G; c++) {
      const u32 oo = RFL(*(const u32*)(lds + L_BC + c * 4));
      const u32 s0 = oo >> 10, n = oo & 1023u;
      for (u32 k = (wv - rot) & 7u; k < n; k += 8) {
        u32x4 rr = __builtin_amdgcn_raw_buffer_load_b128(r_rec, (c * CH + s0 + k) * 16, 0, SC1);
        rr = settle(rr, true, r_rec, (c * CH + s0 + k) * 16, tag0, tag1);
        const u32 nl = (rr[0] >> 14) & 0x3fu, sl = nl >> 3, wn = nl & 7u;
        const u32 bv = __builtin_amdgcn_raw_buffer_load_b32(r_bt, ((rr[0] & 0x3fffu) << 8) + lane * 4, 0, SC1);
        const float d2 = bfhi(dist2(rr[2], rr[3])), eav = bf2f(rr[2]);
        const float a0 = bf2f(*(const u16*)(lds + L_KB + wn * 2048 + (2 * lane) * 16 + sl * 2));
        const float a1 = bf2f(*(const u16*)(lds + L_KB + wn * 2048 + (2 * lane + 1) * 16 + sl * 2));
        const float y0 = a0 + bf2f(bv) + d2 * wd0 + eav * wa0, y1 = a1 + bfhi(bv) + d2 * wd1 + eav * wa1;
        const u32 mp = silu2(y0, y1);
        const float m0 = bf2f(mp), m1 = bfhi(mp);
        atomicAdd(&macc[nl * D + 2 * lane], m0);
        atomicAdd(&macc[nl * D + 2 * lane + 1], m1);
        float csv = m0 * wx0 + m1 * wx1;
#pragma unroll
        for (int o = 32; o > 0; o >>= 1) csv += __shfl_xor(csv, o, 64);
        if (lane < 3) atomicAdd(&pacc[nl * 4 + lane], csv * (lane == 0 ? bfhi(rr[2]) : lane == 1 ? bf2f(rr[3]) : bfhi(rr[3])));
      }
      rot += n;
    }
    __syncthreads();
    for (int i = tid; i < NPB * D / 2; i += NT) {
      const int n = i >> 6, f = (i & 63) * 2;
      *(u32*)(lds + mi_row(n) + f * 2) = pk_bf16(macc[n * D + f], macc[n * D + f + 1]);
    }
    if (tid < NPB * 3) {
      const int n = tid / 3, c = tid - n * 3;
      if (nb0 + n < N) out[(size_t)N * D + (size_t)nb0 * 3 + tid] = (u16)pk_bf16(bf2f(*(const u16*)(lds + L_PD + tid * 2)) + LN2 * pacc[n * 4 + c], 0.f);
    }
  }
  u32x2 hr[3];
  u32x4 f2[4];
#pragma unroll
  for (int t = 0; t < 3; t++) {
    const int n = tile_node(t, c16);
    hr[t] = *(const u32x2*)(lds + L_HS + n * 256 + 16 * ((2 * wv + (g4 >> 1)) ^ hswz(n)) + 8 * (g4 & 1));
  }
#pragma unroll
  for (int q8 = 0; q8 < 8; q8++) wxf[q8] = *(const u32x4*)(lds + L_WX + (16 * q8 + 8 * hh) * 2);
  const int kind = lane & 3, pnl = (lane >> 2) * 8 + win;
  const bool pon = kind != 0 && lane < 20 && !ovf;
  const u32 p0 = *(const u16*)(lds + L_PD + (pon ? pnl * 6 + kind * 2 - 2 : 0));
  const int mia = mi_row(tile_node(0, c16)) + g4 * 16, mib = mi_row(tile_node(2, c16)) + g4 * 16;
  asm volatile("s_waitcnt vmcnt(0)" ::: "memory");
  STAMP(10);
  __syncthreads();
  {
    u32x4 mb[3][4];
#pragma unroll
    for (int s = 0; s < 4; s++) {
      f2[s] = tr_frag(lds + L_W0, s, lane, 2 * wv + ((lane & 3) >> 1), lane & 1);
      mb[0][s] = *(const u32x4*)(lds + mia + s * 64);
      mb[1][s] = *(const u32x4*)(lds + mia + 8192 + s * 64);
      mb[2][s] = *(const u32x4*)(lds + mib + s * 64);
    }
    SB();
#pragma unroll
    for (int s = 0; s < 4; s++)
#pragma unroll
      for (int t = 0; t < 3; t++) cp[t] = mfma16(f2[s], mb[t][s], cp[t]);
    f32x16 z = zero, z1 = zero;
#pragma unroll
    for (int t = 0; t < 3; t++) {
      const f32x4 y = cp[t];
      f32x4 e;
#pragma unroll
      for (int i = 0; i < 4; i++) e[i] = __builtin_amdgcn_exp2f(-y[i]);
      SB();
      if (t & 1) { z1 = mfma32(wxf[3 * t], v[3 * t], z1); PIN(z1); } else { z = mfma32(wxf[3 * t], v[3 * t], z); PIN(z); }
      SB();
      e += 1.0f;
#pragma unroll
      for (int i = 0; i < 4; i++) e[i] = __builtin_amdgcn_rcpf(e[i]);
      SB();
      if (t & 1) { z = mfma32(wxf[3 * t + 1], v[3 * t + 1], z); PIN(z); } else { z1 = mfma32(wxf[3 * t + 1], v[3 * t + 1], z1); PIN(z1); }
      SB();
      const f32x4 o = f32x4{bf2f(hr[t][0]), bfhi(hr[t][0]), bf2f(hr[t][1]), bfhi(hr[t][1])} + (y * LN2) * e;
      *(u32x2*)(lds + L_SM + tile_node(t, c16) * 272 + wv * 32 + g4 * 8) = u32x2{pk_bf16(o[0], o[1]), pk_bf16(o[2], o[3])};
      SB();
      if (t == 0) { z = mfma32(wxf[2], v[2], z); PIN(z); }
      if (t == 1) { z1 = mfma32(wxf[5], v[5], z1); PIN(z1); }
      SB();
    }
    __syncthreads();
    const __amdgpu_buffer_rsrc_t r_out = rsrc(out, N * 262);
    const u32x4 o0 = *(const u32x4*)(lds + L_SM + (tid >> 4) * 272 + (tid & 15) * 16);
    const u32x4 o1 = *(const u32x4*)(lds + L_SM + (32 + ((tid >> 4) & 7)) * 272 + (tid & 15) * 16);
    const u32 oo = nb0 * 256 + tid * 16;
    __builtin_amdgcn_raw_buffer_store_b128(o0, r_out, oo < N * 256 ? oo : OOB, 0, 2);
    __builtin_amdgcn_raw_buffer_store_b128(o1, r_out, tid < 128 && oo + 8192 < N * 256 ? oo + 8192 : OOB, 0, 2);
    __builtin_amdgcn_raw_buffer_store_b16((u16)pk_bf16(bf2f(p0) + LN2 * (z[0] + z1[0]), 0.f), r_out,
                                          pon ? N * 256 + ((nb0 + pnl) * 3 + kind - 1) * 2 : OOB, 0, 2);
  }
  if (b == 0 && tid == 0) __hip_atomic_store((gu64*)(ws + WS_EP), ep + 1, RLX_AGENT);
  STAMP(11);
}

extern "C" void kernel_launch(void* const* d_in, const int* in_sizes, int n_in, void* d_out, int out_size, void* d_ws,
                              size_t ws_size, hipStream_t stream) {
  const int N = in_sizes[0] / D, E = in_sizes[3];
  const int NB = (N + NPB - 1) / NPB, C = (E + CH - 1) / CH;
  const int G = NB > C ? NB : C;
  k_egnn<<<G, NT, 0, stream>>>((const u16*)d_in[0], (const u16*)d_in[1], (const u16*)d_in[2], (const int*)d_in[3],
                               (const int*)d_in[4], (const u16*)d_in[5], (const u16*)d_in[6], (const u16*)d_in[7],
                               (char*)d_ws, (u16*)d_out, N, E, G);
}
```

```cpp
#include <hip/hip_runtime.h>
#include <hip/hip_bf16.h>

typedef unsigned int u32;
typedef unsigned short u16;
typedef unsigned long long u64;
typedef u32 u32x2 __attribute__((ext_vector_type(2)));
typedef u32 u32x4 __attribute__((ext_vector_type(4)));
typedef short s16x4 __attribute__((ext_vector_type(4)));
typedef short bf16x8 __attribute__((ext_vector_type(8)));
typedef float f32x2 __attribute__((ext_vector_type(2)));
typedef float f32x4 __attribute__((ext_vector_type(4)));
typedef float f32x16 __attribute__((ext_vector_type(16)));
typedef __bf16 bf16x2_t __attribute__((ext_vector_type(2)));
typedef __attribute__((address_space(1))) u64 gu64;
typedef __attribute__((address_space(3))) s16x4 lds_s16x4;
typedef __attribute__((address_space(3))) char lds_char;
#define DI __device__ __forceinline__
#define RLX_AGENT __ATOMIC_RELAXED, __HIP_MEMORY_SCOPE_AGENT
#define SC1 16
#define SB() __builtin_amdgcn_sched_barrier(0)
#define PIN(x) asm volatile("" : "+v"(x))
#define RFL(x) __builtin_amdgcn_readfirstlane(x)

constexpr int D = 128;
constexpr int NT = 512;
constexpr int NPB = 40;
constexpr int CH = 640;
constexpr int MAXG = 256;
constexpr int CAPW = 128;
constexpr int MAXPOS = 61440;
constexpr u32 PFR = 12;
constexpr float LOG2E = 1.4426950408889634f;
constexpr float LN2 = 0.6931471805599453f;
constexpr u32 OOB = 0xfffff000u;

constexpr size_t WS_EP = 0;
constexpr size_t WS_SLOT0 = (size_t)2 << 20, WS_SLOT = (size_t)6 << 20;
constexpr size_t WS_GR = 0;
constexpr size_t WS_BT = WS_GR + (size_t)MAXG * MAXG * 8;
constexpr size_t WS_REC = WS_BT + (size_t)MAXG * NPB * 256;
constexpr size_t WS_FL = WS_REC + (size_t)MAXG * CH * 16;
static_assert(WS_FL + MAXG * 8 <= WS_SLOT, "slot");

#define STAMP(k) do { } while (0)

DI float bf2f(u32 b) { return __builtin_bit_cast(float, b << 16); }
DI float bfhi(u32 b) { return __builtin_bit_cast(float, b & 0xffff0000u); }
DI u32 pk_bf16(float lo, float hi) {
  f32x2 v = {lo, hi};
  bf16x2_t r = __builtin_convertvector(v, bf16x2_t);
  return __builtin_bit_cast(u32, r);
}
DI f32x4 mfma16(u32x4 a, u32x4 b, f32x4 c) {
  return __builtin_amdgcn_mfma_f32_16x16x32_bf16(__builtin_bit_cast(bf16x8, a), __builtin_bit_cast(bf16x8, b), c, 0, 0, 0);
}
DI f32x16 mfma32(u32x4 a, u32x4 b, f32x16 c) {
  return __builtin_amdgcn_mfma_f32_32x32x16_bf16(__builtin_bit_cast(bf16x8, a), __builtin_bit_cast(bf16x8, b), c, 0, 0, 0);
}
DI __amdgpu_buffer_rsrc_t rsrc(const void* p, u32 bytes) { return __builtin_amdgcn_make_buffer_rsrc((void*)p, 0, bytes, 0x00020000); }
DI u32x4 srd(const void* p, u32 bytes) { const u64 a = (u64)p; return u32x4{(u32)a, (u32)(a >> 32) & 0xffffu, bytes, 0x00020000u}; }
DI void dma4(u32x4 sd, u32 v0, u32 v1, u32 soff, u32 ldsa) {
  u32 keep;
  asm volatile("s_mov_b32 %0, m0\n\ts_mov_b32 m0, %1\n\ts_nop 0\n\t"
               "buffer_load_dwordx4 %2, %4, %5 offen lds\n\tbuffer_load_dwordx4 %2, %4, %5 offen offset:1024 lds\n\t"
               "buffer_load_dwordx4 %3, %4, %5 offen offset:2048 lds\n\tbuffer_load_dwordx4 %3, %4, %5 offen offset:3072 lds\n\t"
               "s_mov_b32 m0, %0"
               : "=&s"(keep) : "s"(RFL(ldsa)), "v"(v0), "v"(v1), "s"(sd), "s"(soff) : "memory");
}
DI void dma2(u32x4 sd, u32 v0, u32 soff, u32 ldsa) {
  u32 keep;
  asm volatile("s_mov_b32 %0, m0\n\ts_mov_b32 m0, %1\n\ts_nop 0\n\t"
               "buffer_load_dwordx4 %2, %3, %4 offen lds\n\tbuffer_load_dwordx4 %2, %3, %4 offen offset:1024 lds\n\t"
               "s_mov_b32 m0, %0"
               : "=&s"(keep) : "s"(RFL(ldsa)), "v"(v0), "s"(sd), "s"(soff) : "memory");
}
DI u32 scan64(u32 x) {
  x += __builtin_amdgcn_update_dpp(0u, x, 0x111, 0xf, 0xf, false);
  x += __builtin_amdgcn_update_dpp(0u, x, 0x112, 0xf, 0xf, false);
  x += __builtin_amdgcn_update_dpp(0u, x, 0x114, 0xf, 0xf, false);
  x += __builtin_amdgcn_update_dpp(0u, x, 0x118, 0xf, 0xf, false);
  x += __builtin_amdgcn_update_dpp(0u, x, 0x142, 0xa, 0xf, false);
  x += __builtin_amdgcn_update_dpp(0u, x, 0x143, 0xc, 0xf, false);
  return x;
}

constexpr int L_ZERO = 0;
constexpr int L_CNT = L_ZERO + 16;
constexpr int L_BC = L_CNT + 48;
constexpr int L_SY = L_BC + 1024;
constexpr int L_SM = L_SY + 32;
constexpr int L_CF = L_SM + 8 * 8 * 640;
constexpr int L_SRC = L_CF + 8 * CAPW * 16;
constexpr int L_KB = L_SRC + 8 * CAPW * 4 + 64;
constexpr int L_W1 = L_KB + 8 * D * 16;
constexpr int L_W0 = L_W1 + 32768;
constexpr int L_HS = L_W0 + 32768;
constexpr int L_WX = L_HS + NPB * 256;
constexpr int L_PD = L_WX + 256;
constexpr int L_LS = L_PD + 256;
constexpr int L_END = L_LS + 8 * 768;
constexpr int L_RS = L_W0 - CH * 16;
constexpr int L_POS = L_RS - MAXPOS;
static_assert(L_END <= 160 * 1024, "lds");
static_assert(L_POS >= L_SM + 36864 && L_POS <= L_CF && (L_POS % 16) == 0, "pos table");
static_assert((L_SM % 16) == 0 && (L_BC % 16) == 0 && (L_SRC % 16) == 0 && (L_KB % 16) == 0 && (L_W0 % 16) == 0 && (L_HS % 16) == 0 && (L_WX % 16) == 0 && (L_LS % 16) == 0, "align");

DI int tile_node(int t, int c) { return t < 2 ? ((c & 3) * 8 + 4 * t + (c >> 2)) : (32 + (c & 7)); }
DI int mi_row(int nl) { return L_KB + (nl & 7) * 2048 + (nl & 3) * 64 + (nl >> 3) * 272; }
DI int wswz(int k) { return ((k & 3) << 2) | ((k >> 2) & 2); }
DI int hswz(int n) { return ((n & 3) << 2) | ((n >> 3) & 3); }
DI u32x4 tr_frag(const char* img, int s, int lane, int ch, int hf) {
  const int k0 = 8 * (lane >> 4) + ((lane & 15) >> 2);
  const char* p = img + (32 * s + k0) * 256 + 16 * (ch ^ wswz(k0)) + 8 * hf;
  const s16x4 a = __builtin_amdgcn_ds_read_tr16_b64_v4i16((lds_s16x4*)p);
  const s16x4 b = __builtin_amdgcn_ds_read_tr16_b64_v4i16((lds_s16x4*)(p + 1024));
  const u32x2 x = __builtin_bit_cast(u32x2, a), y = __builtin_bit_cast(u32x2, b);
  return u32x4{x[0], x[1], y[0], y[1]};
}
DI u32x4 s_frag(const char* p) {
  const u32x2 x = __builtin_bit_cast(u32x2, __builtin_amdgcn_ds_read_tr16_b64_v4i16((lds_s16x4*)p));
  const u32x2 y = __builtin_bit_cast(u32x2, __builtin_amdgcn_ds_read_tr16_b64_v4i16((lds_s16x4*)(p + 160)));
  return u32x4{x[0], x[1], y[0], y[1]};
}
DI u32x4 h_frag(const char* lds, int t, int s, int lane) {
  const int n = tile_node(t, lane & 15);
  return *(const u32x4*)(lds + L_HS + n * 256 + 16 * ((4 * s + (lane >> 4)) ^ hswz(n)));
}
DI u32 dist2(u32 w2, u32 w3) {
  const float dx = bf2f(w2), dy = bfhi(w2), dz = bf2f(w3);
  const u32 q0 = pk_bf16(dx * dx, dy * dy), q1 = pk_bf16(dz * dz, 0.f);
  const float s2 = bf2f(q0) + bfhi(q0) + bf2f(q1);
  return pk_bf16(s2, s2);
}
DI u32 take_rank(char* lds, u32x4 rc) { return atomicAdd((u32*)(lds + L_CNT) + ((rc[0] >> 14) & 7u), 1u); }
DI void put_rec(char* lds, u32x4 rc, u32 rank) {
  const u32 win = (rc[0] >> 14) & 7u, slot = (rc[0] >> 17) & 7u;
  const u64 oh = 0x3f80ull << (slot * 16);
  const u32x4 cf = {slot < 4 ? (u32)oh : 0u, slot < 4 ? (u32)(oh >> 32) : 0u, (slot == 4 ? 0x3f80u : 0u) | (rc[1] << 16), dist2(rc[2], rc[3])};
  if (rank < CAPW) {
    const u32 k = ((rank & 1u) << 3) + ((rank & 15u) >> 1), sp = win * CAPW + (rank & ~15u) + k;
    *(u32*)(lds + L_SRC + sp * 4) = (rc[0] & 0x3fffu) << 8;
    *(u32x4*)(lds + L_CF + sp * 16) = cf;
    *(u32x2*)(lds + L_SM + sp * 40 + slot * 8) = u32x2{0x3f80u | (rc[2] << 16), __builtin_amdgcn_alignbit(rc[3], rc[2], 16)};
  } else *(u32*)(lds + L_CNT + 32) = 1;
}
DI void rec_tags(u64 tag, u32 (&t)[3]) {
  t[0] = (u32)tag << 20; t[1] = ((u32)tag >> 12) << 16; t[2] = ((u32)(tag >> 28) ^ (u32)tag) << 16;
}
DI bool rec_stale(u32x4 rc, const u32 (&t)[3]) { return (((rc[0] ^ t[0]) >> 20) | (((rc[1] ^ t[1]) | (rc[3] ^ t[2])) >> 16)) != 0; }
DI u32x4 settle(u32x4 rc, bool on, __amdgpu_buffer_rsrc_t r, u32 off, const u32 (&t)[3]) {
  for (u32 spins = 0; __any(on && rec_stale(rc, t)) && spins < (1u << 20); spins++) {
    __builtin_amdgcn_s_sleep(4);
    if (on && rec_stale(rc, t)) rc = __builtin_amdgcn_raw_buffer_load_b128(r, off, 0, SC1);
  }
  return rc;
}
DI void arrive(u32 lb, int i) {
  asm volatile("s_mov_b64 exec, 1\n\tds_add_u32 %0, %1 offset:%2\n\ts_mov_b64 exec, -1" :: "v"(lb), "v"(1u), "i"(L_SY + 4 * i) : "memory");
}
DI void await(char* lds, int i, u32 n) {
  asm volatile("" ::: "memory");
  while ((u32)RFL(__hip_atomic_load((u32*)(lds + L_SY) + i, __ATOMIC_RELAXED, __HIP_MEMORY_SCOPE_WORKGROUP)) < n) { }
  asm volatile("" ::: "memory");
}
DI u32 silu2(float a, float b) {
  f32x2 yy = {a, b};
  f32x2 e = {__builtin_amdgcn_exp2f(-a), __builtin_amdgcn_exp2f(-b)};
  e += 1.0f;
  f32x2 rc = {__builtin_amdgcn_rcpf(e[0]), __builtin_amdgcn_rcpf(e[1])};
  yy *= rc;
  return pk_bf16(yy[0], yy[1]);
}

__global__ __launch_bounds__(NT, 2) void k_egnn(const u16* __restrict__ h, const u16* __restrict__ pos,
                                                const u16* __restrict__ ea, const int* __restrict__ src,
                                                const int* __restrict__ dst, const u16* __restrict__ w_e,
                                                const u16* __restrict__ w_x, const u16* __restrict__ w_h,
                                                char* __restrict__ ws, u16* __restrict__ out, int N, int E, int G) {
  __shared__ __attribute__((aligned(16))) char lds[L_END];
  const int tid = threadIdx.x, lane = tid & 63, b = blockIdx.x;
  const int wv = RFL(tid >> 6);
  const int u = wv & 3, c16 = lane & 15, g4 = lane >> 4;
  const bool role1 = wv >= 4;
  const u32 lb = (u32)(size_t)(lds_char*)lds;
  if (tid < L_SM / 4) *(u32*)(lds + tid * 4) = 0;
  __syncthreads();
  int bk;
  { const int q = G >> 3, r = G & 7, x = b & 7, i = b >> 3;
    bk = (x < r ? x * (q + 1) : r * (q + 1) + (x - r) * q) + i; }
  const int nb0 = bk * NPB;
  const int ne = RFL(max(0, min(CH, E - b * CH)));
  u32 ed[3], es[3], ee[3], pf1, pf2, pf3 = 0;
  u32x2 epoch;
  const u32x4 s_we = srd(w_e, 258 * 256), s_wh = srd(w_h, 65536);
  {
    const u32x4 s_d = srd(dst + b * CH, ne * 4), s_s = srd(src + b * CH, ne * 4), s_e = srd(ea + b * CH, ne * 2), s_wx = srd(w_x, 256);
    const u32 xi = (b >> 3) & 31;
    asm volatile("s_cmp_lt_u32 %17, 4\n\ts_cbranch_scc1 1f\n\t"
                 "buffer_load_dword %0, %12, %14, 0 offen\n\tbuffer_load_dword %1, %12, %14, 0 offen offset:1024\n\t"
                 "buffer_load_dword %2, %12, %14, 0 offen offset:2048\n\tbuffer_load_dword %3, %12, %15, 0 offen\n\t"
                 "buffer_load_dword %4, %12, %15, 0 offen offset:1024\n\tbuffer_load_dword %5, %12, %15, 0 offen offset:2048\n\t"
                 "buffer_load_ushort %6, %13, %16, 0 offen\n\tbuffer_load_ushort %7, %13, %16, 0 offen offset:512\n\t"
                 "buffer_load_ushort %8, %13, %16, 0 offen offset:1024\n\t"
                 "global_load_dwordx2 %9, %18, %19 sc1\n\t"
                 "buffer_load_dword %10, %23, %20, 0 offen\n\tbuffer_load_dword %11, %24, %21, 0 offen\n\t"
                 "s_branch 2f\n"
                 "1:\n\tglobal_load_dwordx2 %9, %18, %19 sc1\n\t"
                 "buffer_load_ushort %6, %25, %20, %26 offen\n\tbuffer_load_ushort %7, %25, %20, %26 offen offset:256\n\t"
                 "buffer_load_ushort %8, %25, %22, 0 offen\n"
                 "2:"
                 : "=&v"(ed[0]), "=&v"(ed[1]), "=&v"(ed[2]), "=&v"(es[0]), "=&v"(es[1]), "=&v"(es[2]), "=&v"(ee[0]), "=&v"(ee[1]), "=&v"(ee[2]),
                   "=&v"(epoch), "=&v"(pf1), "=&v"(pf2)
                 : "v"((tid & 255) * 4), "v"((tid & 255) * 2), "s"(s_d), "s"(s_s), "s"(s_e), "s"(wv), "v"(0u), "s"(ws + WS_EP),
                   "s"(s_we), "s"(s_wh), "s"(s_wx), "v"(tid - 256 < 8u ? (xi * 8 + tid - 256) * 128 : OOB),
                   "v"(tid - 320 < 16u ? (xi * 16 + tid - 320) * 128 : OOB), "v"((tid & 127) * 2), "s"(65536u) : "memory", "scc");
  }
  {
    const u32 k = lane & 3, o = k == 0 ? (u32)WS_GR + b * 2048 : k == 1 ? (u32)WS_BT + nb0 * 256 : k == 2 ? (u32)WS_REC + b * (CH * 16) : (u32)WS_FL;
    const u32x4 s_ws = srd(ws + WS_SLOT0, 4 * (u32)WS_SLOT);
    asm volatile("s_cmp_lg_u32 %3, 7\n\ts_cbranch_scc1 1f\n\tbuffer_load_dword %0, %1, %2, 0 offen sc1\n1:"
                 : "=&v"(pf3) : "v"(lane < 16 ? (lane >> 2) * (u32)WS_SLOT + o : OOB), "s"(s_ws), "s"(wv) : "memory", "scc");
  }
  const int r = lane & 31, hh = lane >> 5;
  const int rh = (r >> 2) & 1, rp = r >> 4, rj = (r & 3) | (((r >> 3) & 1) << 2);
  f32x16 aA0, aA1, aB0, aB1, zero;
  u32x4 sel0, sel1;
  auto setup = [&]() {
#pragma unroll
    for (int i = 0; i < 16; i++) zero[i] = 0.f;
    u32x4 z4 = {0, 0, 0, 0};
    PIN(z4);
    aA0 = mfma32(z4, z4, zero); aA1 = mfma32(z4, z4, zero); aB0 = mfma32(z4, z4, zero); aB1 = mfma32(z4, z4, zero);
    const u32 one = rh == hh ? 0x3f80u << (rp * 16) : 0u;
#pragma unroll
    for (int q = 0; q < 4; q++) { sel0[q] = (rj == q) ? one : 0u; sel1[q] = (rj == 4 + q) ? one : 0u; }
    PIN(aA0); PIN(aA1); PIN(aB0); PIN(aB1); PIN(sel0); PIN(sel1);
  };
  u32 ep_lo, ep_hi;
  u32x4 hb[3][4];
  const u32 wv0 = g4 * 256 + 16 * (c16 ^ (g4 << 2)) + u * 4096, wv1 = wv0 ^ 32;
  if (!role1) {
    const u32x4 s_pos = srd(pos, N * 6), s_h = srd(h, N * 256);
#pragma unroll
    for (int i = 0; i < 4; i++)
      if (i < 3 || u < 3) dma4(s_pos, lane * 16 + u * 4096, lane * 16 + u * 4096, i * 16384, lb + L_POS + i * 16384 + u * 4096);
    dma2(s_h, g4 * 256 + 16 * (c16 ^ ((g4 << 2) | u)) + u * 2048, nb0 * 256, lb + L_HS + u * 2048);
    if (u == 3) dma2(s_h, g4 * 256 + 16 * (c16 ^ (g4 << 2)) + 8192, nb0 * 256, lb + L_HS + 8192);
    dma4(s_we, wv0, wv1, 32768, lb + L_W0 + u * 4096);
    dma4(s_we, wv0, wv1, 32768 + 16384, lb + L_W0 + 16384 + u * 4096);
    u32 wdv, wav, wxv;
    asm volatile("s_waitcnt vmcnt(10)\n\tv_readfirstlane_b32 %0, %5\n\tv_readfirstlane_b32 %1, %6\n\t"
                 "v_mov_b32 %2, %7\n\tv_mov_b32 %3, %8\n\tv_mov_b32 %4, %9"
                 : "=s"(ep_lo), "=s"(ep_hi), "=&v"(wdv), "=&v"(wav), "=&v"(wxv)
                 : "v"(epoch[0]), "v"(epoch[1]), "v"(ee[0]), "v"(ee[1]), "v"(ee[2]) : "memory");
    arrive(lb, 0);
    setup();
    asm volatile("s_waitcnt vmcnt(0)" ::: "memory");
    arrive(lb, 1);
    char* const slot = ws + WS_SLOT0 + (size_t)(RFL(ep_lo) & 3u) * WS_SLOT;
    await(lds, 1, 4);
    STAMP(12);
#pragma unroll
    for (int s = 0; s < 4; s++)
#pragma unroll
      for (int t = 0; t < 3; t++) hb[t][s] = h_frag(lds, t, s, lane);
    f32x4 acc[3][2];
#pragma unroll
    for (int t = 0; t < 3; t++) { acc[t][0] = f32x4{0, 0, 0, 0}; acc[t][1] = f32x4{0, 0, 0, 0}; }
#pragma unroll
    for (int s = 0; s < 4; s++) {
      const u32x4 fa = tr_frag(lds + L_W0, s, lane, 4 * u + (lane & 3), 0), fb = tr_frag(lds + L_W0, s, lane, 4 * u + (lane & 3), 1);
#pragma unroll
      for (int t = 0; t < 3; t++) { acc[t][0] = mfma16(fa, hb[t][s], acc[t][0]); acc[t][1] = mfma16(fb, hb[t][s], acc[t][1]); }
    }
    PIN(acc[2][1]);
    arrive(lb, 3);
    u32x4 bo[3];
#pragma unroll
    for (int t = 0; t < 3; t++) {
      const f32x4 x = acc[t][0] * LOG2E, y = acc[t][1] * LOG2E;
      bo[t] = u32x4{pk_bf16(x[0], x[1]), pk_bf16(x[2], x[3]), pk_bf16(y[0], y[1]), pk_bf16(y[2], y[3])};
    }
    const __amdgpu_buffer_rsrc_t r_bw = rsrc(slot + WS_BT, N * 256);
    auto bstore = [&]() {
#pragma unroll
      for (int t = 0; t < 3; t++)
        __builtin_amdgcn_raw_buffer_store_b128(bo[t], r_bw, (t < 2 || c16 < 8) ? (nb0 + tile_node(t, c16)) * 256 + u * 64 + g4 * 16 : OOB, 0, SC1);
    };
    STAMP(15);
    await(lds, 4, 4);
    dma4(s_we, wv0, wv1, 0, lb + L_W1 + u * 4096);
    dma4(s_we, wv0, wv1, 16384, lb + L_W1 + 16384 + u * 4096);
    await(lds, 3, 4);
    dma4(s_wh, wv0, wv1, 0, lb + L_W0 + u * 4096);
    dma4(s_wh, wv0, wv1, 16384, lb + L_W0 + 16384 + u * 4096);
    bstore();
    {
      const float wd = bf2f(wdv) * LOG2E;
      const u32 wdh = pk_bf16(wd, 0.f);
      const u32 wdp = wdh | (pk_bf16(wd - bf2f(wdh), 0.f) << 16), wap = pk_bf16(bf2f(wav) * LOG2E, 0.f);
#pragma unroll
      for (int i = 0; i < 4; i++) {
        char* kb = lds + L_KB + (i * 256 + tid) * 16;
        *(u32*)(kb + 12) = wdp;
        *(u16*)(kb + 10) = (u16)wap;
      }
      if (tid < D) *(u16*)(lds + L_WX + tid * 2) = (u16)wxv;
    }
    asm volatile("s_waitcnt vmcnt(3)" ::: "memory");
  } else {
    const int t1 = tid - 256;
    const u32x4 z = {0, 0, 0, 0};
#pragma unroll
    for (int i = 0; i < 10; i++) if (i < 9 || L_SM + i * 4096 + t1 * 16 < L_POS) *(u32x4*)(lds + L_SM + i * 4096 + t1 * 16) = z;
    setup();
    asm volatile("s_waitcnt vmcnt(9)" : "+v"(ed[0]), "+v"(ed[1]), "+v"(ed[2]) :: "memory");
    u32 rank[3], ebk[3];
#pragma unroll
    for (int i = 0; i < 3; i++) {
      ebk[i] = ed[i] / NPB;
      rank[i] = 0;
      if (t1 + 256 * i < ne) rank[i] = atomicAdd((u32*)(lds + L_BC) + ebk[i], 1u);
    }
    arrive(lb, 0);
    asm volatile("s_waitcnt vmcnt(2)\n\tv_readfirstlane_b32 %0, %8\n\tv_readfirstlane_b32 %1, %9"
                 : "=s"(ep_lo), "=s"(ep_hi), "+v"(es[0]), "+v"(es[1]), "+v"(es[2]), "+v"(ee[0]), "+v"(ee[1]), "+v"(ee[2])
                 : "v"(epoch[0]), "v"(epoch[1]) : "memory");
    ep_lo = RFL(ep_lo); ep_hi = RFL(ep_hi);
    char* const slot = ws + WS_SLOT0 + (size_t)(ep_lo & 3u) * WS_SLOT;
    const u64 tag = ((((u64)ep_hi << 32) | ep_lo) + 1) & 0xfffffffffffull;
    u32 tg[3];
    rec_tags(tag, tg);
    STAMP(14);
    await(lds, 0, 8);
    STAMP(13);
    const u32x4 c4 = *(const u32x4*)(lds + L_BC + lane * 16);
    const u32 cown = *(const u32*)(lds + L_BC + t1 * 4);
    u32x2 pd[3], ps[3];
#pragma unroll
    for (int i = 0; i < 3; i++) {
      const u32* qd = (const u32*)(lds + L_POS + ((ed[i] * 6) & ~3u));
      const u32* qs = (const u32*)(lds + L_POS + ((es[i] * 6) & ~3u));
      pd[i] = u32x2{qd[0], qd[1]}; ps[i] = u32x2{qs[0], qs[1]};
    }
    const u32 pown = *(const u16*)(lds + L_POS + min((nb0 * 3 + t1) * 2, MAXPOS - 2));
    const u32 tot = c4[0] + c4[1] + c4[2] + c4[3];
    const u32 ex = scan64(tot) - tot;
    u32* mine = (u32*)(lds + L_LS + u * 1024);
    *(u32x4*)(mine + lane * 4) = u32x4{ex, ex + c4[0], ex + c4[0] + c4[1], ex + c4[0] + c4[1] + c4[2]};
    __builtin_amdgcn_wave_barrier();
    u32 at[3];
#pragma unroll
    for (int i = 0; i < 3; i++) at[i] = L_RS + (mine[ebk[i]] + rank[i]) * 16;
    const u32 gword = (mine[t1] << 10) | cown;
#pragma unroll
    for (int i = 0; i < 3; i++) {
      const u32 shd = (ed[i] & 1u) * 16, shs = (es[i] & 1u) * 16;
      const u32 dxy = __builtin_amdgcn_alignbit(pd[i][1], pd[i][0], shd), sxy = __builtin_amdgcn_alignbit(ps[i][1], ps[i][0], shs);
      const u32 xy = pk_bf16(bf2f(dxy) - bf2f(sxy), bfhi(dxy) - bfhi(sxy));
      const u32 zz = pk_bf16(bf2f(pd[i][1] >> shd) - bf2f(ps[i][1] >> shs), 0.f);
      u32x4 rc = {es[i] | ((ed[i] - ebk[i] * NPB) << 14) | tg[0], ee[i] | tg[1], xy, zz | tg[2]};
      PIN(rc);
      if (t1 + 256 * i < ne) *(u32x4*)(lds + at[i]) = rc;
    }
    if (t1 < NPB * 3) *(u16*)(lds + L_PD + t1 * 2) = (u16)pown;
    arrive(lb, 2);
    asm volatile("s_waitcnt vmcnt(0)" :: "v"(pf1), "v"(pf2), "v"(pf3) : "memory");
    await(lds, 2, 4);
    const __amdgpu_buffer_rsrc_t r_rw = rsrc(slot + WS_REC + (size_t)b * CH * 16, CH * 16);
    u32x4 rv[3];
#pragma unroll
    for (int i = 0; i < 3; i++) rv[i] = *(const u32x4*)(lds + L_RS + (t1 + 256 * i) * 16);
#pragma unroll
    for (int i = 0; i < 3; i++) __builtin_amdgcn_raw_buffer_store_b128(rv[i], r_rw, t1 * 16, i * 4096, SC1);
    arrive(lb, 4);
    __hip_atomic_store((gu64*)(slot + WS_GR) + (size_t)b * MAXG + t1, (tag << 20) | gword, RLX_AGENT);
    STAMP(2);
#pragma unroll
    for (int i = 0; i < 5; i++) { const int o = L_POS + i * 4096 + t1 * 16; if (i < 4 || o < L_SRC) *(u32x4*)(lds + o) = z; }
    *(u32x4*)(lds + L_SRC + t1 * 16) = u32x4{OOB, OOB, OOB, OOB};
    if (t1 < 16) *(u32*)(lds + L_SRC + 4096 + t1 * 4) = OOB;
    await(lds, 1, 4);
#pragma unroll
    for (int s = 0; s < 4; s++)
#pragma unroll
      for (int t = 0; t < 3; t++) hb[t][s] = h_frag(lds, t, s, lane);
  }
  STAMP(3);
  __syncthreads();
  ep_lo = RFL(ep_lo); ep_hi = RFL(ep_hi);
  const u64 ep = ((u64)ep_hi << 32) | ep_lo, tag = (ep + 1) & 0xfffffffffffull;
  char* const slot = ws + WS_SLOT0 + (size_t)(ep_lo & 3u) * WS_SLOT;
  const __amdgpu_buffer_rsrc_t r_bt = rsrc(slot + WS_BT, N * 256), r_rec = rsrc(slot + WS_REC, G * CH * 16);
  u32 tg[3];
  rec_tags(tag, tg);
  f32x4 cp[3];
  u32 pw[4] = {0, 0, 0, 0};
  {
    const int cs = wv * 32 + (lane & 31), csc = cs < G ? cs : G - 1;
    gu64* const gp = (gu64*)(slot + WS_GR) + (size_t)csc * MAXG + bk;
    gu64* const fp = (gu64*)(slot + WS_FL) + csc;
    u64 gv = __hip_atomic_load(gp, RLX_AGENT);
    SB();
    {
      f32x4 acc[3] = {f32x4{0, 0, 0, 0}, f32x4{0, 0, 0, 0}, f32x4{0, 0, 0, 0}};
#pragma unroll
      for (int s = 0; s < 4; s++) {
        const u32x4 fw = tr_frag(lds + L_W1, s, lane, 2 * wv + ((lane & 3) >> 1), lane & 1);
#pragma unroll
        for (int t = 0; t < 3; t++) acc[t] = mfma16(hb[t][s], fw, acc[t]);
      }
      char* kb = lds + L_KB + (16 * wv + c16) * 16;
#pragma unroll
      for (int t = 0; t < 2; t++) {
        const f32x4 a = acc[t] * LOG2E;
        *(u32x2*)(kb + (4 * t + g4) * 2048) = u32x2{pk_bf16(a[0], a[1]), pk_bf16(a[2], a[3])};
      }
      if (g4 < 2) {
#pragma unroll
        for (int i = 0; i < 4; i++) *(u16*)(kb + (4 * g4 + i) * 2048 + 8) = (u16)pk_bf16(acc[2][i] * LOG2E, 0.f);
      }
    }
    SB();
    if (!role1) {
      asm volatile("s_waitcnt vmcnt(1)" ::: "memory");
      if (lane == 0 && __hip_atomic_fetch_add((u32*)(lds + L_SY) + 5, 1u, __ATOMIC_RELAXED, __HIP_MEMORY_SCOPE_WORKGROUP) == 3)
        __hip_atomic_store((gu64*)(slot + WS_FL) + b, tag, RLX_AGENT);
    }
    STAMP(4);
    for (u32 spins = 0; !__all((gv >> 20) == tag) && spins < (1u << 20); spins++) {
      __builtin_amdgcn_s_sleep(4);
      if ((gv >> 20) != tag) gv = __hip_atomic_load(gp, RLX_AGENT);
    }
    STAMP(5);
    const bool on = lane < 32 && cs < G;
    const u32 cnt = on ? ((u32)gv & 1023u) : 0u, st = ((u32)gv >> 10) & 1023u;
    const u32 inc = scan64(cnt);
    const u32 total = __builtin_amdgcn_readlane(inc, 63);
    u32* list = (u32*)(lds + L_LS + wv * 768);
    const u32 base = (cs * CH + st) * 16, p0 = inc - cnt;
    const u32 lim = min(cnt, p0 < 128 ? 128 - p0 : 0u);
#pragma unroll
    for (u32 k = 0; k < 6; k++) list[k < lim ? p0 + k : 128 + lane] = base + k * 16;
    if (__any(lim > 6)) {
#pragma unroll 1
      for (u32 k = 6; k < lim; k++) list[p0 + k] = base + k * 16;
    }
    __builtin_amdgcn_wave_barrier();
    {
      const u32 a0 = list[lane], a1 = list[64 + lane];
      const bool v0 = lane < total, v1 = 64 + lane < total;
      u32x4 r0 = __builtin_amdgcn_raw_buffer_load_b128(r_rec, v0 ? a0 : OOB, 0, SC1);
      u32x4 r1 = __builtin_amdgcn_raw_buffer_load_b128(r_rec, v1 ? a1 : OOB, 0, SC1);
      if (on) *(u32*)(lds + L_BC + cs * 4) = (u32)gv & 0xfffffu;
      SB();
#pragma unroll
      for (int t = 0; t < 3; t++) cp[t] = f32x4{0, 0, 0, 0};
#pragma unroll
      for (int s = 0; s < 4; s++) {
        const u32x4 f1 = tr_frag(lds + L_W0, s, lane, 2 * wv + ((lane & 3) >> 1), lane & 1);
#pragma unroll
        for (int t = 0; t < 3; t++) cp[t] = mfma16(f1, hb[t][s], cp[t]);
      }
#pragma unroll
      for (int t = 0; t < 3; t++) { cp[t] *= LOG2E; PIN(cp[t]); }
      SB();
      u64 fv = __hip_atomic_load(fp, RLX_AGENT);
      STAMP(6);
      r0 = settle(r0, v0, r_rec, a0, tg);
      r1 = settle(r1, v1, r_rec, a1, tg);
      STAMP(7);
      u32 k0 = 0, k1 = 0;
      if (v0) k0 = take_rank(lds, r0);
      if (v1) k1 = take_rank(lds, r1);
      for (u32 spins = 0; !__all(fv == tag) && spins < (1u << 20); spins++) {
        __builtin_amdgcn_s_sleep(4);
        fv = __hip_atomic_load(fp, RLX_AGENT);
      }
      __builtin_amdgcn_s_waitcnt(0x0f70);
      pw[0] = __builtin_amdgcn_raw_buffer_load_b32(r_bt, v0 && k0 < PFR ? (r0[0] & 0x3fffu) << 8 : OOB, 0, SC1);
      pw[1] = __builtin_amdgcn_raw_buffer_load_b32(r_bt, v0 && k0 < PFR ? ((r0[0] & 0x3fffu) << 8) + 128 : OOB, 0, SC1);
      pw[2] = __builtin_amdgcn_raw_buffer_load_b32(r_bt, v1 && k1 < PFR ? (r1[0] & 0x3fffu) << 8 : OOB, 0, SC1);
      pw[3] = __builtin_amdgcn_raw_buffer_load_b32(r_bt, v1 && k1 < PFR ? ((r1[0] & 0x3fffu) << 8) + 128 : OOB, 0, SC1);
      SB();
      if (v0) put_rec(lds, r0, k0);
      if (v1) put_rec(lds, r1, k1);
    }
    if (__builtin_expect(total > 128, 0)) {
#pragma unroll 1
      for (u32 done = 128; done < total; done += 64) {
        if (__hip_atomic_load((u32*)(lds + L_CNT + 32), __ATOMIC_RELAXED, __HIP_MEMORY_SCOPE_WORKGROUP)) break;
        __builtin_amdgcn_wave_barrier();
#pragma unroll 1
        for (u32 k = p0 < done ? done - p0 : 0u; k < cnt && p0 + k < done + 64; k++) list[p0 + k - done] = base + k * 16;
        __builtin_amdgcn_wave_barrier();
        const bool v0 = done + lane < total;
        const u32 a0 = list[lane];
        u32x4 r0 = __builtin_amdgcn_raw_buffer_load_b128(r_rec, v0 ? a0 : OOB, 0, SC1);
        r0 = settle(r0, v0, r_rec, a0, tg);
        if (v0) put_rec(lds, r0, take_rank(lds, r0));
      }
      __builtin_amdgcn_s_waitcnt(0x0f70);
    }
  }
  STAMP(9);
  __syncthreads();
  STAMP(8);
  const u32* wcnt = (const u32*)(lds + L_CNT);
  const u32 ci = wcnt[lane >> 3], cj = wcnt[lane & 7];
  const int ovf = RFL(wcnt[8]);
  auto load_wh2 = [&]() {
    dma4(s_wh, wv0 + (wv >> 2) * 16384, wv1 + (wv >> 2) * 16384, 32768, lb + L_W0 + wv * 4096);
  };
  u32x4 v[8], wxf[8];
  int win;
  {
    const u64 gt = __ballot(cj * 8 + (lane & 7) > ci * 8 + (lane >> 3));
    const u32 rk = __builtin_popcount((u32)(gt >> (lane & 56)) & 0xffu);
    const int wl = __builtin_ctzll(__ballot(rk == (u32)(wv < 4 ? wv : 11 - wv)));
    win = wl >> 3;
    const int cntw = ovf ? 1 : __builtin_amdgcn_readlane(ci, wl);
    const int nsteps = (cntw + 15) >> 4;
    const u32 lo = r * 4;
    u32 srca = L_SRC + win * CAPW * 4 + hh * 32;
    u32 GA[8], GB[8], off[8];
    const bool cfon = rp == hh;
    u32 cfa = cfon ? (L_CF + (win * CAPW + 8 * rh + rj) * 16) : L_ZERO;
    const u32 cfs = cfon ? 256u : 0u;
    u32 sma = L_SM + (win * CAPW + 8 * hh + (c16 >> 2)) * 40 + (g4 & 1) * 32 + (lane & 3) * 8;
    u32x4 cf, kbA, kbB, sf = {0, 0, 0, 0}, mB0 = {0, 0, 0, 0}, mB1 = {0, 0, 0, 0}, mA0, mA1;
    f32x16 yA, yB;
    {
      const u32x4 i0 = *(const u32x4*)(lds + srca), i1 = *(const u32x4*)(lds + srca + 16);
      const u32x4 i2 = *(const u32x4*)(lds + srca + 64), i3 = *(const u32x4*)(lds + srca + 80);
      cf = *(const u32x4*)(lds + cfa);
      kbA = *(const u32x4*)(lds + L_KB + win * 2048 + (2 * r + hh) * 16);
      kbB = *(const u32x4*)(lds + L_KB + win * 2048 + (64 + 2 * r + hh) * 16);
      srca += 128;
      SB();
#pragma unroll
      for (int j = 0; j < 4; j++) { off[j] = i0[j] + lo; off[4 + j] = i1[j] + lo; }
#pragma unroll
      for (int j = 0; j < 8; j++) GA[j] = __builtin_amdgcn_raw_buffer_load_b32(r_bt, off[j], 0, SC1);
      SB();
#pragma unroll
      for (int j = 0; j < 8; j++) GB[j] = __builtin_amdgcn_raw_buffer_load_b32(r_bt, off[j] + 128, 0, SC1);
      SB();
      asm volatile("" :: "v"(pw[0]), "v"(pw[1]), "v"(pw[2]), "v"(pw[3]));
      yA = mfma32(cf, kbA, zero);
#pragma unroll
      for (int j = 0; j < 4; j++) { off[j] = i2[j] + lo; off[4 + j] = i3[j] + lo; }
      yA = mfma32(sel0, u32x4{GA[0], GA[1], GA[2], GA[3]}, yA);
      yA = mfma32(sel1, u32x4{GA[4], GA[5], GA[6], GA[7]}, yA);
      PIN(yA);
      SB();
#pragma unroll
      for (int j = 0; j < 8; j++) GA[j] = __builtin_amdgcn_raw_buffer_load_b32(r_bt, off[j], 0, SC1);
      SB();
    }
#define MF(d, a, bb, c) do { SB(); d = mfma32(a, bb, c); PIN(d); SB(); } while (0)
#define SA(y, i, j) do { p_ = f32x2{__builtin_amdgcn_exp2f(-y[i]), __builtin_amdgcn_exp2f(-y[(i) + 1])}; \
                         q_ = f32x2{__builtin_amdgcn_exp2f(-y[j]), __builtin_amdgcn_exp2f(-y[(j) + 1])}; SB(); p_ += 1.0f; q_ += 1.0f; SB(); } while (0)
#define SBB(y, i, j, o0, o1) do { p_ = f32x2{__builtin_amdgcn_rcpf(p_[0]), __builtin_amdgcn_rcpf(p_[1])}; \
                         q_ = f32x2{__builtin_amdgcn_rcpf(q_[0]), __builtin_amdgcn_rcpf(q_[1])}; SB(); \
                         p_ *= f32x2{y[i], y[(i) + 1]}; q_ *= f32x2{y[j], y[(j) + 1]}; SB(); \
                         u32 t0_ = pk_bf16(p_[0], p_[1]), t1_ = pk_bf16(q_[0], q_[1]); \
                         asm volatile("" : "+v"(t0_), "+v"(t1_)); o0 = t0_; o1 = t1_; SB(); } while (0)
#define S4(y, i, j, o0, o1) do { SA(y, i, j); SBB(y, i, j, o0, o1); } while (0)
#define LD2(Gx, j, add) do { Gx[j] = __builtin_amdgcn_raw_buffer_load_b32(r_bt, off[j] + (add), 0, SC1); \
                             Gx[(j) + 1] = __builtin_amdgcn_raw_buffer_load_b32(r_bt, off[(j) + 1] + (add), 0, SC1); SB(); } while (0)
    f32x2 p_, q_;
    for (int s = 1; s < nsteps; s++) {
      { const int left = nsteps - s; if (left >= 4) __builtin_amdgcn_s_setprio(3); else if (left == 3) __builtin_amdgcn_s_setprio(2); else if (left == 2) __builtin_amdgcn_s_setprio(1); else __builtin_amdgcn_s_setprio(0); }
      MF(yB, cf, kbB, zero);
      S4(yA, 0, 2, mA0[0], mA0[1]);
      MF(yB, sel0, (u32x4{GB[0], GB[1], GB[2], GB[3]}), yB);
      cfa += cfs;
      cf = *(const u32x4*)(lds + cfa);
      SA(yA, 4, 6);
      LD2(GB, 0, 128);
      SBB(yA, 4, 6, mA0[2], mA0[3]);
      LD2(GB, 2, 128);
      MF(yB, sel1, (u32x4{GB[4], GB[5], GB[6], GB[7]}), yB);
      SA(yA, 8, 10);
      LD2(GB, 4, 128);
      MF(aB0, mB0, sf, aB0);
      SBB(yA, 8, 10, mA1[0], mA1[1]);
      LD2(GB, 6, 128);
      const u32x4 i0 = *(const u32x4*)(lds + srca), i1 = *(const u32x4*)(lds + srca + 16);
      srca += 64;
      SA(yA, 12, 14);
      MF(aB1, mB1, sf, aB1);
      sf = s_frag(lds + sma);
      sma += 640;
      SBB(yA, 12, 14, mA1[2], mA1[3]);
      MF(yA, cf, kbA, zero);
      S4(yB, 0, 2, mB0[0], mB0[1]);
      MF(yA, sel0, (u32x4{GA[0], GA[1], GA[2], GA[3]}), yA);
#pragma unroll
      for (int j = 0; j < 4; j++) { off[j] = i0[j] + lo; off[4 + j] = i1[j] + lo; }
      SA(yB, 4, 6);
      LD2(GA, 0, 0);
      SBB(yB, 4, 6, mB0[2], mB0[3]);
      LD2(GA, 2, 0);
      MF(yA, sel1, (u32x4{GA[4], GA[5], GA[6], GA[7]}), yA);
      SA(yB, 8, 10);
      LD2(GA, 4, 0);
      MF(aA0, mA0, sf, aA0);
      SBB(yB, 8, 10, mB1[0], mB1[1]);
      LD2(GA, 6, 0);
      SA(yB, 12, 14);
      MF(aA1, mA1, sf, aA1);
      SBB(yB, 12, 14, mB1[2], mB1[3]);
    }
    {
      const int rem = cntw - 16 * (nsteps - 1);
      load_wh2();
      MF(yB, cf, kbB, zero);
      mA0 = u32x4{0, 0, 0, 0}; mA1 = u32x4{0, 0, 0, 0};
      S4(yA, 0, 8, mA0[0], mA1[0]);
      MF(yB, sel0, (u32x4{GB[0], GB[1], GB[2], GB[3]}), yB);
      if (rem > 4) S4(yA, 2, 10, mA0[1], mA1[1]);
      MF(yB, sel1, (u32x4{GB[4], GB[5], GB[6], GB[7]}), yB);
      if (rem > 8) S4(yA, 4, 12, mA0[2], mA1[2]);
      MF(aB0, mB0, sf, aB0);
      if (rem > 12) S4(yA, 6, 14, mA0[3], mA1[3]);
      MF(aB1, mB1, sf, aB1);
      sf = s_frag(lds + sma);
      mB0 = u32x4{0, 0, 0, 0}; mB1 = u32x4{0, 0, 0, 0};
      S4(yB, 0, 8, mB0[0], mB1[0]);
      MF(aA0, mA0, sf, aA0);
      if (rem > 4) S4(yB, 2, 10, mB0[1], mB1[1]);
      MF(aA1, mA1, sf, aA1);
      if (rem > 8) S4(yB, 4, 12, mB0[2], mB1[2]);
      if (rem > 12) S4(yB, 6, 14, mB0[3], mB1[3]);
      MF(aB0, mB0, sf, aB0);
      MF(aB1, mB1, sf, aB1);
    }
#pragma unroll
    for (int q8 = 0; q8 < 8; q8++) {
      const f32x16& a0 = q8 < 4 ? aA0 : aB0;
      const f32x16& a1 = q8 < 4 ? aA1 : aB1;
#pragma unroll
      for (int q = 0; q < 4; q++) v[q8][q] = pk_bf16(a0[4 * (q8 & 3) + q], a1[4 * (q8 & 3) + q]);
    }
    if (r < 20 && (r & 3) == 0 && !ovf) {
      char* mi = lds + mi_row(r * 2 + win) + 16 * hh;
#pragma unroll
      for (int q8 = 0; q8 < 8; q8++) *(u32x4*)(mi + 32 * q8) = v[q8];
    }
  }
  if (__builtin_expect(ovf, 0)) {
    float* macc = (float*)(lds + L_SM);
    float* pacc = (float*)(lds + L_SM + NPB * D * 4);
    __syncthreads();
    for (int i = tid; i < NPB * D + NPB * 4; i += NT) macc[i] = 0.f;
    __syncthreads();
    const u32 wxp = *(const u32*)(lds + L_WX + lane * 4);
    const float wx0 = bf2f(wxp), wx1 = bfhi(wxp);
    const u32x4 kc0 = *(const u32x4*)(lds + L_KB + (2 * lane) * 16), kc1 = *(const u32x4*)(lds + L_KB + (2 * lane + 1) * 16);
    const float wd0 = bf2f(kc0[3]) + bfhi(kc0[3]), wd1 = bf2f(kc1[3]) + bfhi(kc1[3]);
    const float wa0 = bfhi(kc0[2]), wa1 = bfhi(kc1[2]);
    u32 rot = 0;
    for (int c = 0; c < G; c++) {
      const u32 oo = RFL(*(const u32*)(lds + L_BC + c * 4));
      const u32 s0 = oo >> 10, n = oo & 1023u;
      for (u32 k = (wv - rot) & 7u; k < n; k += 8) {
        u32x4 rr = __builtin_amdgcn_raw_buffer_load_b128(r_rec, (c * CH + s0 + k) * 16, 0, SC1);
        rr = settle(rr, true, r_rec, (c * CH + s0 + k) * 16, tg);
        const u32 nl = (rr[0] >> 14) & 0x3fu, sl = nl >> 3, wn = nl & 7u;
        const u32 bv = __builtin_amdgcn_raw_buffer_load_b32(r_bt, ((rr[0] & 0x3fffu) << 8) + lane * 4, 0, SC1);
        const float d2 = bfhi(dist2(rr[2], rr[3])), eav = bf2f(rr[1]);
        const float a0 = bf2f(*(const u16*)(lds + L_KB + wn * 2048 + (2 * lane) * 16 + sl * 2));
        const float a1 = bf2f(*(const u16*)(lds + L_KB + wn * 2048 + (2 * lane + 1) * 16 + sl * 2));
        const float y0 = a0 + bf2f(bv) + d2 * wd0 + eav * wa0, y1 = a1 + bfhi(bv) + d2 * wd1 + eav * wa1;
        const u32 mp = silu2(y0, y1);
        const float m0 = bf2f(mp), m1 = bfhi(mp);
        atomicAdd(&macc[nl * D + 2 * lane], m0);
        atomicAdd(&macc[nl * D + 2 * lane + 1], m1);
        float csv = m0 * wx0 + m1 * wx1;
#pragma unroll
        for (int o = 32; o > 0; o >>= 1) csv += __shfl_xor(csv, o, 64);
        if (lane < 3) atomicAdd(&pacc[nl * 4 + lane], csv * (lane == 0 ? bf2f(rr[2]) : lane == 1 ? bfhi(rr[2]) : bf2f(rr[3])));
      }
      rot += n;
    }
    __syncthreads();
    for (int i = tid; i < NPB * D / 2; i += NT) {
      const int n = i >> 6, f = (i & 63) * 2;
      *(u32*)(lds + mi_row(n) + f * 2) = pk_bf16(macc[n * D + f], macc[n * D + f + 1]);
    }
    if (tid < NPB * 3) {
      const int n = tid / 3, c = tid - n * 3;
      if (nb0 + n < N) out[(size_t)N * D + (size_t)nb0 * 3 + tid] = (u16)pk_bf16(bf2f(*(const u16*)(lds + L_PD + tid * 2)) + LN2 * pacc[n * 4 + c], 0.f);
    }
  }
  u32x2 hr[3];
  u32x4 f2[4];
#pragma unroll
  for (int t = 0; t < 3; t++) {
    const int n = tile_node(t, c16);
    hr[t] = *(const u32x2*)(lds + L_HS + n * 256 + 16 * ((2 * wv + (g4 >> 1)) ^ hswz(n)) + 8 * (g4 & 1));
  }
#pragma unroll
  for (int q8 = 0; q8 < 8; q8++) wxf[q8] = *(const u32x4*)(lds + L_WX + (16 * q8 + 8 * hh) * 2);
  const int kind = lane & 3, pnl = (lane >> 2) * 8 + win;
  const bool pon = kind != 0 && lane < 20 && !ovf;
  const u32 p0 = *(const u16*)(lds + L_PD + (pon ? pnl * 6 + kind * 2 - 2 : 0));
  const int mia = mi_row(tile_node(0, c16)) + g4 * 16, mib = mi_row(tile_node(2, c16)) + g4 * 16;
  asm volatile("s_waitcnt vmcnt(0)" ::: "memory");
  STAMP(10);
  __syncthreads();
  {
    u32x4 mb[3][4];
#pragma unroll
    for (int s = 0; s < 4; s++) {
      f2[s] = tr_frag(lds + L_W0, s, lane, 2 * wv + ((lane & 3) >> 1), lane & 1);
      mb[0][s] = *(const u32x4*)(lds + mia + s * 64);
      mb[1][s] = *(const u32x4*)(lds + mia + 8192 + s * 64);
      mb[2][s] = *(const u32x4*)(lds + mib + s * 64);
    }
    SB();
#pragma unroll
    for (int s = 0; s < 4; s++)
#pragma unroll
      for (int t = 0; t < 3; t++) cp[t] = mfma16(f2[s], mb[t][s], cp[t]);
    f32x16 z = zero, z1 = zero;
#pragma unroll
    for (int t = 0; t < 3; t++) {
      const f32x4 y = cp[t];
      f32x4 e;
#pragma unroll
      for (int i = 0; i < 4; i++) e[i] = __builtin_amdgcn_exp2f(-y[i]);
      SB();
      if (t & 1) { z1 = mfma32(wxf[3 * t], v[3 * t], z1); PIN(z1); } else { z = mfma32(wxf[3 * t], v[3 * t], z); PIN(z); }
      SB();
      e += 1.0f;
#pragma unroll
      for (int i = 0; i < 4; i++) e[i] = __builtin_amdgcn_rcpf(e[i]);
      SB();
      if (t & 1) { z = mfma32(wxf[3 * t + 1], v[3 * t + 1], z); PIN(z); } else { z1 = mfma32(wxf[3 * t + 1], v[3 * t + 1], z1); PIN(z1); }
      SB();
      const f32x4 o = f32x4{bf2f(hr[t][0]), bfhi(hr[t][0]), bf2f(hr[t][1]), bfhi(hr[t][1])} + (y * LN2) * e;
      *(u32x2*)(lds + L_SM + tile_node(t, c16) * 272 + wv * 32 + g4 * 8) = u32x2{pk_bf16(o[0], o[1]), pk_bf16(o[2], o[3])};
      SB();
      if (t == 0) { z = mfma32(wxf[2], v[2], z); PIN(z); }
      if (t == 1) { z1 = mfma32(wxf[5], v[5], z1); PIN(z1); }
      SB();
    }
    __syncthreads();
    const __amdgpu_buffer_rsrc_t r_out = rsrc(out, N * 262);
    const u32x4 o0 = *(const u32x4*)(lds + L_SM + (tid >> 4) * 272 + (tid & 15) * 16);
    const u32x4 o1 = *(const u32x4*)(lds + L_SM + (32 + ((tid >> 4) & 7)) * 272 + (tid & 15) * 16);
    const u32 oo = nb0 * 256 + tid * 16;
    __builtin_amdgcn_raw_buffer_store_b128(o0, r_out, oo < N * 256 ? oo : OOB, 0, 0);
    __builtin_amdgcn_raw_buffer_store_b128(o1, r_out, tid < 128 && oo + 8192 < N * 256 ? oo + 8192 : OOB, 0, 0);
    __builtin_amdgcn_raw_buffer_store_b16((u16)pk_bf16(bf2f(p0) + LN2 * (z[0] + z1[0]), 0.f), r_out,
                                          pon ? N * 256 + ((nb0 + pnl) * 3 + kind - 1) * 2 : OOB, 0, 0);
  }
  if (b == 0 && tid == 0) __hip_atomic_store((gu64*)(ws + WS_EP), ep + 1, RLX_AGENT);
  STAMP(11);
}

extern "C" void kernel_launch(void* const* d_in, const int* in_sizes, int n_in, void* d_out, int out_size, void* d_ws,
                              size_t ws_size, hipStream_t stream) {
  const int N = in_sizes[0] / D, E = in_sizes[3];
  const int NB = (N + NPB - 1) / NPB, C = (E + CH - 1) / CH;
  const int G = NB > C ? NB : C;
  k_egnn<<<G, NT, 0, stream>>>((const u16*)d_in[0], (const u16*)d_in[1], (const u16*)d_in[2], (const int*)d_in[3],
                               (const int*)d_in[4], (const u16*)d_in[5], (const u16*)d_in[6], (const u16*)d_in[7],
                               (char*)d_ws, (u16*)d_out, N, E, G);
}
```

```cpp
#include <hip/hip_runtime.h>
#include <hip/hip_bf16.h>

typedef unsigned int u32;
typedef unsigned short u16;
typedef unsigned long long u64;
typedef u32 u32x2 __attribute__((ext_vector_type(2)));
typedef u32 u32x4 __attribute__((ext_vector_type(4)));
typedef short s16x4 __attribute__((ext_vector_type(4)));
typedef short bf16x8 __attribute__((ext_vector_type(8)));
typedef float f32x2 __attribute__((ext_vector_type(2)));
typedef float f32x4 __attribute__((ext_vector_type(4)));
typedef float f32x16 __attribute__((ext_vector_type(16)));
typedef __bf16 bf16x2_t __attribute__((ext_vector_type(2)));
typedef __attribute__((address_space(1))) u64 gu64;
typedef __attribute__((address_space(3))) s16x4 lds_s16x4;
typedef __attribute__((address_space(3))) char lds_char;
#define DI __device__ __forceinline__
#define RLX_AGENT __ATOMIC_RELAXED, __HIP_MEMORY_SCOPE_AGENT
#define SC1 16
#define SB() __builtin_amdgcn_sched_barrier(0)
#define PIN(x) asm volatile("" : "+v"(x))
#define RFL(x) __builtin_amdgcn_readfirstlane(x)

constexpr int D = 128;
constexpr int NT = 512;
constexpr int NPB = 40;
constexpr int CH = 640;
constexpr int MAXG = 256;
constexpr int CAPW = 128;
constexpr int MAXPOS = 61440;
#define PFR 16
constexpr float LOG2E = 1.4426950408889634f;
constexpr float LN2 = 0.6931471805599453f;
constexpr u32 OOB = 0xfffff000u;

constexpr size_t WS_EP = 0;
constexpr size_t WS_SLOT0 = (size_t)2 << 20, WS_SLOT = (size_t)6 << 20;
constexpr size_t WS_GR = 0;
constexpr size_t WS_BT = WS_GR + (size_t)MAXG * MAXG * 8;
constexpr size_t WS_REC = WS_BT + (size_t)MAXG * NPB * 256;
constexpr size_t WS_FL = WS_REC + (size_t)MAXG * CH * 16;
static_assert(WS_FL + MAXG * 8 <= WS_SLOT, "slot");

#define STAMP(k) do { } while (0)

DI float bf2f(u32 b) { return __builtin_bit_cast(float, b << 16); }
DI float bfhi(u32 b) { return __builtin_bit_cast(float, b & 0xffff0000u); }
DI u32 pk_bf16(float lo, float hi) {
  f32x2 v = {lo, hi};
  bf16x2_t r = __builtin_convertvector(v, bf16x2_t);
  return __builtin_bit_cast(u32, r);
}
DI f32x4 mfma16(u32x4 a, u32x4 b, f32x4 c) {
  return __builtin_amdgcn_mfma_f32_16x16x32_bf16(__builtin_bit_cast(bf16x8, a), __builtin_bit_cast(bf16x8, b), c, 0, 0, 0);
}
DI f32x16 mfma32(u32x4 a, u32x4 b, f32x16 c) {
  return __builtin_amdgcn_mfma_f32_32x32x16_bf16(__builtin_bit_cast(bf16x8, a), __builtin_bit_cast(bf16x8, b), c, 0, 0, 0);
}
DI __amdgpu_buffer_rsrc_t rsrc(const void* p, u32 bytes) { return __builtin_amdgcn_make_buffer_rsrc((void*)p, 0, bytes, 0x00020000); }
DI u32x4 srd(const void* p, u32 bytes) { const u64 a = (u64)p; return u32x4{(u32)a, (u32)(a >> 32) & 0xffffu, bytes, 0x00020000u}; }
DI void dma4(u32x4 sd, u32 v0, u32 v1, u32 soff, u32 ldsa) {
  u32 keep;
  asm volatile("s_mov_b32 %0, m0\n\ts_mov_b32 m0, %1\n\ts_nop 0\n\t"
               "buffer_load_dwordx4 %2, %4, %5 offen lds\n\tbuffer_load_dwordx4 %2, %4, %5 offen offset:1024 lds\n\t"
               "buffer_load_dwordx4 %3, %4, %5 offen offset:2048 lds\n\tbuffer_load_dwordx4 %3, %4, %5 offen offset:3072 lds\n\t"
               "s_mov_b32 m0, %0"
               : "=&s"(keep) : "s"(RFL(ldsa)), "v"(v0), "v"(v1), "s"(sd), "s"(soff) : "memory");
}
DI void dma2(u32x4 sd, u32 v0, u32 soff, u32 ldsa) {
  u32 keep;
  asm volatile("s_mov_b32 %0, m0\n\ts_mov_b32 m0, %1\n\ts_nop 0\n\t"
               "buffer_load_dwordx4 %2, %3, %4 offen lds\n\tbuffer_load_dwordx4 %2, %3, %4 offen offset:1024 lds\n\t"
               "s_mov_b32 m0, %0"
               : "=&s"(keep) : "s"(RFL(ldsa)), "v"(v0), "s"(sd), "s"(soff) : "memory");
}
DI u32 scan64(u32 x) {
  x += __builtin_amdgcn_update_dpp(0u, x, 0x111, 0xf, 0xf, false);
  x += __builtin_amdgcn_update_dpp(0u, x, 0x112, 0xf, 0xf, false);
  x += __builtin_amdgcn_update_dpp(0u, x, 0x114, 0xf, 0xf, false);
  x += __builtin_amdgcn_update_dpp(0u, x, 0x118, 0xf, 0xf, false);
  x += __builtin_amdgcn_update_dpp(0u, x, 0x142, 0xa, 0xf, false);
  x += __builtin_amdgcn_update_dpp(0u, x, 0x143, 0xc, 0xf, false);
  return x;
}

constexpr int L_ZERO = 0;
constexpr int L_CNT = L_ZERO + 16;
constexpr int L_BC = L_CNT + 48;
constexpr int L_SY = L_BC + 1024;
constexpr int L_SM = L_SY + 32;
constexpr int L_CF = L_SM + 8 * 8 * 640;
constexpr int L_SRC = L_CF + 8 * CAPW * 16;
constexpr int L_KB = L_SRC + 8 * CAPW * 4 + 64;
constexpr int L_W1 = L_KB + 8 * D * 16;
constexpr int L_W0 = L_W1 + 32768;
constexpr int L_HS = L_W0 + 32768;
constexpr int L_WX = L_HS + NPB * 256;
constexpr int L_PD = L_WX + 256;
constexpr int L_LS = L_PD + 256;
constexpr int L_END = L_LS + 8 * 768;
constexpr int L_RS = L_W0 - CH * 16;
constexpr int L_POS = L_RS - MAXPOS;
static_assert(L_END <= 160 * 1024, "lds");
static_assert(L_POS >= L_SM + 36864 && L_POS <= L_CF && (L_POS % 16) == 0, "pos table");
static_assert((L_SM % 16) == 0 && (L_BC % 16) == 0 && (L_SRC % 16) == 0 && (L_KB % 16) == 0 && (L_W0 % 16) == 0 && (L_HS % 16) == 0 && (L_WX % 16) == 0 && (L_LS % 16) == 0, "align");

DI int tile_node(int t, int c) { return t < 2 ? ((c & 3) * 8 + 4 * t + (c >> 2)) : (32 + (c & 7)); }
DI int mi_row(int nl) { return L_KB + (nl & 7) * 2048 + (nl & 3) * 64 + (nl >> 3) * 272; }
DI int wswz(int k) { return ((k & 3) << 2) | ((k >> 2) & 2); }
DI int hswz(int n) { return ((n & 3) << 2) | ((n >> 3) & 3); }
DI u32x4 tr_frag(const char* img, int s, int lane, int ch, int hf) {
  const int k0 = 8 * (lane >> 4) + ((lane & 15) >> 2);
  const char* p = img + (32 * s + k0) * 256 + 16 * (ch ^ wswz(k0)) + 8 * hf;
  const s16x4 a = __builtin_amdgcn_ds_read_tr16_b64_v4i16((lds_s16x4*)p);
  const s16x4 b = __builtin_amdgcn_ds_read_tr16_b64_v4i16((lds_s16x4*)(p + 1024));
  const u32x2 x = __builtin_bit_cast(u32x2, a), y = __builtin_bit_cast(u32x2, b);
  return u32x4{x[0], x[1], y[0], y[1]};
}
DI u32x4 s_frag(const char* p) {
  const u32x2 x = __builtin_bit_cast(u32x2, __builtin_amdgcn_ds_read_tr16_b64_v4i16((lds_s16x4*)p));
  const u32x2 y = __builtin_bit_cast(u32x2, __builtin_amdgcn_ds_read_tr16_b64_v4i16((lds_s16x4*)(p + 160)));
  return u32x4{x[0], x[1], y[0], y[1]};
}
DI u32x4 h_frag(const char* lds, int t, int s, int lane) {
  const int n = tile_node(t, lane & 15);
  return *(const u32x4*)(lds + L_HS + n * 256 + 16 * ((4 * s + (lane >> 4)) ^ hswz(n)));
}
DI u32 dist2(u32 w2, u32 w3) {
  const float dx = bfhi(w2), dy = bf2f(w3), dz = bfhi(w3);
  const u32 q0 = pk_bf16(dx * dx, dy * dy), q1 = pk_bf16(dz * dz, 0.f);
  const float s2 = bf2f(q0) + bfhi(q0) + bf2f(q1);
  return pk_bf16(s2, s2);
}
DI u32 take_rank(char* lds, u32x4 rc) { return atomicAdd((u32*)(lds + L_CNT) + ((rc[0] >> 14) & 7u), 1u); }
DI void put_rec(char* lds, u32x4 rc, u32 rank) {
  const u32 win = (rc[0] >> 14) & 7u, slot = (rc[0] >> 17) & 7u;
  const u64 oh = 0x3f80ull << (slot * 16);
  const u32x4 cf = {slot < 4 ? (u32)oh : 0u, slot < 4 ? (u32)(oh >> 32) : 0u, (slot == 4 ? 0x3f80u : 0u) | (rc[2] << 16), dist2(rc[2], rc[3])};
  if (rank < CAPW) {
    const u32 k = ((rank & 1u) << 3) + ((rank & 15u) >> 1), sp = win * CAPW + (rank & ~15u) + k;
    *(u32*)(lds + L_SRC + sp * 4) = (rc[0] & 0x3fffu) << 8;
    *(u32x4*)(lds + L_CF + sp * 16) = cf;
    *(u32x2*)(lds + L_SM + sp * 40 + slot * 8) = u32x2{0x3f80u | (rc[2] & 0xffff0000u), rc[3]};
  } else *(u32*)(lds + L_CNT + 32) = 1;
}
DI u32x4 settle(u32x4 rc, bool on, __amdgpu_buffer_rsrc_t r, u32 off, u32 tag0, u32 tag1) {
  for (u32 spins = 0; __any(on && ((rc[0] >> 20) != tag0 || rc[1] != tag1)) && spins < (1u << 20); spins++) {
    __builtin_amdgcn_s_sleep(4);
    if (on && ((rc[0] >> 20) != tag0 || rc[1] != tag1)) rc = __builtin_amdgcn_raw_buffer_load_b128(r, off, 0, SC1);
  }
  return rc;
}
DI void arrive(u32 lb, int i) {
  asm volatile("s_mov_b64 exec, 1\n\tds_add_u32 %0, %1 offset:%2\n\ts_mov_b64 exec, -1" :: "v"(lb), "v"(1u), "i"(L_SY + 4 * i) : "memory");
}
DI void await(char* lds, int i, u32 n) {
  asm volatile("" ::: "memory");
  while ((u32)RFL(__hip_atomic_load((u32*)(lds + L_SY) + i, __ATOMIC_RELAXED, __HIP_MEMORY_SCOPE_WORKGROUP)) < n) { }
  asm volatile("" ::: "memory");
}
DI u32 silu2(float a, float b) {
  f32x2 yy = {a, b};
  f32x2 e = {__builtin_amdgcn_exp2f(-a), __builtin_amdgcn_exp2f(-b)};
  e += 1.0f;
  f32x2 rc = {__builtin_amdgcn_rcpf(e[0]), __builtin_amdgcn_rcpf(e[1])};
  yy *= rc;
  return pk_bf16(yy[0], yy[1]);
}

__global__ __launch_bounds__(NT, 2) void k_egnn(const u16* __restrict__ h, const u16* __restrict__ pos,
                                                const u16* __restrict__ ea, const int* __restrict__ src,
                                                const int* __restrict__ dst, const u16* __restrict__ w_e,
                                                const u16* __restrict__ w_x, const u16* __restrict__ w_h,
                                                char* __restrict__ ws, u16* __restrict__ out, int N, int E, int G) {
  __shared__ __attribute__((aligned(16))) char lds[L_END];
  const int tid = threadIdx.x, lane = tid & 63, b = blockIdx.x;
  const int wv = RFL(tid >> 6);
  const int u = wv & 3, c16 = lane & 15, g4 = lane >> 4;
  const bool role1 = wv >= 4;
  const u32 lb = (u32)(size_t)(lds_char*)lds;
  if (tid < L_SM / 4) *(u32*)(lds + tid * 4) = 0;
  __syncthreads();
  const int ne = min(CH, E - b * CH);
  u32 ed[3], es[3], ee[3];
  u32x2 epoch;
  {
    const u32x4 s_d = srd(dst + b * CH, ne * 4), s_s = srd(src + b * CH, ne * 4), s_e = srd(ea + b * CH, ne * 2);
    asm volatile("s_cmp_lt_u32 %15, 4\n\ts_cbranch_scc1 1f\n\t"
                 "buffer_load_dword %0, %10, %12, 0 offen\n\tbuffer_load_dword %1, %10, %12, 0 offen offset:1024\n\t"
                 "buffer_load_dword %2, %10, %12, 0 offen offset:2048\n\tbuffer_load_dword %3, %10, %13, 0 offen\n\t"
                 "buffer_load_dword %4, %10, %13, 0 offen offset:1024\n\tbuffer_load_dword %5, %10, %13, 0 offen offset:2048\n\t"
                 "buffer_load_ushort %6, %11, %14, 0 offen\n\tbuffer_load_ushort %7, %11, %14, 0 offen offset:512\n\t"
                 "buffer_load_ushort %8, %11, %14, 0 offen offset:1024\n"
                 "1:\n\tglobal_load_dwordx2 %9, %16, %17 sc1"
                 : "=&v"(ed[0]), "=&v"(ed[1]), "=&v"(ed[2]), "=&v"(es[0]), "=&v"(es[1]), "=&v"(es[2]), "=&v"(ee[0]), "=&v"(ee[1]), "=&v"(ee[2]), "=&v"(epoch)
                 : "v"((tid & 255) * 4), "v"((tid & 255) * 2), "s"(s_d), "s"(s_s), "s"(s_e), "s"(wv), "v"(0u), "s"(ws + WS_EP) : "memory", "scc");
  }
  int bk;
  { const int q = G >> 3, r = G & 7, x = b & 7, i = b >> 3;
    bk = (x < r ? x * (q + 1) : r * (q + 1) + (x - r) * q) + i; }
  const int nb0 = bk * NPB;
  const int r = lane & 31, hh = lane >> 5;
  const int rh = (r >> 2) & 1, rp = r >> 4, rj = (r & 3) | (((r >> 3) & 1) << 2);
  f32x16 aA0, aA1, aB0, aB1, zero;
  u32x4 sel0, sel1;
  auto setup = [&]() {
#pragma unroll
    for (int i = 0; i < 16; i++) zero[i] = 0.f;
    u32x4 z4 = {0, 0, 0, 0};
    PIN(z4);
    aA0 = mfma32(z4, z4, zero); aA1 = mfma32(z4, z4, zero); aB0 = mfma32(z4, z4, zero); aB1 = mfma32(z4, z4, zero);
    const u32 one = rh == hh ? 0x3f80u << (rp * 16) : 0u;
#pragma unroll
    for (int q = 0; q < 4; q++) { sel0[q] = (rj == q) ? one : 0u; sel1[q] = (rj == 4 + q) ? one : 0u; }
    PIN(aA0); PIN(aA1); PIN(aB0); PIN(aB1); PIN(sel0); PIN(sel1);
  };
  u32 ep_lo, ep_hi;
  u32x4 hb[3][4];
  const u32 wv0 = g4 * 256 + 16 * (c16 ^ (g4 << 2)) + u * 4096, wv1 = wv0 ^ 32;
  if (!role1) {
    const u32x4 s_pos = srd(pos, N * 6), s_h = srd(h, N * 256), s_we = srd(w_e, 258 * 256), s_wh = srd(w_h, 65536);
#pragma unroll
    for (int i = 0; i < 4; i++)
      if (i < 3 || u < 3) dma4(s_pos, lane * 16 + u * 4096, lane * 16 + u * 4096, i * 16384, lb + L_POS + i * 16384 + u * 4096);
    dma2(s_h, g4 * 256 + 16 * (c16 ^ ((g4 << 2) | u)) + u * 2048, nb0 * 256, lb + L_HS + u * 2048);
    if (u == 0) dma2(s_h, g4 * 256 + 16 * (c16 ^ (g4 << 2)) + 8192, nb0 * 256, lb + L_HS + 8192);
    dma4(s_we, wv0, wv1, 32768, lb + L_W0 + u * 4096);
    dma4(s_we, wv0, wv1, 32768 + 16384, lb + L_W0 + 16384 + u * 4096);
    u32 wdv, wav, wxv, pf1, pf2;
    {
      const u32x4 s_wx = srd(w_x, 256);
      const u32 xi = (b >> 3) & 31;
      asm volatile("buffer_load_ushort %0, %5, %6, %7 offen\n\tbuffer_load_ushort %1, %5, %6, %7 offen offset:256\n\t"
                   "buffer_load_ushort %2, %5, %8, 0 offen\n\tbuffer_load_dword %3, %9, %6, 0 offen\n\t"
                   "buffer_load_dword %4, %10, %11, 0 offen"
                   : "=&v"(wdv), "=&v"(wav), "=&v"(wxv), "=&v"(pf1), "=&v"(pf2)
                   : "v"((tid & 127) * 2), "s"(s_we), "s"(65536u), "s"(s_wx), "v"(tid < 8 ? (xi * 8 + tid) * 128 : OOB),
                     "v"(tid - 64 < 16u ? (xi * 16 + tid - 64) * 128 : OOB), "s"(s_wh) : "memory");
    }
    asm volatile("s_waitcnt vmcnt(15)\n\tv_readfirstlane_b32 %0, %2\n\tv_readfirstlane_b32 %1, %3"
                 : "=s"(ep_lo), "=s"(ep_hi) : "v"(epoch[0]), "v"(epoch[1]) : "memory");
    arrive(lb, 0);
    setup();
    asm volatile("s_waitcnt vmcnt(5)" ::: "memory");
    arrive(lb, 1);
    char* const slot = ws + WS_SLOT0 + (size_t)(RFL(ep_lo) & 3u) * WS_SLOT;
    await(lds, 1, 4);
    STAMP(12);
#pragma unroll
    for (int s = 0; s < 4; s++)
#pragma unroll
      for (int t = 0; t < 3; t++) hb[t][s] = h_frag(lds, t, s, lane);
    f32x4 acc[3][2];
#pragma unroll
    for (int t = 0; t < 3; t++) { acc[t][0] = f32x4{0, 0, 0, 0}; acc[t][1] = f32x4{0, 0, 0, 0}; }
#pragma unroll
    for (int s = 0; s < 4; s++) {
      const u32x4 fa = tr_frag(lds + L_W0, s, lane, 4 * u + (lane & 3), 0), fb = tr_frag(lds + L_W0, s, lane, 4 * u + (lane & 3), 1);
#pragma unroll
      for (int t = 0; t < 3; t++) { acc[t][0] = mfma16(fa, hb[t][s], acc[t][0]); acc[t][1] = mfma16(fb, hb[t][s], acc[t][1]); }
    }
    asm volatile("s_waitcnt vmcnt(0)" : "+v"(wdv), "+v"(wav), "+v"(wxv), "+v"(pf1), "+v"(pf2), "+v"(acc[2][1]) :: "memory");
    arrive(lb, 3);
    const __amdgpu_buffer_rsrc_t r_bw = rsrc(slot + WS_BT, N * 256);
#pragma unroll
    for (int t = 0; t < 3; t++) {
      const f32x4 x = acc[t][0] * LOG2E, y = acc[t][1] * LOG2E;
      const u32x4 o = {pk_bf16(x[0], x[1]), pk_bf16(x[2], x[3]), pk_bf16(y[0], y[1]), pk_bf16(y[2], y[3])};
      __builtin_amdgcn_raw_buffer_store_b128(o, r_bw, (t < 2 || c16 < 8) ? (nb0 + tile_node(t, c16)) * 256 + u * 64 + g4 * 16 : OOB, 0, SC1);
    }
    STAMP(15);
    await(lds, 4, 4);
    dma4(s_we, wv0, wv1, 0, lb + L_W1 + u * 4096);
    dma4(s_we, wv0, wv1, 16384, lb + L_W1 + 16384 + u * 4096);
    await(lds, 3, 4);
    dma4(s_wh, wv0, wv1, 0, lb + L_W0 + u * 4096);
    dma4(s_wh, wv0, wv1, 16384, lb + L_W0 + 16384 + u * 4096);
    {
      const float wd = bf2f(wdv) * LOG2E;
      const u32 wdh = pk_bf16(wd, 0.f);
      const u32 wdp = wdh | (pk_bf16(wd - bf2f(wdh), 0.f) << 16), wap = pk_bf16(bf2f(wav) * LOG2E, 0.f);
#pragma unroll
      for (int i = 0; i < 4; i++) {
        char* kb = lds + L_KB + (i * 256 + tid) * 16;
        *(u32*)(kb + 12) = wdp;
        *(u16*)(kb + 10) = (u16)wap;
      }
      if (tid < D) *(u16*)(lds + L_WX + tid * 2) = (u16)wxv;
    }
    asm volatile("s_waitcnt vmcnt(16)" ::: "memory");
    if (lane == 0 && __hip_atomic_fetch_add((u32*)(lds + L_SY) + 5, 1u, __ATOMIC_RELAXED, __HIP_MEMORY_SCOPE_WORKGROUP) == 3)
      __hip_atomic_store((gu64*)(slot + WS_FL) + b, ((((u64)ep_hi << 32) | ep_lo) + 1) & 0xfffffffffffull, RLX_AGENT);
    STAMP(1);
    asm volatile("s_waitcnt vmcnt(0)" ::: "memory");
  } else {
    const int t1 = tid - 256;
    const u32x4 z = {0, 0, 0, 0};
#pragma unroll
    for (int i = 0; i < 10; i++) if (i < 9 || L_SM + i * 4096 + t1 * 16 < L_POS) *(u32x4*)(lds + L_SM + i * 4096 + t1 * 16) = z;
    setup();
    asm volatile("s_waitcnt vmcnt(7)" : "+v"(ed[0]), "+v"(ed[1]), "+v"(ed[2]) :: "memory");
    u32 rank[3], ebk[3];
#pragma unroll
    for (int i = 0; i < 3; i++) {
      ebk[i] = ed[i] / NPB;
      rank[i] = 0;
      if (t1 + 256 * i < ne) rank[i] = atomicAdd((u32*)(lds + L_BC) + ebk[i], 1u);
    }
    arrive(lb, 0);
    asm volatile("s_waitcnt vmcnt(0)\n\tv_readfirstlane_b32 %0, %8\n\tv_readfirstlane_b32 %1, %9"
                 : "=s"(ep_lo), "=s"(ep_hi), "+v"(es[0]), "+v"(es[1]), "+v"(es[2]), "+v"(ee[0]), "+v"(ee[1]), "+v"(ee[2])
                 : "v"(epoch[0]), "v"(epoch[1]) : "memory");
    ep_lo = RFL(ep_lo); ep_hi = RFL(ep_hi);
    char* const slot = ws + WS_SLOT0 + (size_t)(ep_lo & 3u) * WS_SLOT;
    const u64 tag = ((((u64)ep_hi << 32) | ep_lo) + 1) & 0xfffffffffffull;
    STAMP(14);
    await(lds, 0, 8);
    STAMP(13);
    const u32x4 c4 = *(const u32x4*)(lds + L_BC + lane * 16);
    const u32 cown = *(const u32*)(lds + L_BC + t1 * 4);
    u32x2 pd[3], ps[3];
#pragma unroll
    for (int i = 0; i < 3; i++) {
      const u32* qd = (const u32*)(lds + L_POS + ((ed[i] * 6) & ~3u));
      const u32* qs = (const u32*)(lds + L_POS + ((es[i] * 6) & ~3u));
      pd[i] = u32x2{qd[0], qd[1]}; ps[i] = u32x2{qs[0], qs[1]};
    }
    const u32 pown = *(const u16*)(lds + L_POS + min((nb0 * 3 + t1) * 2, MAXPOS - 2));
    const u32 tot = c4[0] + c4[1] + c4[2] + c4[3];
    const u32 ex = scan64(tot) - tot;
    u32* mine = (u32*)(lds + L_LS + u * 1024);
    *(u32x4*)(mine + lane * 4) = u32x4{ex, ex + c4[0], ex + c4[0] + c4[1], ex + c4[0] + c4[1] + c4[2]};
    __builtin_amdgcn_wave_barrier();
    u32 at[3];
#pragma unroll
    for (int i = 0; i < 3; i++) at[i] = L_RS + (mine[ebk[i]] + rank[i]) * 16;
    const u32 gword = (mine[t1] << 10) | cown;
#pragma unroll
    for (int i = 0; i < 3; i++) {
      const u32 shd = (ed[i] & 1u) * 16, shs = (es[i] & 1u) * 16;
      const u32 dxy = __builtin_amdgcn_alignbit(pd[i][1], pd[i][0], shd), sxy = __builtin_amdgcn_alignbit(ps[i][1], ps[i][0], shs);
      const u32 ex2 = pk_bf16(0.f, bf2f(dxy) - bf2f(sxy));
      const u32 yz = pk_bf16(bfhi(dxy) - bfhi(sxy), bf2f(pd[i][1] >> shd) - bf2f(ps[i][1] >> shs));
      u32x4 rc = {es[i] | ((ed[i] - ebk[i] * NPB) << 14) | ((u32)tag << 20), (u32)(tag >> 12), ee[i] | ex2, yz};
      PIN(rc);
      if (t1 + 256 * i < ne) *(u32x4*)(lds + at[i]) = rc;
    }
    if (t1 < NPB * 3) *(u16*)(lds + L_PD + t1 * 2) = (u16)pown;
    arrive(lb, 2);
    await(lds, 2, 4);
    const __amdgpu_buffer_rsrc_t r_rw = rsrc(slot + WS_REC + (size_t)b * CH * 16, CH * 16);
    u32x4 rv[3];
#pragma unroll
    for (int i = 0; i < 3; i++) rv[i] = *(const u32x4*)(lds + L_RS + (t1 + 256 * i) * 16);
#pragma unroll
    for (int i = 0; i < 3; i++) __builtin_amdgcn_raw_buffer_store_b128(rv[i], r_rw, t1 * 16, i * 4096, SC1);
    arrive(lb, 4);
    __hip_atomic_store((gu64*)(slot + WS_GR) + (size_t)b * MAXG + t1, (tag << 20) | gword, RLX_AGENT);
    STAMP(2);
#pragma unroll
    for (int i = 0; i < 5; i++) { const int o = L_POS + i * 4096 + t1 * 16; if (i < 4 || o < L_SRC) *(u32x4*)(lds + o) = z; }
    *(u32x4*)(lds + L_SRC + t1 * 16) = u32x4{OOB, OOB, OOB, OOB};
    if (t1 < 16) *(u32*)(lds + L_SRC + 4096 + t1 * 4) = OOB;
    await(lds, 1, 4);
#pragma unroll
    for (int s = 0; s < 4; s++)
#pragma unroll
      for (int t = 0; t < 3; t++) hb[t][s] = h_frag(lds, t, s, lane);
  }
  STAMP(3);
  __syncthreads();
  ep_lo = RFL(ep_lo); ep_hi = RFL(ep_hi);
  const u64 ep = ((u64)ep_hi << 32) | ep_lo, tag = (ep + 1) & 0xfffffffffffull;
  char* const slot = ws + WS_SLOT0 + (size_t)(ep_lo & 3u) * WS_SLOT;
  const __amdgpu_buffer_rsrc_t r_bt = rsrc(slot + WS_BT, N * 256), r_rec = rsrc(slot + WS_REC, G * CH * 16);
  const u32 tag0 = (u32)tag & 0xfffu, tag1 = (u32)(tag >> 12);
  f32x4 cp[3];
  u32 pw[4] = {0, 0, 0, 0};
  {
    const int cs = wv * 32 + (lane & 31), csc = cs < G ? cs : G - 1;
    gu64* const gp = (gu64*)(slot + WS_GR) + (size_t)csc * MAXG + bk;
    gu64* const fp = (gu64*)(slot + WS_FL) + csc;
    u64 gv = __hip_atomic_load(gp, RLX_AGENT);
    SB();
    {
      f32x4 acc[3] = {f32x4{0, 0, 0, 0}, f32x4{0, 0, 0, 0}, f32x4{0, 0, 0, 0}};
#pragma unroll
      for (int s = 0; s < 4; s++) {
        const u32x4 fw = tr_frag(lds + L_W1, s, lane, 2 * wv + ((lane & 3) >> 1), lane & 1);
#pragma unroll
        for (int t = 0; t < 3; t++) acc[t] = mfma16(hb[t][s], fw, acc[t]);
      }
      char* kb = lds + L_KB + (16 * wv + c16) * 16;
#pragma unroll
      for (int t = 0; t < 2; t++) {
        const f32x4 a = acc[t] * LOG2E;
        *(u32x2*)(kb + (4 * t + g4) * 2048) = u32x2{pk_bf16(a[0], a[1]), pk_bf16(a[2], a[3])};
      }
      if (g4 < 2) {
#pragma unroll
        for (int i = 0; i < 4; i++) *(u16*)(kb + (4 * g4 + i) * 2048 + 8) = (u16)pk_bf16(acc[2][i] * LOG2E, 0.f);
      }
    }
    SB();
    STAMP(4);
    for (u32 spins = 0; !__all((gv >> 20) == tag) && spins < (1u << 20); spins++) {
      __builtin_amdgcn_s_sleep(4);
      if ((gv >> 20) != tag) gv = __hip_atomic_load(gp, RLX_AGENT);
    }
    STAMP(5);
    const bool on = lane < 32 && cs < G;
    const u32 cnt = on ? ((u32)gv & 1023u) : 0u, st = ((u32)gv >> 10) & 1023u;
    const u32 inc = scan64(cnt);
    const u32 total = __builtin_amdgcn_readlane(inc, 63);
    u32* list = (u32*)(lds + L_LS + wv * 768);
    const u32 base = (cs * CH + st) * 16, p0 = inc - cnt;
    const u32 lim = min(cnt, p0 < 128 ? 128 - p0 : 0u);
#pragma unroll
    for (u32 k = 0; k < 6; k++) list[k < lim ? p0 + k : 128 + lane] = base + k * 16;
    if (__any(lim > 6)) {
#pragma unroll 1
      for (u32 k = 6; k < lim; k++) list[p0 + k] = base + k * 16;
    }
    __builtin_amdgcn_wave_barrier();
    {
      const u32 a0 = list[lane], a1 = list[64 + lane];
      const bool v0 = lane < total, v1 = 64 + lane < total;
      u32x4 r0 = __builtin_amdgcn_raw_buffer_load_b128(r_rec, v0 ? a0 : OOB, 0, SC1);
      u32x4 r1 = __builtin_amdgcn_raw_buffer_load_b128(r_rec, v1 ? a1 : OOB, 0, SC1);
      u64 fv = __hip_atomic_load(fp, RLX_AGENT);
      if (on) *(u32*)(lds + L_BC + cs * 4) = (u32)gv & 0xfffffu;
      SB();
#pragma unroll
      for (int t = 0; t < 3; t++) cp[t] = f32x4{0, 0, 0, 0};
#pragma unroll
      for (int s = 0; s < 4; s++) {
        const u32x4 f1 = tr_frag(lds + L_W0, s, lane, 2 * wv + ((lane & 3) >> 1), lane & 1);
#pragma unroll
        for (int t = 0; t < 3; t++) cp[t] = mfma16(f1, hb[t][s], cp[t]);
      }
#pragma unroll
      for (int t = 0; t < 3; t++) { cp[t] *= LOG2E; PIN(cp[t]); }
      SB();
      STAMP(6);
      r0 = settle(r0, v0, r_rec, a0, tag0, tag1);
      r1 = settle(r1, v1, r_rec, a1, tag0, tag1);
      STAMP(7);
      u32 k0 = 0, k1 = 0;
      if (v0) k0 = take_rank(lds, r0);
      if (v1) k1 = take_rank(lds, r1);
      for (u32 spins = 0; !__all(fv == tag) && spins < (1u << 20); spins++) {
        __builtin_amdgcn_s_sleep(4);
        fv = __hip_atomic_load(fp, RLX_AGENT);
      }
      __builtin_amdgcn_s_waitcnt(0x0f70);
      pw[0] = __builtin_amdgcn_raw_buffer_load_b32(r_bt, v0 && k0 < PFR ? (r0[0] & 0x3fffu) << 8 : OOB, 0, SC1);
      pw[1] = __builtin_amdgcn_raw_buffer_load_b32(r_bt, v0 && k0 < PFR ? ((r0[0] & 0x3fffu) << 8) + 128 : OOB, 0, SC1);
      pw[2] = __builtin_amdgcn_raw_buffer_load_b32(r_bt, v1 && k1 < PFR ? (r1[0] & 0x3fffu) << 8 : OOB, 0, SC1);
      pw[3] = __builtin_amdgcn_raw_buffer_load_b32(r_bt, v1 && k1 < PFR ? ((r1[0] & 0x3fffu) << 8) + 128 : OOB, 0, SC1);
      SB();
      if (v0) put_rec(lds, r0, k0);
      if (v1) put_rec(lds, r1, k1);
    }
    if (__builtin_expect(total > 128, 0)) {
#pragma unroll 1
      for (u32 done = 128; done < total; done += 64) {
        if (__hip_atomic_load((u32*)(lds + L_CNT + 32), __ATOMIC_RELAXED, __HIP_MEMORY_SCOPE_WORKGROUP)) break;
        __builtin_amdgcn_wave_barrier();
#pragma unroll 1
        for (u32 k = p0 < done ? done - p0 : 0u; k < cnt && p0 + k < done + 64; k++) list[p0 + k - done] = base + k * 16;
        __builtin_amdgcn_wave_barrier();
        const bool v0 = done + lane < total;
        const u32 a0 = list[lane];
        u32x4 r0 = __builtin_amdgcn_raw_buffer_load_b128(r_rec, v0 ? a0 : OOB, 0, SC1);
        r0 = settle(r0, v0, r_rec, a0, tag0, tag1);
        if (v0) put_rec(lds, r0, take_rank(lds, r0));
      }
      __builtin_amdgcn_s_waitcnt(0x0f70);
    }
  }
  STAMP(9);
  __syncthreads();
  STAMP(8);
  const u32* wcnt = (const u32*)(lds + L_CNT);
  const u32 ci = wcnt[lane >> 3], cj = wcnt[lane & 7];
  const int ovf = RFL(wcnt[8]);
  auto load_wh2 = [&]() {
    const u32x4 s_wh = srd(w_h, 65536);
    dma4(s_wh, wv0 + (wv >> 2) * 16384, wv1 + (wv >> 2) * 16384, 32768, lb + L_W0 + wv * 4096);
  };
  u32x4 v[8], wxf[8];
  int win;
  {
    const u64 gt = __ballot(cj * 8 + (lane & 7) > ci * 8 + (lane >> 3));
    const u32 rk = __builtin_popcount((u32)(gt >> (lane & 56)) & 0xffu);
    const int wl = __builtin_ctzll(__ballot(rk == (u32)(wv < 4 ? wv : 11 - wv)));
    win = wl >> 3;
    const int cntw = ovf ? 1 : __builtin_amdgcn_readlane(ci, wl);
    const int nsteps = (cntw + 15) >> 4;
    const u32 lo = r * 4;
    u32 srca = L_SRC + win * CAPW * 4 + hh * 32;
    u32 GA[8], GB[8], off[8];
    const bool cfon = rp == hh;
    u32 cfa = cfon ? (L_CF + (win * CAPW + 8 * rh + rj) * 16) : L_ZERO;
    const u32 cfs = cfon ? 256u : 0u;
    u32 sma = L_SM + (win * CAPW + 8 * hh + (c16 >> 2)) * 40 + (g4 & 1) * 32 + (lane & 3) * 8;
    u32x4 cf, kbA, kbB, sf = {0, 0, 0, 0}, mB0 = {0, 0, 0, 0}, mB1 = {0, 0, 0, 0}, mA0, mA1;
    f32x16 yA, yB;
    {
      const u32x4 i0 = *(const u32x4*)(lds + srca), i1 = *(const u32x4*)(lds + srca + 16);
      const u32x4 i2 = *(const u32x4*)(lds + srca + 64), i3 = *(const u32x4*)(lds + srca + 80);
      cf = *(const u32x4*)(lds + cfa);
      kbA = *(const u32x4*)(lds + L_KB + win * 2048 + (2 * r + hh) * 16);
      kbB = *(const u32x4*)(lds + L_KB + win * 2048 + (64 + 2 * r + hh) * 16);
      srca += 128;
      SB();
#pragma unroll
      for (int j = 0; j < 4; j++) { off[j] = i0[j] + lo; off[4 + j] = i1[j] + lo; }
#pragma unroll
      for (int j = 0; j < 8; j++) GA[j] = __builtin_amdgcn_raw_buffer_load_b32(r_bt, off[j], 0, SC1);
      SB();
#pragma unroll
      for (int j = 0; j < 8; j++) GB[j] = __builtin_amdgcn_raw_buffer_load_b32(r_bt, off[j] + 128, 0, SC1);
      SB();
      asm volatile("" :: "v"(pw[0]), "v"(pw[1]), "v"(pw[2]), "v"(pw[3]));
      yA = mfma32(cf, kbA, zero);
#pragma unroll
      for (int j = 0; j < 4; j++) { off[j] = i2[j] + lo; off[4 + j] = i3[j] + lo; }
      yA = mfma32(sel0, u32x4{GA[0], GA[1], GA[2], GA[3]}, yA);
      yA = mfma32(sel1, u32x4{GA[4], GA[5], GA[6], GA[7]}, yA);
      PIN(yA);
      SB();
#pragma unroll
      for (int j = 0; j < 8; j++) GA[j] = __builtin_amdgcn_raw_buffer_load_b32(r_bt, off[j], 0, SC1);
      SB();
    }
#define MF(d, a, bb, c) do { SB(); d = mfma32(a, bb, c); PIN(d); SB(); } while (0)
#define SA(y, i, j) do { p_ = f32x2{__builtin_amdgcn_exp2f(-y[i]), __builtin_amdgcn_exp2f(-y[(i) + 1])}; \
                         q_ = f32x2{__builtin_amdgcn_exp2f(-y[j]), __builtin_amdgcn_exp2f(-y[(j) + 1])}; SB(); p_ += 1.0f; q_ += 1.0f; SB(); } while (0)
#define SBB(y, i, j, o0, o1) do { p_ = f32x2{__builtin_amdgcn_rcpf(p_[0]), __builtin_amdgcn_rcpf(p_[1])}; \
                         q_ = f32x2{__builtin_amdgcn_rcpf(q_[0]), __builtin_amdgcn_rcpf(q_[1])}; SB(); \
                         p_ *= f32x2{y[i], y[(i) + 1]}; q_ *= f32x2{y[j], y[(j) + 1]}; SB(); \
                         u32 t0_ = pk_bf16(p_[0], p_[1]), t1_ = pk_bf16(q_[0], q_[1]); \
                         asm volatile("" : "+v"(t0_), "+v"(t1_)); o0 = t0_; o1 = t1_; SB(); } while (0)
#define S4(y, i, j, o0, o1) do { SA(y, i, j); SBB(y, i, j, o0, o1); } while (0)
#define LD2(Gx, j, add) do { Gx[j] = __builtin_amdgcn_raw_buffer_load_b32(r_bt, off[j] + (add), 0, SC1); \
                             Gx[(j) + 1] = __builtin_amdgcn_raw_buffer_load_b32(r_bt, off[(j) + 1] + (add), 0, SC1); SB(); } while (0)
    f32x2 p_, q_;
    for (int s = 1; s < nsteps; s++) {
      { const int left = nsteps - s; if (left >= 4) __builtin_amdgcn_s_setprio(3); else if (left == 3) __builtin_amdgcn_s_setprio(2); else if (left == 2) __builtin_amdgcn_s_setprio(1); else __builtin_amdgcn_s_setprio(0); }
      MF(yB, cf, kbB, zero);
      S4(yA, 0, 2, mA0[0], mA0[1]);
      MF(yB, sel0, (u32x4{GB[0], GB[1], GB[2], GB[3]}), yB);
      cfa += cfs;
      cf = *(const u32x4*)(lds + cfa);
      SA(yA, 4, 6);
      LD2(GB, 0, 128);
      SBB(yA, 4, 6, mA0[2], mA0[3]);
      LD2(GB, 2, 128);
      MF(yB, sel1, (u32x4{GB[4], GB[5], GB[6], GB[7]}), yB);
      SA(yA, 8, 10);
      LD2(GB, 4, 128);
      MF(aB0, mB0, sf, aB0);
      SBB(yA, 8, 10, mA1[0], mA1[1]);
      LD2(GB, 6, 128);
      const u32x4 i0 = *(const u32x4*)(lds + srca), i1 = *(const u32x4*)(lds + srca + 16);
      srca += 64;
      SA(yA, 12, 14);
      MF(aB1, mB1, sf, aB1);
      sf = s_frag(lds + sma);
      sma += 640;
      SBB(yA, 12, 14, mA1[2], mA1[3]);
      MF(yA, cf, kbA, zero);
      S4(yB, 0, 2, mB0[0], mB0[1]);
      MF(yA, sel0, (u32x4{GA[0], GA[1], GA[2], GA[3]}), yA);
#pragma unroll
      for (int j = 0; j < 4; j++) { off[j] = i0[j] + lo; off[4 + j] = i1[j] + lo; }
      SA(yB, 4, 6);
      LD2(GA, 0, 0);
      SBB(yB, 4, 6, mB0[2], mB0[3]);
      LD2(GA, 2, 0);
      MF(yA, sel1, (u32x4{GA[4], GA[5], GA[6], GA[7]}), yA);
      SA(yB, 8, 10);
      LD2(GA, 4, 0);
      MF(aA0, mA0, sf, aA0);
      SBB(yB, 8, 10, mB1[0], mB1[1]);
      LD2(GA, 6, 0);
      SA(yB, 12, 14);
      MF(aA1, mA1, sf, aA1);
      SBB(yB, 12, 14, mB1[2], mB1[3]);
    }
    {
      const int rem = cntw - 16 * (nsteps - 1);
      load_wh2();
      MF(yB, cf, kbB, zero);
      mA0 = u32x4{0, 0, 0, 0}; mA1 = u32x4{0, 0, 0, 0};
      S4(yA, 0, 8, mA0[0], mA1[0]);
      MF(yB, sel0, (u32x4{GB[0], GB[1], GB[2], GB[3]}), yB);
      if (rem > 4) S4(yA, 2, 10, mA0[1], mA1[1]);
      MF(yB, sel1, (u32x4{GB[4], GB[5], GB[6], GB[7]}), yB);
      if (rem > 8) S4(yA, 4, 12, mA0[2], mA1[2]);
      MF(aB0, mB0, sf, aB0);
      if (rem > 12) S4(yA, 6, 14, mA0[3], mA1[3]);
      MF(aB1, mB1, sf, aB1);
      sf = s_frag(lds + sma);
      mB0 = u32x4{0, 0, 0, 0}; mB1 = u32x4{0, 0, 0, 0};
      S4(yB, 0, 8, mB0[0], mB1[0]);
      MF(aA0, mA0, sf, aA0);
      if (rem > 4) S4(yB, 2, 10, mB0[1], mB1[1]);
      MF(aA1, mA1, sf, aA1);
      if (rem > 8) S4(yB, 4, 12, mB0[2], mB1[2]);
      if (rem > 12) S4(yB, 6, 14, mB0[3], mB1[3]);
      MF(aB0, mB0, sf, aB0);
      MF(aB1, mB1, sf, aB1);
    }
#pragma unroll
    for (int q8 = 0; q8 < 8; q8++) {
      const f32x16& a0 = q8 < 4 ? aA0 : aB0;
      const f32x16& a1 = q8 < 4 ? aA1 : aB1;
#pragma unroll
      for (int q = 0; q < 4; q++) v[q8][q] = pk_bf16(a0[4 * (q8 & 3) + q], a1[4 * (q8 & 3) + q]);
    }
    if (r < 20 && (r & 3) == 0 && !ovf) {
      char* mi = lds + mi_row(r * 2 + win) + 16 * hh;
#pragma unroll
      for (int q8 = 0; q8 < 8; q8++) *(u32x4*)(mi + 32 * q8) = v[q8];
    }
  }
  if (__builtin_expect(ovf, 0)) {
    float* macc = (float*)(lds + L_SM);
    float* pacc = (float*)(lds + L_SM + NPB * D * 4);
    __syncthreads();
    for (int i = tid; i < NPB * D + NPB * 4; i += NT) macc[i] = 0.f;
    __syncthreads();
    const u32 wxp = *(const u32*)(lds + L_WX + lane * 4);
    const float wx0 = bf2f(wxp), wx1 = bfhi(wxp);
    const u32x4 kc0 = *(const u32x4*)(lds + L_KB + (2 * lane) * 16), kc1 = *(const u32x4*)(lds + L_KB + (2 * lane + 1) * 16);
    const float wd0 = bf2f(kc0[3]) + bfhi(kc0[3]), wd1 = bf2f(kc1[3]) + bfhi(kc1[3]);
    const float wa0 = bfhi(kc0[2]), wa1 = bfhi(kc1[2]);
    u32 rot = 0;
    for (int c = 0; c < G; c++) {
      const u32 oo = RFL(*(const u32*)(lds + L_BC + c * 4));
      const u32 s0 = oo >> 10, n = oo & 1023u;
      for (u32 k = (wv - rot) & 7u; k < n; k += 8) {
        u32x4 rr = __builtin_amdgcn_raw_buffer_load_b128(r_rec, (c * CH + s0 + k) * 16, 0, SC1);
        rr = settle(rr, true, r_rec, (c * CH + s0 + k) * 16, tag0, tag1);
        const u32 nl = (rr[0] >> 14) & 0x3fu, sl = nl >> 3, wn = nl & 7u;
        const u32 bv = __builtin_amdgcn_raw_buffer_load_b32(r_bt, ((rr[0] & 0x3fffu) << 8) + lane * 4, 0, SC1);
        const float d2 = bfhi(dist2(rr[2], rr[3])), eav = bf2f(rr[2]);
        const float a0 = bf2f(*(const u16*)(lds + L_KB + wn * 2048 + (2 * lane) * 16 + sl * 2));
        const float a1 = bf2f(*(const u16*)(lds + L_KB + wn * 2048 + (2 * lane + 1) * 16 + sl * 2));
        const float y0 = a0 + bf2f(bv) + d2 * wd0 + eav * wa0, y1 = a1 + bfhi(bv) + d2 * wd1 + eav * wa1;
        const u32 mp = silu2(y0, y1);
        const float m0 = bf2f(mp), m1 = bfhi(mp);
        atomicAdd(&macc[nl * D + 2 * lane], m0);
        atomicAdd(&macc[nl * D + 2 * lane + 1], m1);
        float csv = m0 * wx0 + m1 * wx1;
#pragma unroll
        for (int o = 32; o > 0; o >>= 1) csv += __shfl_xor(csv, o, 64);
        if (lane < 3) atomicAdd(&pacc[nl * 4 + lane], csv * (lane == 0 ? bfhi(rr[2]) : lane == 1 ? bf2f(rr[3]) : bfhi(rr[3])));
      }
      rot += n;
    }
    __syncthreads();
    for (int i = tid; i < NPB * D / 2; i += NT) {
      const int n = i >> 6, f = (i & 63) * 2;
      *(u32*)(lds + mi_row(n) + f * 2) = pk_bf16(macc[n * D + f], macc[n * D + f + 1]);
    }
    if (tid < NPB * 3) {
      const int n = tid / 3, c = tid - n * 3;
      if (nb0 + n < N) out[(size_t)N * D + (size_t)nb0 * 3 + tid] = (u16)pk_bf16(bf2f(*(const u16*)(lds + L_PD + tid * 2)) + LN2 * pacc[n * 4 + c], 0.f);
    }
  }
  u32x2 hr[3];
  u32x4 f2[4];
#pragma unroll
  for (int t = 0; t < 3; t++) {
    const int n = tile_node(t, c16);
    hr[t] = *(const u32x2*)(lds + L_HS + n * 256 + 16 * ((2 * wv + (g4 >> 1)) ^ hswz(n)) + 8 * (g4 & 1));
  }
#pragma unroll
  for (int q8 = 0; q8 < 8; q8++) wxf[q8] = *(const u32x4*)(lds + L_WX + (16 * q8 + 8 * hh) * 2);
  const int kind = lane & 3, pnl = (lane >> 2) * 8 + win;
  const bool pon = kind != 0 && lane < 20 && !ovf;
  const u32 p0 = *(const u16*)(lds + L_PD + (pon ? pnl * 6 + kind * 2 - 2 : 0));
  const int mia = mi_row(tile_node(0, c16)) + g4 * 16, mib = mi_row(tile_node(2, c16)) + g4 * 16;
  asm volatile("s_waitcnt vmcnt(0)" ::: "memory");
  STAMP(10);
  __syncthreads();
  {
    u32x4 mb[3][4];
#pragma unroll
    for (int s = 0; s < 4; s++) {
      f2[s] = tr_frag(lds + L_W0, s, lane, 2 * wv + ((lane & 3) >> 1), lane & 1);
      mb[0][s] = *(const u32x4*)(lds + mia + s * 64);
      mb[1][s] = *(const u32x4*)(lds + mia + 8192 + s * 64);
      mb[2][s] = *(const u32x4*)(lds + mib + s * 64);
    }
    SB();
#pragma unroll
    for (int s = 0; s < 4; s++)
#pragma unroll
      for (int t = 0; t < 3; t++) cp[t] = mfma16(f2[s], mb[t][s], cp[t]);
    f32x16 z = zero, z1 = zero;
#pragma unroll
    for (int t = 0; t < 3; t++) {
      const f32x4 y = cp[t];
      f32x4 e;
#pragma unroll
      for (int i = 0; i < 4; i++) e[i] = __builtin_amdgcn_exp2f(-y[i]);
      SB();
      if (t & 1) { z1 = mfma32(wxf[3 * t], v[3 * t], z1); PIN(z1); } else { z = mfma32(wxf[3 * t], v[3 * t], z); PIN(z); }
      SB();
      e += 1.0f;
#pragma unroll
      for (int i = 0; i < 4; i++) e[i] = __builtin_amdgcn_rcpf(e[i]);
      SB();
      if (t & 1) { z = mfma32(wxf[3 * t + 1], v[3 * t + 1], z); PIN(z); } else { z1 = mfma32(wxf[3 * t + 1], v[3 * t + 1], z1); PIN(z1); }
      SB();
      const f32x4 o = f32x4{bf2f(hr[t][0]), bfhi(hr[t][0]), bf2f(hr[t][1]), bfhi(hr[t][1])} + (y * LN2) * e;
      *(u32x2*)(lds + L_SM + tile_node(t, c16) * 272 + wv * 32 + g4 * 8) = u32x2{pk_bf16(o[0], o[1]), pk_bf16(o[2], o[3])};
      SB();
      if (t == 0) { z = mfma32(wxf[2], v[2], z); PIN(z); }
      if (t == 1) { z1 = mfma32(wxf[5], v[5], z1); PIN(z1); }
      SB();
    }
    __syncthreads();
    const __amdgpu_buffer_rsrc_t r_out = rsrc(out, N * 262);
    const u32x4 o0 = *(const u32x4*)(lds + L_SM + (tid >> 4) * 272 + (tid & 15) * 16);
    const u32x4 o1 = *(const u32x4*)(lds + L_SM + (32 + ((tid >> 4) & 7)) * 272 + (tid & 15) * 16);
    const u32 oo = nb0 * 256 + tid * 16;
    __builtin_amdgcn_raw_buffer_store_b128(o0, r_out, oo < N * 256 ? oo : OOB, 0, 0);
    __builtin_amdgcn_raw_buffer_store_b128(o1, r_out, tid < 128 && oo + 8192 < N * 256 ? oo + 8192 : OOB, 0, 0);
    __builtin_amdgcn_raw_buffer_store_b16((u16)pk_bf16(bf2f(p0) + LN2 * (z[0] + z1[0]), 0.f), r_out,
                                          pon ? N * 256 + ((nb0 + pnl) * 3 + kind - 1) * 2 : OOB, 0, 0);
  }
  if (b == 0 && tid == 0) __hip_atomic_store((gu64*)(ws + WS_EP), ep + 1, RLX_AGENT);
  STAMP(11);
}

extern "C" void kernel_launch(void* const* d_in, const int* in_sizes, int n_in, void* d_out, int out_size, void* d_ws,
                              size_t ws_size, hipStream_t stream) {
  const int N = in_sizes[0] / D, E = in_sizes[3];
  const int NB = (N + NPB - 1) / NPB, C = (E + CH - 1) / CH;
  const int G = NB > C ? NB : C;
  k_egnn<<<G, NT, 0, stream>>>((const u16*)d_in[0], (const u16*)d_in[1], (const u16*)d_in[2], (const int*)d_in[3],
                               (const int*)d_in[4], (const u16*)d_in[5], (const u16*)d_in[6], (const u16*)d_in[7],
                               (char*)d_ws, (u16*)d_out, N, E, G);
}
```

```cpp
#include <hip/hip_runtime.h>
#include <hip/hip_bf16.h>

typedef unsigned int u32;
typedef unsigned short u16;
typedef unsigned long long u64;
typedef u32 u32x2 __attribute__((ext_vector_type(2)));
typedef u32 u32x4 __attribute__((ext_vector_type(4)));
typedef short s16x4 __attribute__((ext_vector_type(4)));
typedef short bf16x8 __attribute__((ext_vector_type(8)));
typedef float f32x2 __attribute__((ext_vector_type(2)));
typedef float f32x4 __attribute__((ext_vector_type(4)));
typedef float f32x16 __attribute__((ext_vector_type(16)));
typedef __bf16 bf16x2_t __attribute__((ext_vector_type(2)));
typedef __attribute__((address_space(1))) u64 gu64;
typedef __attribute__((address_space(3))) s16x4 lds_s16x4;
typedef __attribute__((address_space(3))) char lds_char;
#define DI __device__ __forceinline__
#define RLX_AGENT __ATOMIC_RELAXED, __HIP_MEMORY_SCOPE_AGENT
#define SC1 16
#define SB() __builtin_amdgcn_sched_barrier(0)
#define PIN(x) asm volatile("" : "+v"(x))
#define RFL(x) __builtin_amdgcn_readfirstlane(x)

constexpr int D = 128;
constexpr int NT = 512;
constexpr int NPB = 40;
constexpr int CH = 640;
constexpr int MAXG = 256;
constexpr int CAPW = 128;
constexpr int MAXPOS = 61440;
constexpr u32 PFR = 16;
constexpr u32 SPINS = 1u << 16;
constexpr float LOG2E = 1.4426950408889634f;
constexpr float LN2 = 0.6931471805599453f;
constexpr u32 OOB = 0xfffff000u;

constexpr size_t WS_EP = 0;
constexpr size_t WS_SLOT0 = (size_t)2 << 20, WS_SLOT = (size_t)6 << 20;
constexpr size_t WS_GR = 0;
constexpr size_t WS_BT = WS_GR + (size_t)MAXG * MAXG * 8;
constexpr size_t WS_REC = WS_BT + (size_t)MAXG * NPB * 256;
constexpr size_t WS_FL = WS_REC + (size_t)MAXG * CH * 16;
static_assert(WS_FL + MAXG * 8 <= WS_SLOT, "slot");

DI float bf2f(u32 b) { return __builtin_bit_cast(float, b << 16); }
DI float bfhi(u32 b) { return __builtin_bit_cast(float, b & 0xffff0000u); }
DI u32 pk_bf16(float lo, float hi) {
  f32x2 v = {lo, hi};
  bf16x2_t r = __builtin_convertvector(v, bf16x2_t);
  return __builtin_bit_cast(u32, r);
}
DI f32x4 mfma16(u32x4 a, u32x4 b, f32x4 c) {
  return __builtin_amdgcn_mfma_f32_16x16x32_bf16(__builtin_bit_cast(bf16x8, a), __builtin_bit_cast(bf16x8, b), c, 0, 0, 0);
}
DI f32x16 mfma32(u32x4 a, u32x4 b, f32x16 c) {
  return __builtin_amdgcn_mfma_f32_32x32x16_bf16(__builtin_bit_cast(bf16x8, a), __builtin_bit_cast(bf16x8, b), c, 0, 0, 0);
}
DI __amdgpu_buffer_rsrc_t rsrc(const void* p, u32 bytes) { return __builtin_amdgcn_make_buffer_rsrc((void*)p, 0, bytes, 0x00020000); }
DI u32x4 srd(const void* p, u32 bytes) { const u64 a = (u64)p; return u32x4{(u32)a, (u32)(a >> 32) & 0xffffu, bytes, 0x00020000u}; }
DI void dma4(u32x4 sd, u32 v0, u32 v1, u32 soff, u32 ldsa) {
  u32 keep;
  asm volatile("s_mov_b32 %0, m0\n\ts_mov_b32 m0, %1\n\ts_nop 0\n\t"
               "buffer_load_dwordx4 %2, %4, %5 offen lds\n\tbuffer_load_dwordx4 %2, %4, %5 offen offset:1024 lds\n\t"
               "buffer_load_dwordx4 %3, %4, %5 offen offset:2048 lds\n\tbuffer_load_dwordx4 %3, %4, %5 offen offset:3072 lds\n\t"
               "s_mov_b32 m0, %0"
               : "=&s"(keep) : "s"(RFL(ldsa)), "v"(v0), "v"(v1), "s"(sd), "s"(soff) : "memory");
}
DI void dma2(u32x4 sd, u32 v0, u32 soff, u32 ldsa) {
  u32 keep;
  asm volatile("s_mov_b32 %0, m0\n\ts_mov_b32 m0, %1\n\ts_nop 0\n\t"
               "buffer_load_dwordx4 %2, %3, %4 offen lds\n\tbuffer_load_dwordx4 %2, %3, %4 offen offset:1024 lds\n\t"
               "s_mov_b32 m0, %0"
               : "=&s"(keep) : "s"(RFL(ldsa)), "v"(v0), "s"(sd), "s"(soff) : "memory");
}
DI u32 scan64(u32 x) {
  x += __builtin_amdgcn_update_dpp(0u, x, 0x111, 0xf, 0xf, false);
  x += __builtin_amdgcn_update_dpp(0u, x, 0x112, 0xf, 0xf, false);
  x += __builtin_amdgcn_update_dpp(0u, x, 0x114, 0xf, 0xf, false);
  x += __builtin_amdgcn_update_dpp(0u, x, 0x118, 0xf, 0xf, false);
  x += __builtin_amdgcn_update_dpp(0u, x, 0x142, 0xa, 0xf, false);
  x += __builtin_amdgcn_update_dpp(0u, x, 0x143, 0xc, 0xf, false);
  return x;
}

constexpr int L_ZERO = 0;
constexpr int L_CNT = L_ZERO + 16;
constexpr int L_BC = L_CNT + 48;
constexpr int L_SY = L_BC + 1024;
constexpr int L_SM = L_SY + 32;
constexpr int L_CF = L_SM + 8 * 8 * 640;
constexpr int L_SRC = L_CF + 8 * CAPW * 16;
constexpr int L_KB = L_SRC + 8 * CAPW * 4 + 64;
constexpr int L_W1 = L_KB + 8 * D * 16;
constexpr int L_W0 = L_W1 + 32768;
constexpr int L_HS = L_W0 + 32768;
constexpr int L_WX = L_HS + NPB * 256;
constexpr int L_PD = L_WX + 256;
constexpr int L_LS = L_PD + 256;
constexpr int L_END = L_LS + 8 * 768;
constexpr int L_RS = L_W0 - CH * 16;
constexpr int L_POS = L_RS - MAXPOS;
static_assert(L_END <= 160 * 1024, "lds");
static_assert(L_POS >= L_SM + 36864 && L_POS <= L_CF && (L_POS % 16) == 0, "pos table");
static_assert((L_SM % 16) == 0 && (L_BC % 16) == 0 && (L_SRC % 16) == 0 && (L_KB % 16) == 0 && (L_W0 % 16) == 0 && (L_HS % 16) == 0 && (L_WX % 16) == 0 && (L_LS % 16) == 0, "align");

DI int tile_node(int t, int c) { return t < 2 ? ((c & 3) * 8 + 4 * t + (c >> 2)) : (32 + (c & 7)); }
DI int mi_row(int nl) { return L_KB + (nl & 7) * 2048 + (nl & 3) * 64 + (nl >> 3) * 272; }
DI int wswz(int k) { return ((k & 3) << 2) | ((k >> 2) & 2); }
DI int hswz(int n) { return ((n & 3) << 2) | ((n >> 3) & 3); }
DI u32x4 tr_frag(const char* img, int s, int lane, int ch, int hf) {
  const int k0 = 8 * (lane >> 4) + ((lane & 15) >> 2);
  const char* p = img + (32 * s + k0) * 256 + 16 * (ch ^ wswz(k0)) + 8 * hf;
  const s16x4 a = __builtin_amdgcn_ds_read_tr16_b64_v4i16((lds_s16x4*)p);
  const s16x4 b = __builtin_amdgcn_ds_read_tr16_b64_v4i16((lds_s16x4*)(p + 1024));
  const u32x2 x = __builtin_bit_cast(u32x2, a), y = __builtin_bit_cast(u32x2, b);
  return u32x4{x[0], x[1], y[0], y[1]};
}
DI u32x4 s_frag(const char* p) {
  const u32x2 x = __builtin_bit_cast(u32x2, __builtin_amdgcn_ds_read_tr16_b64_v4i16((lds_s16x4*)p));
  const u32x2 y = __builtin_bit_cast(u32x2, __builtin_amdgcn_ds_read_tr16_b64_v4i16((lds_s16x4*)(p + 160)));
  return u32x4{x[0], x[1], y[0], y[1]};
}
DI u32x4 h_frag(const char* lds, int t, int s, int lane) {
  const int n = tile_node(t, lane & 15);
  return *(const u32x4*)(lds + L_HS + n * 256 + 16 * ((4 * s + (lane >> 4)) ^ hswz(n)));
}
DI u32 dist2(u32 w2, u32 w3) {
  const float dx = bf2f(w2), dy = bfhi(w2), dz = bf2f(w3);
  const u32 q0 = pk_bf16(dx * dx, dy * dy), q1 = pk_bf16(dz * dz, 0.f);
  const float s2 = bf2f(q0) + bfhi(q0) + bf2f(q1);
  return pk_bf16(s2, s2);
}
DI u32 take_rank(char* lds, u32x4 rc) { return atomicAdd((u32*)(lds + L_CNT) + ((rc[0] >> 14) & 7u), 1u); }
DI void put_rec(char* lds, u32x4 rc, u32 rank) {
  const u32 win = (rc[0] >> 14) & 7u, slot = (rc[0] >> 17) & 7u;
  const u64 oh = 0x3f80ull << (slot * 16);
  const u32x4 cf = {slot < 4 ? (u32)oh : 0u, slot < 4 ? (u32)(oh >> 32) : 0u, (slot == 4 ? 0x3f80u : 0u) | (rc[1] << 16), dist2(rc[2], rc[3])};
  if (rank < CAPW) {
    const u32 k = ((rank & 1u) << 3) + ((rank & 15u) >> 1), sp = win * CAPW + (rank & ~15u) + k;
    *(u32*)(lds + L_SRC + sp * 4) = (rc[0] & 0x3fffu) << 8;
    *(u32x4*)(lds + L_CF + sp * 16) = cf;
    *(u32x2*)(lds + L_SM + sp * 40 + slot * 8) = u32x2{0x3f80u | (rc[2] << 16), __builtin_amdgcn_alignbit(rc[3], rc[2], 16)};
  } else *(u32*)(lds + L_CNT + 32) = 1;
}
DI void rec_tags(u64 tag, u32 (&t)[3]) {
  t[0] = (u32)tag << 20; t[1] = ((u32)tag >> 12) << 16; t[2] = ((u32)(tag >> 28) ^ (u32)tag) << 16;
}
DI bool rec_stale(u32x4 rc, const u32 (&t)[3]) { return (((rc[0] ^ t[0]) >> 20) | (((rc[1] ^ t[1]) | (rc[3] ^ t[2])) >> 16)) != 0; }
DI u32x4 settle(u32x4 rc, bool on, __amdgpu_buffer_rsrc_t r, u32 off, const u32 (&t)[3]) {
  for (u32 spins = 0; __any(on && rec_stale(rc, t)) && spins < SPINS; spins++) {
    __builtin_amdgcn_s_sleep(4);
    if (on && rec_stale(rc, t)) rc = __builtin_amdgcn_raw_buffer_load_b128(r, off, 0, SC1);
  }
  return rc;
}
DI void arrive(u32 lb, int i) {
  asm volatile("s_mov_b64 exec, 1\n\tds_add_u32 %0, %1 offset:%2\n\ts_mov_b64 exec, -1" :: "v"(lb), "v"(1u), "i"(L_SY + 4 * i) : "memory");
}
DI void await(char* lds, int i, u32 n) {
  asm volatile("" ::: "memory");
  while ((u32)RFL(__hip_atomic_load((u32*)(lds + L_SY) + i, __ATOMIC_RELAXED, __HIP_MEMORY_SCOPE_WORKGROUP)) < n) { }
  asm volatile("" ::: "memory");
}
DI u32 silu2(float a, float b) {
  f32x2 yy = {a, b};
  f32x2 e = {__builtin_amdgcn_exp2f(-a), __builtin_amdgcn_exp2f(-b)};
  e += 1.0f;
  f32x2 rc = {__builtin_amdgcn_rcpf(e[0]), __builtin_amdgcn_rcpf(e[1])};
  yy *= rc;
  return pk_bf16(yy[0], yy[1]);
}

__global__ __launch_bounds__(NT, 2) void k_egnn(const u16* __restrict__ h, const u16* __restrict__ pos,
                                                const u16* __restrict__ ea, const int* __restrict__ src,
                                                const int* __restrict__ dst, const u16* __restrict__ w_e,
                                                const u16* __restrict__ w_x, const u16* __restrict__ w_h,
                                                char* __restrict__ ws, u16* __restrict__ out, int N, int E, int G) {
  __shared__ __attribute__((aligned(16))) char lds[L_END];
  const int tid = threadIdx.x, lane = tid & 63, b = blockIdx.x;
  const int wv = RFL(tid >> 6);
  const int u = wv & 3, c16 = lane & 15, g4 = lane >> 4;
  const bool role1 = wv >= 4;
  const u32 lb = (u32)(size_t)(lds_char*)lds;
  if (tid < L_SM / 4) *(u32*)(lds + tid * 4) = 0;
  __syncthreads();
  const int ne = RFL(max(0, min(CH, E - b * CH)));
  u32 ed[3], es[3], ee[3];
  u32x2 epoch;
  {
    const u32x4 s_d = srd(dst + b * CH, ne * 4), s_s = srd(src + b * CH, ne * 4), s_e = srd(ea + b * CH, ne * 2);
    asm volatile("s_cmp_lt_u32 %15, 4\n\ts_cbranch_scc1 1f\n\t"
                 "buffer_load_dword %0, %10, %12, 0 offen\n\tbuffer_load_dword %1, %10, %12, 0 offen offset:1024\n\t"
                 "buffer_load_dword %2, %10, %12, 0 offen offset:2048\n\tbuffer_load_dword %3, %10, %13, 0 offen\n\t"
                 "buffer_load_dword %4, %10, %13, 0 offen offset:1024\n\tbuffer_load_dword %5, %10, %13, 0 offen offset:2048\n\t"
                 "buffer_load_ushort %6, %11, %14, 0 offen\n\tbuffer_load_ushort %7, %11, %14, 0 offen offset:512\n\t"
                 "buffer_load_ushort %8, %11, %14, 0 offen offset:1024\n"
                 "1:\n\tglobal_load_dwordx2 %9, %16, %17 sc1"
                 : "=&v"(ed[0]), "=&v"(ed[1]), "=&v"(ed[2]), "=&v"(es[0]), "=&v"(es[1]), "=&v"(es[2]), "=&v"(ee[0]), "=&v"(ee[1]), "=&v"(ee[2]), "=&v"(epoch)
                 : "v"((tid & 255) * 4), "v"((tid & 255) * 2), "s"(s_d), "s"(s_s), "s"(s_e), "s"(wv), "v"(0u), "s"(ws + WS_EP) : "memory", "scc");
  }
  int bk;
  { const int q = G >> 3, r = G & 7, x = b & 7, i = b >> 3;
    bk = (x < r ? x * (q + 1) : r * (q + 1) + (x - r) * q) + i; }
  const int nb0 = bk * NPB;
  const int r = lane & 31, hh = lane >> 5;
  const int rh = (r >> 2) & 1, rp = r >> 4, rj = (r & 3) | (((r >> 3) & 1) << 2);
  f32x16 aA0, aA1, aB0, aB1, zero;
  u32x4 sel0, sel1;
  auto setup = [&]() {
#pragma unroll
    for (int i = 0; i < 16; i++) zero[i] = 0.f;
    u32x4 z4 = {0, 0, 0, 0};
    PIN(z4);
    aA0 = mfma32(z4, z4, zero); aA1 = mfma32(z4, z4, zero); aB0 = mfma32(z4, z4, zero); aB1 = mfma32(z4, z4, zero);
    const u32 one = rh == hh ? 0x3f80u << (rp * 16) : 0u;
#pragma unroll
    for (int q = 0; q < 4; q++) { sel0[q] = (rj == q) ? one : 0u; sel1[q] = (rj == 4 + q) ? one : 0u; }
    PIN(aA0); PIN(aA1); PIN(aB0); PIN(aB1); PIN(sel0); PIN(sel1);
  };
  u32 ep_lo, ep_hi;
  u32x4 hb[3][4];
  const u32 wv0 = g4 * 256 + 16 * (c16 ^ (g4 << 2)) + u * 4096, wv1 = wv0 ^ 32;
  if (!role1) {
    const u32x4 s_pos = srd(pos, N * 6), s_h = srd(h, N * 256), s_we = srd(w_e, 258 * 256), s_wh = srd(w_h, 65536);
#pragma unroll
    for (int i = 0; i < 4; i++)
      if (i < 3 || u < 3) dma4(s_pos, lane * 16 + u * 4096, lane * 16 + u * 4096, i * 16384, lb + L_POS + i * 16384 + u * 4096);
    dma2(s_h, g4 * 256 + 16 * (c16 ^ ((g4 << 2) | u)) + u * 2048, nb0 * 256, lb + L_HS + u * 2048);
    if (u == 0) dma2(s_h, g4 * 256 + 16 * (c16 ^ (g4 << 2)) + 8192, nb0 * 256, lb + L_HS + 8192);
    dma4(s_we, wv0, wv1, 32768, lb + L_W0 + u * 4096);
    dma4(s_we, wv0, wv1, 32768 + 16384, lb + L_W0 + 16384 + u * 4096);
    u32 wdv, wav, wxv, pf1, pf2;
    {
      const u32x4 s_wx = srd(w_x, 256);
      const u32 xi = (b >> 3) & 31;
      asm volatile("buffer_load_ushort %0, %5, %6, %7 offen\n\tbuffer_load_ushort %1, %5, %6, %7 offen offset:256\n\t"
                   "buffer_load_ushort %2, %5, %8, 0 offen\n\tbuffer_load_dword %3, %9, %6, 0 offen\n\t"
                   "buffer_load_dword %4, %10, %11, 0 offen"
                   : "=&v"(wdv), "=&v"(wav), "=&v"(wxv), "=&v"(pf1), "=&v"(pf2)
                   : "v"((tid & 127) * 2), "s"(s_we), "s"(65536u), "s"(s_wx), "v"(tid < 8 ? (xi * 8 + tid) * 128 : OOB),
                     "v"(tid - 64 < 16u ? (xi * 16 + tid - 64) * 128 : OOB), "s"(s_wh) : "memory");
    }
    asm volatile("s_waitcnt vmcnt(15)\n\tv_readfirstlane_b32 %0, %2\n\tv_readfirstlane_b32 %1, %3"
                 : "=s"(ep_lo), "=s"(ep_hi) : "v"(epoch[0]), "v"(epoch[1]) : "memory");
    arrive(lb, 0);
    setup();
    asm volatile("s_waitcnt vmcnt(5)" ::: "memory");
    arrive(lb, 1);
    char* const slot = ws + WS_SLOT0 + (size_t)(RFL(ep_lo) & 3u) * WS_SLOT;
    await(lds, 1, 4);
#pragma unroll
    for (int s = 0; s < 4; s++)
#pragma unroll
      for (int t = 0; t < 3; t++) hb[t][s] = h_frag(lds, t, s, lane);
    f32x4 acc[3][2];
#pragma unroll
    for (int t = 0; t < 3; t++) { acc[t][0] = f32x4{0, 0, 0, 0}; acc[t][1] = f32x4{0, 0, 0, 0}; }
#pragma unroll
    for (int s = 0; s < 4; s++) {
      const u32x4 fa = tr_frag(lds + L_W0, s, lane, 4 * u + (lane & 3), 0), fb = tr_frag(lds + L_W0, s, lane, 4 * u + (lane & 3), 1);
#pragma unroll
      for (int t = 0; t < 3; t++) { acc[t][0] = mfma16(fa, hb[t][s], acc[t][0]); acc[t][1] = mfma16(fb, hb[t][s], acc[t][1]); }
    }
    asm volatile("s_waitcnt vmcnt(0)" : "+v"(wdv), "+v"(wav), "+v"(wxv), "+v"(pf1), "+v"(pf2), "+v"(acc[2][1]) :: "memory");
    arrive(lb, 3);
    const __amdgpu_buffer_rsrc_t r_bw = rsrc(slot + WS_BT, N * 256);
#pragma unroll
    for (int t = 0; t < 3; t++) {
      const f32x4 x = acc[t][0] * LOG2E, y = acc[t][1] * LOG2E;
      const u32x4 o = {pk_bf16(x[0], x[1]), pk_bf16(x[2], x[3]), pk_bf16(y[0], y[1]), pk_bf16(y[2], y[3])};
      __builtin_amdgcn_raw_buffer_store_b128(o, r_bw, (t < 2 || c16 < 8) ? (nb0 + tile_node(t, c16)) * 256 + u * 64 + g4 * 16 : OOB, 0, SC1);
    }
    await(lds, 4, 4);
    dma4(s_we, wv0, wv1, 0, lb + L_W1 + u * 4096);
    dma4(s_we, wv0, wv1, 16384, lb + L_W1 + 16384 + u * 4096);
    await(lds, 3, 4);
    dma4(s_wh, wv0, wv1, 0, lb + L_W0 + u * 4096);
    dma4(s_wh, wv0, wv1, 16384, lb + L_W0 + 16384 + u * 4096);
    {
      const float wd = bf2f(wdv) * LOG2E;
      const u32 wdh = pk_bf16(wd, 0.f);
      const u32 wdp = wdh | (pk_bf16(wd - bf2f(wdh), 0.f) << 16), wap = pk_bf16(bf2f(wav) * LOG2E, 0.f);
#pragma unroll
      for (int i = 0; i < 4; i++) {
        char* kb = lds + L_KB + (i * 256 + tid) * 16;
        *(u32*)(kb + 12) = wdp;
        *(u16*)(kb + 10) = (u16)wap;
      }
      if (tid < D) *(u16*)(lds + L_WX + tid * 2) = (u16)wxv;
    }
    asm volatile("s_waitcnt vmcnt(16)" ::: "memory");
    if (lane == 0 && __hip_atomic_fetch_add((u32*)(lds + L_SY) + 5, 1u, __ATOMIC_RELAXED, __HIP_MEMORY_SCOPE_WORKGROUP) == 3)
      __hip_atomic_store((gu64*)(slot + WS_FL) + b, ((((u64)ep_hi << 32) | ep_lo) + 1) & 0xfffffffffffull, RLX_AGENT);
    asm volatile("s_waitcnt vmcnt(0)" ::: "memory");
  } else {
    const int t1 = tid - 256;
    const u32x4 z = {0, 0, 0, 0};
#pragma unroll
    for (int i = 0; i < 10; i++) if (i < 9 || L_SM + i * 4096 + t1 * 16 < L_POS) *(u32x4*)(lds + L_SM + i * 4096 + t1 * 16) = z;
    setup();
    asm volatile("s_waitcnt vmcnt(7)" : "+v"(ed[0]), "+v"(ed[1]), "+v"(ed[2]) :: "memory");
    u32 rank[3], ebk[3];
#pragma unroll
    for (int i = 0; i < 3; i++) {
      ebk[i] = ed[i] / NPB;
      rank[i] = 0;
      if (t1 + 256 * i < ne) rank[i] = atomicAdd((u32*)(lds + L_BC) + ebk[i], 1u);
    }
    arrive(lb, 0);
    asm volatile("s_waitcnt vmcnt(0)\n\tv_readfirstlane_b32 %0, %8\n\tv_readfirstlane_b32 %1, %9"
                 : "=s"(ep_lo), "=s"(ep_hi), "+v"(es[0]), "+v"(es[1]), "+v"(es[2]), "+v"(ee[0]), "+v"(ee[1]), "+v"(ee[2])
                 : "v"(epoch[0]), "v"(epoch[1]) : "memory");
    ep_lo = RFL(ep_lo); ep_hi = RFL(ep_hi);
    char* const slot = ws + WS_SLOT0 + (size_t)(ep_lo & 3u) * WS_SLOT;
    const u64 tag = ((((u64)ep_hi << 32) | ep_lo) + 1) & 0xfffffffffffull;
    u32 tg[3];
    rec_tags(tag, tg);
    await(lds, 0, 8);
    const u32x4 c4 = *(const u32x4*)(lds + L_BC + lane * 16);
    const u32 cown = *(const u32*)(lds + L_BC + t1 * 4);
    u32x2 pd[3], ps[3];
#pragma unroll
    for (int i = 0; i < 3; i++) {
      const u32* qd = (const u32*)(lds + L_POS + ((ed[i] * 6) & ~3u));
      const u32* qs = (const u32*)(lds + L_POS + ((es[i] * 6) & ~3u));
      pd[i] = u32x2{qd[0], qd[1]}; ps[i] = u32x2{qs[0], qs[1]};
    }
    const u32 pown = *(const u16*)(lds + L_POS + min((nb0 * 3 + t1) * 2, MAXPOS - 2));
    const u32 tot = c4[0] + c4[1] + c4[2] + c4[3];
    const u32 ex = scan64(tot) - tot;
    u32* mine = (u32*)(lds + L_LS + u * 1024);
    *(u32x4*)(mine + lane * 4) = u32x4{ex, ex + c4[0], ex + c4[0] + c4[1], ex + c4[0] + c4[1] + c4[2]};
    __builtin_amdgcn_wave_barrier();
    u32 at[3];
#pragma unroll
    for (int i = 0; i < 3; i++) at[i] = L_RS + (mine[ebk[i]] + rank[i]) * 16;
    const u32 gword = (mine[t1] << 10) | cown;
#pragma unroll
    for (int i = 0; i < 3; i++) {
      const u32 shd = (ed[i] & 1u) * 16, shs = (es[i] & 1u) * 16;
      const u32 dxy = __builtin_amdgcn_alignbit(pd[i][1], pd[i][0], shd), sxy = __builtin_amdgcn_alignbit(ps[i][1], ps[i][0], shs);
      const u32 xy = pk_bf16(bf2f(dxy) - bf2f(sxy), bfhi(dxy) - bfhi(sxy));
      const u32 zz = pk_bf16(bf2f(pd[i][1] >> shd) - bf2f(ps[i][1] >> shs), 0.f);
      u32x4 rc = {es[i] | ((ed[i] - ebk[i] * NPB) << 14) | tg[0], ee[i] | tg[1], xy, zz | tg[2]};
      PIN(rc);
      if (t1 + 256 * i < ne) *(u32x4*)(lds + at[i]) = rc;
    }
    if (t1 < NPB * 3) *(u16*)(lds + L_PD + t1 * 2) = (u16)pown;
    arrive(lb, 2);
    await(lds, 2, 4);
    const __amdgpu_buffer_rsrc_t r_rw = rsrc(slot + WS_REC + (size_t)b * CH * 16, CH * 16);
    u32x4 rv[3];
#pragma unroll
    for (int i = 0; i < 3; i++) rv[i] = *(const u32x4*)(lds + L_RS + (t1 + 256 * i) * 16);
#pragma unroll
    for (int i = 0; i < 3; i++) __builtin_amdgcn_raw_buffer_store_b128(rv[i], r_rw, t1 * 16, i * 4096, SC1);
    arrive(lb, 4);
    __hip_atomic_store((gu64*)(slot + WS_GR) + (size_t)b * MAXG + t1, (tag << 20) | gword, RLX_AGENT);
#pragma unroll
    for (int i = 0; i < 5; i++) { const int o = L_POS + i * 4096 + t1 * 16; if (i < 4 || o < L_SRC) *(u32x4*)(lds + o) = z; }
    *(u32x4*)(lds + L_SRC + t1 * 16) = u32x4{OOB, OOB, OOB, OOB};
    if (t1 < 16) *(u32*)(lds + L_SRC + 4096 + t1 * 4) = OOB;
    await(lds, 1, 4);
#pragma unroll
    for (int s = 0; s < 4; s++)
#pragma unroll
      for (int t = 0; t < 3; t++) hb[t][s] = h_frag(lds, t, s, lane);
  }
  __syncthreads();
  ep_lo = RFL(ep_lo); ep_hi = RFL(ep_hi);
  const u64 ep = ((u64)ep_hi << 32) | ep_lo, tag = (ep + 1) & 0xfffffffffffull;
  char* const slot = ws + WS_SLOT0 + (size_t)(ep_lo & 3u) * WS_SLOT;
  const __amdgpu_buffer_rsrc_t r_bt = rsrc(slot + WS_BT, N * 256), r_rec = rsrc(slot + WS_REC, G * CH * 16);
  u32 tg[3];
  rec_tags(tag, tg);
  f32x4 cp[3];
  u32 pw[4] = {0, 0, 0, 0};
  {
    const int cs = wv * 32 + (lane & 31), csc = cs < G ? cs : G - 1;
    gu64* const gp = (gu64*)(slot + WS_GR) + (size_t)csc * MAXG + bk;
    gu64* const fp = (gu64*)(slot + WS_FL) + csc;
    u64 gv = __hip_atomic_load(gp, RLX_AGENT);
    SB();
    {
      f32x4 acc[3] = {f32x4{0, 0, 0, 0}, f32x4{0, 0, 0, 0}, f32x4{0, 0, 0, 0}};
#pragma unroll
      for (int s = 0; s < 4; s++) {
        const u32x4 fw = tr_frag(lds + L_W1, s, lane, 2 * wv + ((lane & 3) >> 1), lane & 1);
#pragma unroll
        for (int t = 0; t < 3; t++) acc[t] = mfma16(hb[t][s], fw, acc[t]);
      }
      char* kb = lds + L_KB + (16 * wv + c16) * 16;
#pragma unroll
      for (int t = 0; t < 2; t++) {
        const f32x4 a = acc[t] * LOG2E;
        *(u32x2*)(kb + (4 * t + g4) * 2048) = u32x2{pk_bf16(a[0], a[1]), pk_bf16(a[2], a[3])};
      }
      if (g4 < 2) {
#pragma unroll
        for (int i = 0; i < 4; i++) *(u16*)(kb + (4 * g4 + i) * 2048 + 8) = (u16)pk_bf16(acc[2][i] * LOG2E, 0.f);
      }
    }
    SB();
    for (u32 spins = 0; !__all((gv >> 20) == tag) && spins < SPINS; spins++) {
      __builtin_amdgcn_s_sleep(4);
      if ((gv >> 20) != tag) gv = __hip_atomic_load(gp, RLX_AGENT);
    }
    const bool on = lane < 32 && cs < G;
    const u32 cnt = on ? ((u32)gv & 1023u) : 0u, st = ((u32)gv >> 10) & 1023u;
    const u32 inc = scan64(cnt);
    const u32 total = __builtin_amdgcn_readlane(inc, 63);
    u32* list = (u32*)(lds + L_LS + wv * 768);
    const u32 base = (cs * CH + st) * 16, p0 = inc - cnt;
    const u32 lim = min(cnt, p0 < 128 ? 128 - p0 : 0u);
#pragma unroll
    for (u32 k = 0; k < 6; k++) list[k < lim ? p0 + k : 128 + lane] = base + k * 16;
    if (__any(lim > 6)) {
#pragma unroll 1
      for (u32 k = 6; k < lim; k++) list[p0 + k] = base + k * 16;
    }
    __builtin_amdgcn_wave_barrier();
    {
      const u32 a0 = list[lane], a1 = list[64 + lane];
      const bool v0 = lane < total, v1 = 64 + lane < total;
      u32x4 r0 = __builtin_amdgcn_raw_buffer_load_b128(r_rec, v0 ? a0 : OOB, 0, SC1);
      u32x4 r1 = __builtin_amdgcn_raw_buffer_load_b128(r_rec, v1 ? a1 : OOB, 0, SC1);
      u64 fv = __hip_atomic_load(fp, RLX_AGENT);
      if (on) *(u32*)(lds + L_BC + cs * 4) = (u32)gv & 0xfffffu;
      SB();
#pragma unroll
      for (int t = 0; t < 3; t++) cp[t] = f32x4{0, 0, 0, 0};
#pragma unroll
      for (int s = 0; s < 4; s++) {
        const u32x4 f1 = tr_frag(lds + L_W0, s, lane, 2 * wv + ((lane & 3) >> 1), lane & 1);
#pragma unroll
        for (int t = 0; t < 3; t++) cp[t] = mfma16(f1, hb[t][s], cp[t]);
      }
#pragma unroll
      for (int t = 0; t < 3; t++) { cp[t] *= LOG2E; PIN(cp[t]); }
      SB();
      r0 = settle(r0, v0, r_rec, a0, tg);
      r1 = settle(r1, v1, r_rec, a1, tg);
      u32 k0 = 0, k1 = 0;
      if (v0) k0 = take_rank(lds, r0);
      if (v1) k1 = take_rank(lds, r1);
      for (u32 spins = 0; !__all(fv == tag) && spins < SPINS; spins++) {
        __builtin_amdgcn_s_sleep(4);
        fv = __hip_atomic_load(fp, RLX_AGENT);
      }
      __builtin_amdgcn_s_waitcnt(0x0f70);
      pw[0] = __builtin_amdgcn_raw_buffer_load_b32(r_bt, v0 && k0 < PFR ? (r0[0] & 0x3fffu) << 8 : OOB, 0, SC1);
      pw[1] = __builtin_amdgcn_raw_buffer_load_b32(r_bt, v0 && k0 < PFR ? ((r0[0] & 0x3fffu) << 8) + 128 : OOB, 0, SC1);
      pw[2] = __builtin_amdgcn_raw_buffer_load_b32(r_bt, v1 && k1 < PFR ? (r1[0] & 0x3fffu) << 8 : OOB, 0, SC1);
      pw[3] = __builtin_amdgcn_raw_buffer_load_b32(r_bt, v1 && k1 < PFR ? ((r1[0] & 0x3fffu) << 8) + 128 : OOB, 0, SC1);
      SB();
      if (v0) put_rec(lds, r0, k0);
      if (v1) put_rec(lds, r1, k1);
    }
    if (__builtin_expect(total > 128, 0)) {
#pragma unroll 1
      for (u32 done = 128; done < total; done += 64) {
        if (__hip_atomic_load((u32*)(lds + L_CNT + 32), __ATOMIC_RELAXED, __HIP_MEMORY_SCOPE_WORKGROUP)) break;
        __builtin_amdgcn_wave_barrier();
#pragma unroll 1
        for (u32 k = p0 < done ? done - p0 : 0u; k < cnt && p0 + k < done + 64; k++) list[p0 + k - done] = base + k * 16;
        __builtin_amdgcn_wave_barrier();
        const bool v0 = done + lane < total;
        const u32 a0 = list[lane];
        u32x4 r0 = __builtin_amdgcn_raw_buffer_load_b128(r_rec, v0 ? a0 : OOB, 0, SC1);
        r0 = settle(r0, v0, r_rec, a0, tg);
        if (v0) put_rec(lds, r0, take_rank(lds, r0));
      }
      __builtin_amdgcn_s_waitcnt(0x0f70);
    }
  }
  __syncthreads();
  const u32* wcnt = (const u32*)(lds + L_CNT);
  const u32 ci = wcnt[lane >> 3], cj = wcnt[lane & 7];
  const int ovf = RFL(wcnt[8]);
  auto load_wh2 = [&]() {
    const u32x4 s_wh = srd(w_h, 65536);
    dma4(s_wh, wv0 + (wv >> 2) * 16384, wv1 + (wv >> 2) * 16384, 32768, lb + L_W0 + wv * 4096);
  };
  u32x4 v[8], wxf[8];
  int win;
  {
    const u64 gt = __ballot(cj * 8 + (lane & 7) > ci * 8 + (lane >> 3));
    const u32 rk = __builtin_popcount((u32)(gt >> (lane & 56)) & 0xffu);
    const int wl = __builtin_ctzll(__ballot(rk == (u32)(wv < 4 ? wv : 11 - wv)));
    win = wl >> 3;
    const int cntw = ovf ? 1 : __builtin_amdgcn_readlane(ci, wl);
    const int nsteps = (cntw + 15) >> 4;
    const u32 lo = r * 4;
    u32 srca = L_SRC + win * CAPW * 4 + hh * 32;
    u32 GA[8], GB[8], off[8];
    const bool cfon = rp == hh;
    u32 cfa = cfon ? (L_CF + (win * CAPW + 8 * rh + rj) * 16) : L_ZERO;
    const u32 cfs = cfon ? 256u : 0u;
    u32 sma = L_SM + (win * CAPW + 8 * hh + (c16 >> 2)) * 40 + (g4 & 1) * 32 + (lane & 3) * 8;
    u32x4 cf, kbA, kbB, sf = {0, 0, 0, 0}, mB0 = {0, 0, 0, 0}, mB1 = {0, 0, 0, 0}, mA0, mA1;
    f32x16 yA, yB;
    {
      const u32x4 i0 = *(const u32x4*)(lds + srca), i1 = *(const u32x4*)(lds + srca + 16);
      const u32x4 i2 = *(const u32x4*)(lds + srca + 64), i3 = *(const u32x4*)(lds + srca + 80);
      cf = *(const u32x4*)(lds + cfa);
      kbA = *(const u32x4*)(lds + L_KB + win * 2048 + (2 * r + hh) * 16);
      kbB = *(const u32x4*)(lds + L_KB + win * 2048 + (64 + 2 * r + hh) * 16);
      srca += 128;
      SB();
#pragma unroll
      for (int j = 0; j < 4; j++) { off[j] = i0[j] + lo; off[4 + j] = i1[j] + lo; }
#pragma unroll
      for (int j = 0; j < 8; j++) GA[j] = __builtin_amdgcn_raw_buffer_load_b32(r_bt, off[j], 0, SC1);
      SB();
#pragma unroll
      for (int j = 0; j < 8; j++) GB[j] = __builtin_amdgcn_raw_buffer_load_b32(r_bt, off[j] + 128, 0, SC1);
      SB();
      asm volatile("" :: "v"(pw[0]), "v"(pw[1]), "v"(pw[2]), "v"(pw[3]));
      yA = mfma32(cf, kbA, zero);
#pragma unroll
      for (int j = 0; j < 4; j++) { off[j] = i2[j] + lo; off[4 + j] = i3[j] + lo; }
      yA = mfma32(sel0, u32x4{GA[0], GA[1], GA[2], GA[3]}, yA);
      yA = mfma32(sel1, u32x4{GA[4], GA[5], GA[6], GA[7]}, yA);
      PIN(yA);
      SB();
#pragma unroll
      for (int j = 0; j < 8; j++) GA[j] = __builtin_amdgcn_raw_buffer_load_b32(r_bt, off[j], 0, SC1);
      SB();
    }
#define MF(d, a, bb, c) do { SB(); d = mfma32(a, bb, c); PIN(d); SB(); } while (0)
#define SA(y, i, j) do { p_ = f32x2{__builtin_amdgcn_exp2f(-y[i]), __builtin_amdgcn_exp2f(-y[(i) + 1])}; \
                         q_ = f32x2{__builtin_amdgcn_exp2f(-y[j]), __builtin_amdgcn_exp2f(-y[(j) + 1])}; SB(); p_ += 1.0f; q_ += 1.0f; SB(); } while (0)
#define SBB(y, i, j, o0, o1) do { p_ = f32x2{__builtin_amdgcn_rcpf(p_[0]), __builtin_amdgcn_rcpf(p_[1])}; \
                         q_ = f32x2{__builtin_amdgcn_rcpf(q_[0]), __builtin_amdgcn_rcpf(q_[1])}; SB(); \
                         p_ *= f32x2{y[i], y[(i) + 1]}; q_ *= f32x2{y[j], y[(j) + 1]}; SB(); \
                         u32 t0_ = pk_bf16(p_[0], p_[1]), t1_ = pk_bf16(q_[0], q_[1]); \
                         asm volatile("" : "+v"(t0_), "+v"(t1_)); o0 = t0_; o1 = t1_; SB(); } while (0)
#define S4(y, i, j, o0, o1) do { SA(y, i, j); SBB(y, i, j, o0, o1); } while (0)
#define LD2(Gx, j, add) do { Gx[j] = __builtin_amdgcn_raw_buffer_load_b32(r_bt, off[j] + (add), 0, SC1); \
                             Gx[(j) + 1] = __builtin_amdgcn_raw_buffer_load_b32(r_bt, off[(j) + 1] + (add), 0, SC1); SB(); } while (0)
    f32x2 p_, q_;
    for (int s = 1; s < nsteps; s++) {
      MF(yB, cf, kbB, zero);
      S4(yA, 0, 2, mA0[0], mA0[1]);
      MF(yB, sel0, (u32x4{GB[0], GB[1], GB[2], GB[3]}), yB);
      cfa += cfs;
      cf = *(const u32x4*)(lds + cfa);
      SA(yA, 4, 6);
      LD2(GB, 0, 128);
      SBB(yA, 4, 6, mA0[2], mA0[3]);
      LD2(GB, 2, 128);
      MF(yB, sel1, (u32x4{GB[4], GB[5], GB[6], GB[7]}), yB);
      SA(yA, 8, 10);
      LD2(GB, 4, 128);
      MF(aB0, mB0, sf, aB0);
      SBB(yA, 8, 10, mA1[0], mA1[1]);
      LD2(GB, 6, 128);
      const u32x4 i0 = *(const u32x4*)(lds + srca), i1 = *(const u32x4*)(lds + srca + 16);
      srca += 64;
      SA(yA, 12, 14);
      MF(aB1, mB1, sf, aB1);
      sf = s_frag(lds + sma);
      sma += 640;
      SBB(yA, 12, 14, mA1[2], mA1[3]);
      MF(yA, cf, kbA, zero);
      S4(yB, 0, 2, mB0[0], mB0[1]);
      MF(yA, sel0, (u32x4{GA[0], GA[1], GA[2], GA[3]}), yA);
#pragma unroll
      for (int j = 0; j < 4; j++) { off[j] = i0[j] + lo; off[4 + j] = i1[j] + lo; }
      SA(yB, 4, 6);
      LD2(GA, 0, 0);
      SBB(yB, 4, 6, mB0[2], mB0[3]);
      LD2(GA, 2, 0);
      MF(yA, sel1, (u32x4{GA[4], GA[5], GA[6], GA[7]}), yA);
      SA(yB, 8, 10);
      LD2(GA, 4, 0);
      MF(aA0, mA0, sf, aA0);
      SBB(yB, 8, 10, mB1[0], mB1[1]);
      LD2(GA, 6, 0);
      SA(yB, 12, 14);
      MF(aA1, mA1, sf, aA1);
      SBB(yB, 12, 14, mB1[2], mB1[3]);
    }
    {
      const int rem = cntw - 16 * (nsteps - 1);
      load_wh2();
      MF(yB, cf, kbB, zero);
      mA0 = u32x4{0, 0, 0, 0}; mA1 = u32x4{0, 0, 0, 0};
      S4(yA, 0, 8, mA0[0], mA1[0]);
      MF(yB, sel0, (u32x4{GB[0], GB[1], GB[2], GB[3]}), yB);
      if (rem > 4) S4(yA, 2, 10, mA0[1], mA1[1]);
      MF(yB, sel1, (u32x4{GB[4], GB[5], GB[6], GB[7]}), yB);
      if (rem > 8) S4(yA, 4, 12, mA0[2], mA1[2]);
      MF(aB0, mB0, sf, aB0);
      if (rem > 12) S4(yA, 6, 14, mA0[3], mA1[3]);
      MF(aB1, mB1, sf, aB1);
      sf = s_frag(lds + sma);
      mB0 = u32x4{0, 0, 0, 0}; mB1 = u32x4{0, 0, 0, 0};
      S4(yB, 0, 8, mB0[0], mB1[0]);
      MF(aA0, mA0, sf, aA0);
      if (rem > 4) S4(yB, 2, 10, mB0[1], mB1[1]);
      MF(aA1, mA1, sf, aA1);
      if (rem > 8) S4(yB, 4, 12, mB0[2], mB1[2]);
      if (rem > 12) S4(yB, 6, 14, mB0[3], mB1[3]);
      MF(aB0, mB0, sf, aB0);
      MF(aB1, mB1, sf, aB1);
    }
#pragma unroll
    for (int q8 = 0; q8 < 8; q8++) {
      const f32x16& a0 = q8 < 4 ? aA0 : aB0;
      const f32x16& a1 = q8 < 4 ? aA1 : aB1;
#pragma unroll
      for (int q = 0; q < 4; q++) v[q8][q] = pk_bf16(a0[4 * (q8 & 3) + q], a1[4 * (q8 & 3) + q]);
    }
    if (r < 20 && (r & 3) == 0 && !ovf) {
      char* mi = lds + mi_row(r * 2 + win) + 16 * hh;
#pragma unroll
      for (int q8 = 0; q8 < 8; q8++) *(u32x4*)(mi + 32 * q8) = v[q8];
    }
  }
  if (__builtin_expect(ovf, 0)) {
    float* macc = (float*)(lds + L_SM);
    float* pacc = (float*)(lds + L_SM + NPB * D * 4);
    __syncthreads();
    for (int i = tid; i < NPB * D + NPB * 4; i += NT) macc[i] = 0.f;
    __syncthreads();
    const u32 wxp = *(const u32*)(lds + L_WX + lane * 4);
    const float wx0 = bf2f(wxp), wx1 = bfhi(wxp);
    const u32x4 kc0 = *(const u32x4*)(lds + L_KB + (2 * lane) * 16), kc1 = *(const u32x4*)(lds + L_KB + (2 * lane + 1) * 16);
    const float wd0 = bf2f(kc0[3]) + bfhi(kc0[3]), wd1 = bf2f(kc1[3]) + bfhi(kc1[3]);
    const float wa0 = bfhi(kc0[2]), wa1 = bfhi(kc1[2]);
    u32 rot = 0;
    for (int c = 0; c < G; c++) {
      const u32 oo = RFL(*(const u32*)(lds + L_BC + c * 4));
      const u32 s0 = oo >> 10, n = oo & 1023u;
      for (u32 k = (wv - rot) & 7u; k < n; k += 8) {
        u32x4 rr = __builtin_amdgcn_raw_buffer_load_b128(r_rec, (c * CH + s0 + k) * 16, 0, SC1);
        rr = settle(rr, true, r_rec, (c * CH + s0 + k) * 16, tg);
        const u32 nl = (rr[0] >> 14) & 0x3fu, sl = nl >> 3, wn = nl & 7u;
        const u32 bv = __builtin_amdgcn_raw_buffer_load_b32(r_bt, ((rr[0] & 0x3fffu) << 8) + lane * 4, 0, SC1);
        const float d2 = bfhi(dist2(rr[2], rr[3])), eav = bf2f(rr[1]);
        const float a0 = bf2f(*(const u16*)(lds + L_KB + wn * 2048 + (2 * lane) * 16 + sl * 2));
        const float a1 = bf2f(*(const u16*)(lds + L_KB + wn * 2048 + (2 * lane + 1) * 16 + sl * 2));
        const float y0 = a0 + bf2f(bv) + d2 * wd0 + eav * wa0, y1 = a1 + bfhi(bv) + d2 * wd1 + eav * wa1;
        const u32 mp = silu2(y0, y1);
        const float m0 = bf2f(mp), m1 = bfhi(mp);
        atomicAdd(&macc[nl * D + 2 * lane], m0);
        atomicAdd(&macc[nl * D + 2 * lane + 1], m1);
        float csv = m0 * wx0 + m1 * wx1;
#pragma unroll
        for (int o = 32; o > 0; o >>= 1) csv += __shfl_xor(csv, o, 64);
        if (lane < 3) atomicAdd(&pacc[nl * 4 + lane], csv * (lane == 0 ? bf2f(rr[2]) : lane == 1 ? bfhi(rr[2]) : bf2f(rr[3])));
      }
      rot += n;
    }
    __syncthreads();
    for (int i = tid; i < NPB * D / 2; i += NT) {
      const int n = i >> 6, f = (i & 63) * 2;
      *(u32*)(lds + mi_row(n) + f * 2) = pk_bf16(macc[n * D + f], macc[n * D + f + 1]);
    }
    if (tid < NPB * 3) {
      const int n = tid / 3, c = tid - n * 3;
      if (nb0 + n < N) out[(size_t)N * D + (size_t)nb0 * 3 + tid] = (u16)pk_bf16(bf2f(*(const u16*)(lds + L_PD + tid * 2)) + LN2 * pacc[n * 4 + c], 0.f);
    }
  }
  u32x2 hr[3];
  u32x4 f2[4];
#pragma unroll
  for (int t = 0; t < 3; t++) {
    const int n = tile_node(t, c16);
    hr[t] = *(const u32x2*)(lds + L_HS + n * 256 + 16 * ((2 * wv + (g4 >> 1)) ^ hswz(n)) + 8 * (g4 & 1));
  }
#pragma unroll
  for (int q8 = 0; q8 < 8; q8++) wxf[q8] = *(const u32x4*)(lds + L_WX + (16 * q8 + 8 * hh) * 2);
  const int kind = lane & 3, pnl = (lane >> 2) * 8 + win;
  const bool pon = kind != 0 && lane < 20 && !ovf;
  const u32 p0 = *(const u16*)(lds + L_PD + (pon ? pnl * 6 + kind * 2 - 2 : 0));
  const int mia = mi_row(tile_node(0, c16)) + g4 * 16, mib = mi_row(tile_node(2, c16)) + g4 * 16;
  asm volatile("s_waitcnt vmcnt(0)" ::: "memory");
  __syncthreads();
  {
    u32x4 mb[3][4];
#pragma unroll
    for (int s = 0; s < 4; s++) {
      f2[s] = tr_frag(lds + L_W0, s, lane, 2 * wv + ((lane & 3) >> 1), lane & 1);
      mb[0][s] = *(const u32x4*)(lds + mia + s * 64);
      mb[1][s] = *(const u32x4*)(lds + mia + 8192 + s * 64);
      mb[2][s] = *(const u32x4*)(lds + mib + s * 64);
    }
    SB();
#pragma unroll
    for (int s = 0; s < 4; s++)
#pragma unroll
      for (int t = 0; t < 3; t++) cp[t] = mfma16(f2[s], mb[t][s], cp[t]);
    f32x16 z = zero, z1 = zero;
#pragma unroll
    for (int t = 0; t < 3; t++) {
      const f32x4 y = cp[t];
      f32x4 e;
#pragma unroll
      for (int i = 0; i < 4; i++) e[i] = __builtin_amdgcn_exp2f(-y[i]);
      SB();
      if (t & 1) { z1 = mfma32(wxf[3 * t], v[3 * t], z1); PIN(z1); } else { z = mfma32(wxf[3 * t], v[3 * t], z); PIN(z); }
      SB();
      e += 1.0f;
#pragma unroll
      for (int i = 0; i < 4; i++) e[i] = __builtin_amdgcn_rcpf(e[i]);
      SB();
      if (t & 1) { z = mfma32(wxf[3 * t + 1], v[3 * t + 1], z); PIN(z); } else { z1 = mfma32(wxf[3 * t + 1], v[3 * t + 1], z1); PIN(z1); }
      SB();
      const f32x4 o = f32x4{bf2f(hr[t][0]), bfhi(hr[t][0]), bf2f(hr[t][1]), bfhi(hr[t][1])} + (y * LN2) * e;
      *(u32x2*)(lds + L_SM + tile_node(t, c16) * 272 + wv * 32 + g4 * 8) = u32x2{pk_bf16(o[0], o[1]), pk_bf16(o[2], o[3])};
      SB();
      if (t == 0) { z = mfma32(wxf[2], v[2], z); PIN(z); }
      if (t == 1) { z1 = mfma32(wxf[5], v[5], z1); PIN(z1); }
      SB();
    }
    __syncthreads();
    const __amdgpu_buffer_rsrc_t r_out = rsrc(out, N * 262);
    const u32x4 o0 = *(const u32x4*)(lds + L_SM + (tid >> 4) * 272 + (tid & 15) * 16);
    const u32x4 o1 = *(const u32x4*)(lds + L_SM + (32 + ((tid >> 4) & 7)) * 272 + (tid & 15) * 16);
    const u32 oo = nb0 * 256 + tid * 16;
    __builtin_amdgcn_raw_buffer_store_b128(o0, r_out, oo < N * 256 ? oo : OOB, 0, 0);
    __builtin_amdgcn_raw_buffer_store_b128(o1, r_out, tid < 128 && oo + 8192 < N * 256 ? oo + 8192 : OOB, 0, 0);
    __builtin_amdgcn_raw_buffer_store_b16((u16)pk_bf16(bf2f(p0) + LN2 * (z[0] + z1[0]), 0.f), r_out,
                                          pon ? N * 256 + ((nb0 + pnl) * 3 + kind - 1) * 2 : OOB, 0, 0);
  }
  if (b == 0 && tid == 0) __hip_atomic_store((gu64*)(ws + WS_EP), ep + 1, RLX_AGENT);
}

extern "C" void kernel_launch(void* const* d_in, const int* in_sizes, int n_in, void* d_out, int out_size, void* d_ws,
                              size_t ws_size, hipStream_t stream) {
  const int N = in_sizes[0] / D, E = in_sizes[3];
  const int NB = (N + NPB - 1) / NPB, C = (E + CH - 1) / CH;
  const int G = NB > C ? NB : C;
  k_egnn<<<G, NT, 0, stream>>>((const u16*)d_in[0], (const u16*)d_in[1], (const u16*)d_in[2], (const int*)d_in[3],
                               (const int*)d_in[4], (const u16*)d_in[5], (const u16*)d_in[6], (const u16*)d_in[7],
                               (char*)d_ws, (u16*)d_out, N, E, G);
}
```

```cpp
#include <hip/hip_runtime.h>
#include <hip/hip_bf16.h>

typedef unsigned int u32;
typedef unsigned short u16;
typedef unsigned long long u64;
typedef u32 u32x2 __attribute__((ext_vector_type(2)));
typedef u32 u32x4 __attribute__((ext_vector_type(4)));
typedef short s16x4 __attribute__((ext_vector_type(4)));
typedef short bf16x8 __attribute__((ext_vector_type(8)));
typedef float f32x2 __attribute__((ext_vector_type(2)));
typedef float f32x4 __attribute__((ext_vector_type(4)));
typedef float f32x16 __attribute__((ext_vector_type(16)));
typedef __bf16 bf16x2_t __attribute__((ext_vector_type(2)));
typedef __attribute__((address_space(1))) u64 gu64;
typedef __attribute__((address_space(3))) s16x4 lds_s16x4;
typedef __attribute__((address_space(3))) char lds_char;
#define DI __device__ __forceinline__
#define RLX_AGENT __ATOMIC_RELAXED, __HIP_MEMORY_SCOPE_AGENT
#define SC1 16
#define SB() __builtin_amdgcn_sched_barrier(0)
#define PIN(x) asm volatile("" : "+v"(x))
#define RFL(x) __builtin_amdgcn_readfirstlane(x)

constexpr int D = 128;
constexpr int NT = 512;
constexpr int NPB = 40;
constexpr int CH = 640;
constexpr int MAXG = 256;
constexpr int CAPW = 128;
constexpr int MAXPOS = 61440;
constexpr u32 PFR = 16;
constexpr u32 SPINS = 1u << 16;
constexpr float LOG2E = 1.4426950408889634f;
constexpr float LN2 = 0.6931471805599453f;
constexpr u32 OOB = 0xfffff000u;

constexpr size_t WS_EP = 0;
constexpr size_t WS_SLOT0 = (size_t)2 << 20, WS_SLOT = (size_t)6 << 20;
constexpr size_t WS_GR = 0;
constexpr size_t WS_BT = WS_GR + (size_t)MAXG * MAXG * 8;
constexpr size_t WS_REC = WS_BT + (size_t)MAXG * NPB * 256;
constexpr size_t WS_FL = WS_REC + (size_t)MAXG * CH * 16;
static_assert(WS_FL + MAXG * 8 <= WS_SLOT, "slot");

DI float bf2f(u32 b) { return __builtin_bit_cast(float, b << 16); }
DI float bfhi(u32 b) { return __builtin_bit_cast(float, b & 0xffff0000u); }
DI u32 pk_bf16(float lo, float hi) {
  f32x2 v = {lo, hi};
  bf16x2_t r = __builtin_convertvector(v, bf16x2_t);
  return __builtin_bit_cast(u32, r);
}
DI f32x4 mfma16(u32x4 a, u32x4 b, f32x4 c) {
  return __builtin_amdgcn_mfma_f32_16x16x32_bf16(__builtin_bit_cast(bf16x8, a), __builtin_bit_cast(bf16x8, b), c, 0, 0, 0);
}
DI f32x16 mfma32(u32x4 a, u32x4 b, f32x16 c) {
  return __builtin_amdgcn_mfma_f32_32x32x16_bf16(__builtin_bit_cast(bf16x8, a), __builtin_bit_cast(bf16x8, b), c, 0, 0, 0);
}
DI __amdgpu_buffer_rsrc_t rsrc(const void* p, u32 bytes) { return __builtin_amdgcn_make_buffer_rsrc((void*)p, 0, bytes, 0x00020000); }
DI u32x4 srd(const void* p, u32 bytes) { const u64 a = (u64)p; return u32x4{(u32)a, (u32)(a >> 32) & 0xffffu, bytes, 0x00020000u}; }
DI void dma4(u32x4 sd, u32 v0, u32 v1, u32 soff, u32 ldsa) {
  u32 keep;
  asm volatile("s_mov_b32 %0, m0\n\ts_mov_b32 m0, %1\n\ts_nop 0\n\t"
               "buffer_load_dwordx4 %2, %4, %5 offen lds\n\tbuffer_load_dwordx4 %2, %4, %5 offen offset:1024 lds\n\t"
               "buffer_load_dwordx4 %3, %4, %5 offen offset:2048 lds\n\tbuffer_load_dwordx4 %3, %4, %5 offen offset:3072 lds\n\t"
               "s_mov_b32 m0, %0"
               : "=&s"(keep) : "s"(RFL(ldsa)), "v"(v0), "v"(v1), "s"(sd), "s"(soff) : "memory");
}
DI void dma2(u32x4 sd, u32 v0, u32 soff, u32 ldsa) {
  u32 keep;
  asm volatile("s_mov_b32 %0, m0\n\ts_mov_b32 m0, %1\n\ts_nop 0\n\t"
               "buffer_load_dwordx4 %2, %3, %4 offen lds\n\tbuffer_load_dwordx4 %2, %3, %4 offen offset:1024 lds\n\t"
               "s_mov_b32 m0, %0"
               : "=&s"(keep) : "s"(RFL(ldsa)), "v"(v0), "s"(sd), "s"(soff) : "memory");
}
DI u32 scan64(u32 x) {
  x += __builtin_amdgcn_update_dpp(0u, x, 0x111, 0xf, 0xf, false);
  x += __builtin_amdgcn_update_dpp(0u, x, 0x112, 0xf, 0xf, false);
  x += __builtin_amdgcn_update_dpp(0u, x, 0x114, 0xf, 0xf, false);
  x += __builtin_amdgcn_update_dpp(0u, x, 0x118, 0xf, 0xf, false);
  x += __builtin_amdgcn_update_dpp(0u, x, 0x142, 0xa, 0xf, false);
  x += __builtin_amdgcn_update_dpp(0u, x, 0x143, 0xc, 0xf, false);
  return x;
}

constexpr int L_ZERO = 0;
constexpr int L_CNT = L_ZERO + 16;
constexpr int L_BC = L_CNT + 48;
constexpr int L_SY = L_BC + 1024;
constexpr int L_SM = L_SY + 32;
constexpr int L_CF = L_SM + 8 * 8 * 640;
constexpr int L_SRC = L_CF + 8 * CAPW * 16;
constexpr int L_KB = L_SRC + 8 * CAPW * 4 + 64;
constexpr int L_W1 = L_KB + 8 * D * 16;
constexpr int L_W0 = L_W1 + 32768;
constexpr int L_HS = L_W0 + 32768;
constexpr int L_WX = L_HS + NPB * 256;
constexpr int L_PD = L_WX + 256;
constexpr int L_LS = L_PD + 256;
constexpr int L_END = L_LS + 8 * 768;
constexpr int L_RS = L_W0 - CH * 16;
constexpr int L_POS = L_RS - MAXPOS;
static_assert(L_END <= 160 * 1024, "lds");
static_assert(L_POS >= L_SM + 36864 && L_POS <= L_CF && (L_POS % 16) == 0, "pos table");
static_assert((L_SM % 16) == 0 && (L_BC % 16) == 0 && (L_SRC % 16) == 0 && (L_KB % 16) == 0 && (L_W0 % 16) == 0 && (L_HS % 16) == 0 && (L_WX % 16) == 0 && (L_LS % 16) == 0, "align");

DI int tile_node(int t, int c) { return t < 2 ? ((c & 3) * 8 + 4 * t + (c >> 2)) : (32 + (c & 7)); }
DI int mi_row(int nl) { return L_KB + (nl & 7) * 2048 + (nl & 3) * 64 + (nl >> 3) * 272; }
DI int wswz(int k) { return ((k & 3) << 2) | ((k >> 2) & 2); }
DI int hswz(int n) { return ((n & 3) << 2) | ((n >> 3) & 3); }
DI u32x4 tr_frag(const char* img, int s, int lane, int ch, int hf) {
  const int k0 = 8 * (lane >> 4) + ((lane & 15) >> 2);
  const char* p = img + (32 * s + k0) * 256 + 16 * (ch ^ wswz(k0)) + 8 * hf;
  const s16x4 a = __builtin_amdgcn_ds_read_tr16_b64_v4i16((lds_s16x4*)p);
  const s16x4 b = __builtin_amdgcn_ds_read_tr16_b64_v4i16((lds_s16x4*)(p + 1024));
  const u32x2 x = __builtin_bit_cast(u32x2, a), y = __builtin_bit_cast(u32x2, b);
  return u32x4{x[0], x[1], y[0], y[1]};
}
DI u32x4 s_frag(const char* p) {
  const u32x2 x = __builtin_bit_cast(u32x2, __builtin_amdgcn_ds_read_tr16_b64_v4i16((lds_s16x4*)p));
  const u32x2 y = __builtin_bit_cast(u32x2, __builtin_amdgcn_ds_read_tr16_b64_v4i16((lds_s16x4*)(p + 160)));
  return u32x4{x[0], x[1], y[0], y[1]};
}
DI u32x4 h_frag(const char* lds, int t, int s, int lane) {
  const int n = tile_node(t, lane & 15);
  return *(const u32x4*)(lds + L_HS + n * 256 + 16 * ((4 * s + (lane >> 4)) ^ hswz(n)));
}
DI u32 dist2(u32 w2, u32 w3) {
  const float dx = bf2f(w2), dy = bfhi(w2), dz = bf2f(w3);
  const u32 q0 = pk_bf16(dx * dx, dy * dy), q1 = pk_bf16(dz * dz, 0.f);
  const float s2 = bf2f(q0) + bfhi(q0) + bf2f(q1);
  return pk_bf16(s2, s2);
}
DI u32 take_rank(char* lds, u32x4 rc) { return atomicAdd((u32*)(lds + L_CNT) + ((rc[0] >> 14) & 7u), 1u); }
DI void put_rec(char* lds, u32x4 rc, u32 rank) {
  const u32 win = (rc[0] >> 14) & 7u, slot = (rc[0] >> 17) & 7u;
  const u64 oh = 0x3f80ull << (slot * 16);
  const u32x4 cf = {slot < 4 ? (u32)oh : 0u, slot < 4 ? (u32)(oh >> 32) : 0u, (slot == 4 ? 0x3f80u : 0u) | (rc[1] << 16), dist2(rc[2], rc[3])};
  if (rank < CAPW) {
    const u32 k = ((rank & 1u) << 3) + ((rank & 15u) >> 1), sp = win * CAPW + (rank & ~15u) + k;
    *(u32*)(lds + L_SRC + sp * 4) = (rc[0] & 0x3fffu) << 8;
    *(u32x4*)(lds + L_CF + sp * 16) = cf;
    *(u32x2*)(lds + L_SM + sp * 40 + slot * 8) = u32x2{0x3f80u | (rc[2] << 16), __builtin_amdgcn_alignbit(rc[3], rc[2], 16)};
  } else *(u32*)(lds + L_CNT + 32) = 1;
}
DI void rec_tags(u64 tag, u32 (&t)[3]) {
  t[0] = (u32)tag << 20; t[1] = ((u32)tag >> 12) << 16; t[2] = ((u32)(tag >> 28) ^ (u32)tag) << 16;
}
DI bool rec_stale(u32x4 rc, const u32 (&t)[3]) { return (((rc[0] ^ t[0]) >> 20) | (((rc[1] ^ t[1]) | (rc[3] ^ t[2])) >> 16)) != 0; }
DI u32x4 settle(u32x4 rc, bool on, __amdgpu_buffer_rsrc_t r, u32 off, const u32 (&t)[3]) {
  for (u32 spins = 0; __any(on && rec_stale(rc, t)) && spins < SPINS; spins++) {
    __builtin_amdgcn_s_sleep(4);
    if (on && rec_stale(rc, t)) rc = __builtin_amdgcn_raw_buffer_load_b128(r, off, 0, SC1);
  }
  return rc;
}
DI void arrive(u32 lb, int i) {
  asm volatile("s_mov_b64 exec, 1\n\tds_add_u32 %0, %1 offset:%2\n\ts_mov_b64 exec, -1" :: "v"(lb), "v"(1u), "i"(L_SY + 4 * i) : "memory");
}
DI void await(char* lds, int i, u32 n) {
  asm volatile("" ::: "memory");
  while ((u32)RFL(__hip_atomic_load((u32*)(lds + L_SY) + i, __ATOMIC_RELAXED, __HIP_MEMORY_SCOPE_WORKGROUP)) < n) { }
  asm volatile("" ::: "memory");
}
DI u32 silu2(float a, float b) {
  f32x2 yy = {a, b};
  f32x2 e = {__builtin_amdgcn_exp2f(-a), __builtin_amdgcn_exp2f(-b)};
  e += 1.0f;
  f32x2 rc = {__builtin_amdgcn_rcpf(e[0]), __builtin_amdgcn_rcpf(e[1])};
  yy *= rc;
  return pk_bf16(yy[0], yy[1]);
}

__global__ __launch_bounds__(NT, 2) void k_egnn(const u16* __restrict__ h, const u16* __restrict__ pos,
                                                const u16* __restrict__ ea, const int* __restrict__ src,
                                                const int* __restrict__ dst, const u16* __restrict__ w_e,
                                                const u16* __restrict__ w_x, const u16* __restrict__ w_h,
                                                char* __restrict__ ws, u16* __restrict__ out, int N, int E, int G) {
  __shared__ __attribute__((aligned(16))) char lds[L_END];
  const int tid = threadIdx.x, lane = tid & 63, b = blockIdx.x;
  const int wv = RFL(tid >> 6);
  const int u = wv & 3, c16 = lane & 15, g4 = lane >> 4;
  const bool role1 = wv >= 4;
  const u32 lb = (u32)(size_t)(lds_char*)lds;
  const u64 tk0 = __builtin_amdgcn_s_memrealtime();
  if (tid < L_SM / 4) *(u32*)(lds + tid * 4) = 0;
  __syncthreads();
  const int ne = RFL(max(0, min(CH, E - b * CH)));
  u32 ed[3], es[3], ee[3];
  u32x2 epoch;
  {
    const u32x4 s_d = srd(dst + b * CH, ne * 4), s_s = srd(src + b * CH, ne * 4), s_e = srd(ea + b * CH, ne * 2);
    asm volatile("s_cmp_lt_u32 %15, 4\n\ts_cbranch_scc1 1f\n\t"
                 "buffer_load_dword %0, %10, %12, 0 offen\n\tbuffer_load_dword %1, %10, %12, 0 offen offset:1024\n\t"
                 "buffer_load_dword %2, %10, %12, 0 offen offset:2048\n\tbuffer_load_dword %3, %10, %13, 0 offen\n\t"
                 "buffer_load_dword %4, %10, %13, 0 offen offset:1024\n\tbuffer_load_dword %5, %10, %13, 0 offen offset:2048\n\t"
                 "buffer_load_ushort %6, %11, %14, 0 offen\n\tbuffer_load_ushort %7, %11, %14, 0 offen offset:512\n\t"
                 "buffer_load_ushort %8, %11, %14, 0 offen offset:1024\n"
                 "1:\n\tglobal_load_dwordx2 %9, %16, %17 sc1"
                 : "=&v"(ed[0]), "=&v"(ed[1]), "=&v"(ed[2]), "=&v"(es[0]), "=&v"(es[1]), "=&v"(es[2]), "=&v"(ee[0]), "=&v"(ee[1]), "=&v"(ee[2]), "=&v"(epoch)
                 : "v"((tid & 255) * 4), "v"((tid & 255) * 2), "s"(s_d), "s"(s_s), "s"(s_e), "s"(wv), "v"(0u), "s"(ws + WS_EP) : "memory", "scc");
  }
  int bk;
  { const int q = G >> 3, r = G & 7, x = b & 7, i = b >> 3;
    bk = (x < r ? x * (q + 1) : r * (q + 1) + (x - r) * q) + i; }
  const int nb0 = bk * NPB;
  const int r = lane & 31, hh = lane >> 5;
  const int rh = (r >> 2) & 1, rp = r >> 4, rj = (r & 3) | (((r >> 3) & 1) << 2);
  f32x16 aA0, aA1, aB0, aB1, zero;
  u32x4 sel0, sel1;
  auto setup = [&]() {
#pragma unroll
    for (int i = 0; i < 16; i++) zero[i] = 0.f;
    u32x4 z4 = {0, 0, 0, 0};
    PIN(z4);
    aA0 = mfma32(z4, z4, zero); aA1 = mfma32(z4, z4, zero); aB0 = mfma32(z4, z4, zero); aB1 = mfma32(z4, z4, zero);
    const u32 one = rh == hh ? 0x3f80u << (rp * 16) : 0u;
#pragma unroll
    for (int q = 0; q < 4; q++) { sel0[q] = (rj == q) ? one : 0u; sel1[q] = (rj == 4 + q) ? one : 0u; }
    PIN(aA0); PIN(aA1); PIN(aB0); PIN(aB1); PIN(sel0); PIN(sel1);
  };
  u32 ep_lo, ep_hi;
  u32x4 hb[3][4];
  const u32 wv0 = g4 * 256 + 16 * (c16 ^ (g4 << 2)) + u * 4096, wv1 = wv0 ^ 32;
  if (!role1) {
    const u32x4 s_pos = srd(pos, N * 6), s_h = srd(h, N * 256), s_we = srd(w_e, 258 * 256), s_wh = srd(w_h, 65536);
#pragma unroll
    for (int i = 0; i < 4; i++)
      if (i < 3 || u < 3) dma4(s_pos, lane * 16 + u * 4096, lane * 16 + u * 4096, i * 16384, lb + L_POS + i * 16384 + u * 4096);
    dma2(s_h, g4 * 256 + 16 * (c16 ^ ((g4 << 2) | u)) + u * 2048, nb0 * 256, lb + L_HS + u * 2048);
    if (u == 0) dma2(s_h, g4 * 256 + 16 * (c16 ^ (g4 << 2)) + 8192, nb0 * 256, lb + L_HS + 8192);
    dma4(s_we, wv0, wv1, 32768, lb + L_W0 + u * 4096);
    dma4(s_we, wv0, wv1, 32768 + 16384, lb + L_W0 + 16384 + u * 4096);
    u32 wdv, wav, wxv, pf1, pf2;
    {
      const u32x4 s_wx = srd(w_x, 256);
      const u32 xi = (b >> 3) & 31;
      asm volatile("buffer_load_ushort %0, %5, %6, %7 offen\n\tbuffer_load_ushort %1, %5, %6, %7 offen offset:256\n\t"
                   "buffer_load_ushort %2, %5, %8, 0 offen\n\tbuffer_load_dword %3, %9, %6, 0 offen\n\t"
                   "buffer_load_dword %4, %10, %11, 0 offen"
                   : "=&v"(wdv), "=&v"(wav), "=&v"(wxv), "=&v"(pf1), "=&v"(pf2)
                   : "v"((tid & 127) * 2), "s"(s_we), "s"(65536u), "s"(s_wx), "v"(tid < 8 ? (xi * 8 + tid) * 128 : OOB),
                     "v"(tid - 64 < 16u ? (xi * 16 + tid - 64) * 128 : OOB), "s"(s_wh) : "memory");
    }
    asm volatile("s_waitcnt vmcnt(15)\n\tv_readfirstlane_b32 %0, %2\n\tv_readfirstlane_b32 %1, %3"
                 : "=s"(ep_lo), "=s"(ep_hi) : "v"(epoch[0]), "v"(epoch[1]) : "memory");
    arrive(lb, 0);
    setup();
    asm volatile("s_waitcnt vmcnt(5)" ::: "memory");
    arrive(lb, 1);
    char* const slot = ws + WS_SLOT0 + (size_t)(RFL(ep_lo) & 3u) * WS_SLOT;
    await(lds, 1, 4);
#pragma unroll
    for (int s = 0; s < 4; s++)
#pragma unroll
      for (int t = 0; t < 3; t++) hb[t][s] = h_frag(lds, t, s, lane);
    f32x4 acc[3][2];
#pragma unroll
    for (int t = 0; t < 3; t++) { acc[t][0] = f32x4{0, 0, 0, 0}; acc[t][1] = f32x4{0, 0, 0, 0}; }
#pragma unroll
    for (int s = 0; s < 4; s++) {
      const u32x4 fa = tr_frag(lds + L_W0, s, lane, 4 * u + (lane & 3), 0), fb = tr_frag(lds + L_W0, s, lane, 4 * u + (lane & 3), 1);
#pragma unroll
      for (int t = 0; t < 3; t++) { acc[t][0] = mfma16(fa, hb[t][s], acc[t][0]); acc[t][1] = mfma16(fb, hb[t][s], acc[t][1]); }
    }
    asm volatile("s_waitcnt vmcnt(0)" : "+v"(wdv), "+v"(wav), "+v"(wxv), "+v"(pf1), "+v"(pf2), "+v"(acc[2][1]) :: "memory");
    arrive(lb, 3);
    const __amdgpu_buffer_rsrc_t r_bw = rsrc(slot + WS_BT, N * 256);
#pragma unroll
    for (int t = 0; t < 3; t++) {
      const f32x4 x = acc[t][0] * LOG2E, y = acc[t][1] * LOG2E;
      const u32x4 o = {pk_bf16(x[0], x[1]), pk_bf16(x[2], x[3]), pk_bf16(y[0], y[1]), pk_bf16(y[2], y[3])};
      __builtin_amdgcn_raw_buffer_store_b128(o, r_bw, (t < 2 || c16 < 8) ? (nb0 + tile_node(t, c16)) * 256 + u * 64 + g4 * 16 : OOB, 0, SC1);
    }
    await(lds, 4, 4);
    dma4(s_we, wv0, wv1, 0, lb + L_W1 + u * 4096);
    dma4(s_we, wv0, wv1, 16384, lb + L_W1 + 16384 + u * 4096);
    await(lds, 3, 4);
    dma4(s_wh, wv0, wv1, 0, lb + L_W0 + u * 4096);
    dma4(s_wh, wv0, wv1, 16384, lb + L_W0 + 16384 + u * 4096);
    {
      const float wd = bf2f(wdv) * LOG2E;
      const u32 wdh = pk_bf16(wd, 0.f);
      const u32 wdp = wdh | (pk_bf16(wd - bf2f(wdh), 0.f) << 16), wap = pk_bf16(bf2f(wav) * LOG2E, 0.f);
#pragma unroll
      for (int i = 0; i < 4; i++) {
        char* kb = lds + L_KB + (i * 256 + tid) * 16;
        *(u32*)(kb + 12) = wdp;
        *(u16*)(kb + 10) = (u16)wap;
      }
      if (tid < D) *(u16*)(lds + L_WX + tid * 2) = (u16)wxv;
    }
    asm volatile("s_waitcnt vmcnt(16)" ::: "memory");
    if (lane == 0 && __hip_atomic_fetch_add((u32*)(lds + L_SY) + 5, 1u, __ATOMIC_RELAXED, __HIP_MEMORY_SCOPE_WORKGROUP) == 3)
      __hip_atomic_store((gu64*)(slot + WS_FL) + b, (((((u64)ep_hi << 32) | ep_lo) + 1) & 0xfffffffffffull) | (tk0 << 44), RLX_AGENT);
    asm volatile("s_waitcnt vmcnt(0)" ::: "memory");
  } else {
    const int t1 = tid - 256;
    const u32x4 z = {0, 0, 0, 0};
#pragma unroll
    for (int i = 0; i < 10; i++) if (i < 9 || L_SM + i * 4096 + t1 * 16 < L_POS) *(u32x4*)(lds + L_SM + i * 4096 + t1 * 16) = z;
    setup();
    asm volatile("s_waitcnt vmcnt(7)" : "+v"(ed[0]), "+v"(ed[1]), "+v"(ed[2]) :: "memory");
    u32 rank[3], ebk[3];
#pragma unroll
    for (int i = 0; i < 3; i++) {
      ebk[i] = ed[i] / NPB;
      rank[i] = 0;
      if (t1 + 256 * i < ne) rank[i] = atomicAdd((u32*)(lds + L_BC) + ebk[i], 1u);
    }
    arrive(lb, 0);
    asm volatile("s_waitcnt vmcnt(0)\n\tv_readfirstlane_b32 %0, %8\n\tv_readfirstlane_b32 %1, %9"
                 : "=s"(ep_lo), "=s"(ep_hi), "+v"(es[0]), "+v"(es[1]), "+v"(es[2]), "+v"(ee[0]), "+v"(ee[1]), "+v"(ee[2])
                 : "v"(epoch[0]), "v"(epoch[1]) : "memory");
    ep_lo = RFL(ep_lo); ep_hi = RFL(ep_hi);
    char* const slot = ws + WS_SLOT0 + (size_t)(ep_lo & 3u) * WS_SLOT;
    const u64 tag = ((((u64)ep_hi << 32) | ep_lo) + 1) & 0xfffffffffffull;
    u32 tg[3];
    rec_tags(tag, tg);
    await(lds, 0, 8);
    const u32x4 c4 = *(const u32x4*)(lds + L_BC + lane * 16);
    const u32 cown = *(const u32*)(lds + L_BC + t1 * 4);
    u32x2 pd[3], ps[3];
#pragma unroll
    for (int i = 0; i < 3; i++) {
      const u32* qd = (const u32*)(lds + L_POS + ((ed[i] * 6) & ~3u));
      const u32* qs = (const u32*)(lds + L_POS + ((es[i] * 6) & ~3u));
      pd[i] = u32x2{qd[0], qd[1]}; ps[i] = u32x2{qs[0], qs[1]};
    }
    const u32 pown = *(const u16*)(lds + L_POS + min((nb0 * 3 + t1) * 2, MAXPOS - 2));
    const u32 tot = c4[0] + c4[1] + c4[2] + c4[3];
    const u32 ex = scan64(tot) - tot;
    u32* mine = (u32*)(lds + L_LS + u * 1024);
    *(u32x4*)(mine + lane * 4) = u32x4{ex, ex + c4[0], ex + c4[0] + c4[1], ex + c4[0] + c4[1] + c4[2]};
    __builtin_amdgcn_wave_barrier();
    u32 at[3];
#pragma unroll
    for (int i = 0; i < 3; i++) at[i] = L_RS + (mine[ebk[i]] + rank[i]) * 16;
    const u32 gword = (mine[t1] << 10) | cown;
#pragma unroll
    for (int i = 0; i < 3; i++) {
      const u32 shd = (ed[i] & 1u) * 16, shs = (es[i] & 1u) * 16;
      const u32 dxy = __builtin_amdgcn_alignbit(pd[i][1], pd[i][0], shd), sxy = __builtin_amdgcn_alignbit(ps[i][1], ps[i][0], shs);
      const u32 xy = pk_bf16(bf2f(dxy) - bf2f(sxy), bfhi(dxy) - bfhi(sxy));
      const u32 zz = pk_bf16(bf2f(pd[i][1] >> shd) - bf2f(ps[i][1] >> shs), 0.f);
      u32x4 rc = {es[i] | ((ed[i] - ebk[i] * NPB) << 14) | tg[0], ee[i] | tg[1], xy, zz | tg[2]};
      PIN(rc);
      if (t1 + 256 * i < ne) *(u32x4*)(lds + at[i]) = rc;
    }
    if (t1 < NPB * 3) *(u16*)(lds + L_PD + t1 * 2) = (u16)pown;
    arrive(lb, 2);
    await(lds, 2, 4);
    const __amdgpu_buffer_rsrc_t r_rw = rsrc(slot + WS_REC + (size_t)b * CH * 16, CH * 16);
    u32x4 rv[3];
#pragma unroll
    for (int i = 0; i < 3; i++) rv[i] = *(const u32x4*)(lds + L_RS + (t1 + 256 * i) * 16);
#pragma unroll
    for (int i = 0; i < 3; i++) __builtin_amdgcn_raw_buffer_store_b128(rv[i], r_rw, t1 * 16, i * 4096, SC1);
    arrive(lb, 4);
    __hip_atomic_store((gu64*)(slot + WS_GR) + (size_t)b * MAXG + t1, (tag << 20) | gword, RLX_AGENT);
#pragma unroll
    for (int i = 0; i < 5; i++) { const int o = L_POS + i * 4096 + t1 * 16; if (i < 4 || o < L_SRC) *(u32x4*)(lds + o) = z; }
    *(u32x4*)(lds + L_SRC + t1 * 16) = u32x4{OOB, OOB, OOB, OOB};
    if (t1 < 16) *(u32*)(lds + L_SRC + 4096 + t1 * 4) = OOB;
    await(lds, 1, 4);
#pragma unroll
    for (int s = 0; s < 4; s++)
#pragma unroll
      for (int t = 0; t < 3; t++) hb[t][s] = h_frag(lds, t, s, lane);
  }
  __syncthreads();
  ep_lo = RFL(ep_lo); ep_hi = RFL(ep_hi);
  const u64 ep = ((u64)ep_hi << 32) | ep_lo, tag = (ep + 1) & 0xfffffffffffull;
  char* const slot = ws + WS_SLOT0 + (size_t)(ep_lo & 3u) * WS_SLOT;
  const __amdgpu_buffer_rsrc_t r_bt = rsrc(slot + WS_BT, N * 256), r_rec = rsrc(slot + WS_REC, G * CH * 16);
  u32 tg[3];
  rec_tags(tag, tg);
  f32x4 cp[3];
  u32 pw[4] = {0, 0, 0, 0};
  {
    const int cs = wv * 32 + (lane & 31), csc = cs < G ? cs : G - 1;
    gu64* const gp = (gu64*)(slot + WS_GR) + (size_t)csc * MAXG + bk;
    gu64* const fp = (gu64*)(slot + WS_FL) + csc;
    u64 gv = __hip_atomic_load(gp, RLX_AGENT);
    SB();
    {
      f32x4 acc[3] = {f32x4{0, 0, 0, 0}, f32x4{0, 0, 0, 0}, f32x4{0, 0, 0, 0}};
#pragma unroll
      for (int s = 0; s < 4; s++) {
        const u32x4 fw = tr_frag(lds + L_W1, s, lane, 2 * wv + ((lane & 3) >> 1), lane & 1);
#pragma unroll
        for (int t = 0; t < 3; t++) acc[t] = mfma16(hb[t][s], fw, acc[t]);
      }
      char* kb = lds + L_KB + (16 * wv + c16) * 16;
#pragma unroll
      for (int t = 0; t < 2; t++) {
        const f32x4 a = acc[t] * LOG2E;
        *(u32x2*)(kb + (4 * t + g4) * 2048) = u32x2{pk_bf16(a[0], a[1]), pk_bf16(a[2], a[3])};
      }
      if (g4 < 2) {
#pragma unroll
        for (int i = 0; i < 4; i++) *(u16*)(kb + (4 * g4 + i) * 2048 + 8) = (u16)pk_bf16(acc[2][i] * LOG2E, 0.f);
      }
    }
    SB();
    for (u32 spins = 0; !__all((gv >> 20) == tag) && spins < SPINS; spins++) {
      __builtin_amdgcn_s_sleep(4);
      if ((gv >> 20) != tag) gv = __hip_atomic_load(gp, RLX_AGENT);
    }
    const bool on = lane < 32 && cs < G;
    const u32 cnt = on ? ((u32)gv & 1023u) : 0u, st = ((u32)gv >> 10) & 1023u;
    const u32 inc = scan64(cnt);
    const u32 total = __builtin_amdgcn_readlane(inc, 63);
    u32* list = (u32*)(lds + L_LS + wv * 768);
    const u32 base = (cs * CH + st) * 16, p0 = inc - cnt;
    const u32 lim = min(cnt, p0 < 128 ? 128 - p0 : 0u);
#pragma unroll
    for (u32 k = 0; k < 6; k++) list[k < lim ? p0 + k : 128 + lane] = base + k * 16;
    if (__any(lim > 6)) {
#pragma unroll 1
      for (u32 k = 6; k < lim; k++) list[p0 + k] = base + k * 16;
    }
    __builtin_amdgcn_wave_barrier();
    {
      const u32 a0 = list[lane], a1 = list[64 + lane];
      const bool v0 = lane < total, v1 = 64 + lane < total;
      u32x4 r0 = __builtin_amdgcn_raw_buffer_load_b128(r_rec, v0 ? a0 : OOB, 0, SC1);
      u32x4 r1 = __builtin_amdgcn_raw_buffer_load_b128(r_rec, v1 ? a1 : OOB, 0, SC1);
      u64 fv = __hip_atomic_load(fp, RLX_AGENT);
      if (on) *(u32*)(lds + L_BC + cs * 4) = (u32)gv & 0xfffffu;
      SB();
#pragma unroll
      for (int t = 0; t < 3; t++) cp[t] = f32x4{0, 0, 0, 0};
#pragma unroll
      for (int s = 0; s < 4; s++) {
        const u32x4 f1 = tr_frag(lds + L_W0, s, lane, 2 * wv + ((lane & 3) >> 1), lane & 1);
#pragma unroll
        for (int t = 0; t < 3; t++) cp[t] = mfma16(f1, hb[t][s], cp[t]);
      }
#pragma unroll
      for (int t = 0; t < 3; t++) { cp[t] *= LOG2E; PIN(cp[t]); }
      SB();
      r0 = settle(r0, v0, r_rec, a0, tg);
      r1 = settle(r1, v1, r_rec, a1, tg);
      u32 k0 = 0, k1 = 0;
      if (v0) k0 = take_rank(lds, r0);
      if (v1) k1 = take_rank(lds, r1);
      for (u32 spins = 0; !__all((fv & 0xfffffffffffull) == tag) && spins < SPINS; spins++) {
        __builtin_amdgcn_s_sleep(4);
        fv = __hip_atomic_load(fp, RLX_AGENT);
      }
      { int d = (int)(((u32)tk0 - (u32)(fv >> 44)) << 12) >> 12; atomicMax((int*)(lds + L_SY) + 7, d); }
      __builtin_amdgcn_s_waitcnt(0x0f70);
      pw[0] = __builtin_amdgcn_raw_buffer_load_b32(r_bt, v0 && k0 < PFR ? (r0[0] & 0x3fffu) << 8 : OOB, 0, SC1);
      pw[1] = __builtin_amdgcn_raw_buffer_load_b32(r_bt, v0 && k0 < PFR ? ((r0[0] & 0x3fffu) << 8) + 128 : OOB, 0, SC1);
      pw[2] = __builtin_amdgcn_raw_buffer_load_b32(r_bt, v1 && k1 < PFR ? (r1[0] & 0x3fffu) << 8 : OOB, 0, SC1);
      pw[3] = __builtin_amdgcn_raw_buffer_load_b32(r_bt, v1 && k1 < PFR ? ((r1[0] & 0x3fffu) << 8) + 128 : OOB, 0, SC1);
      SB();
      if (v0) put_rec(lds, r0, k0);
      if (v1) put_rec(lds, r1, k1);
    }
    if (__builtin_expect(total > 128, 0)) {
#pragma unroll 1
      for (u32 done = 128; done < total; done += 64) {
        if (__hip_atomic_load((u32*)(lds + L_CNT + 32), __ATOMIC_RELAXED, __HIP_MEMORY_SCOPE_WORKGROUP)) break;
        __builtin_amdgcn_wave_barrier();
#pragma unroll 1
        for (u32 k = p0 < done ? done - p0 : 0u; k < cnt && p0 + k < done + 64; k++) list[p0 + k - done] = base + k * 16;
        __builtin_amdgcn_wave_barrier();
        const bool v0 = done + lane < total;
        const u32 a0 = list[lane];
        u32x4 r0 = __builtin_amdgcn_raw_buffer_load_b128(r_rec, v0 ? a0 : OOB, 0, SC1);
        r0 = settle(r0, v0, r_rec, a0, tg);
        if (v0) put_rec(lds, r0, take_rank(lds, r0));
      }
      __builtin_amdgcn_s_waitcnt(0x0f70);
    }
  }
  __syncthreads();
  const u32* wcnt = (const u32*)(lds + L_CNT);
  const u32 ci = wcnt[lane >> 3], cj = wcnt[lane & 7];
  const int ovf = RFL(wcnt[8]);
  auto load_wh2 = [&]() {
    const u32x4 s_wh = srd(w_h, 65536);
    dma4(s_wh, wv0 + (wv >> 2) * 16384, wv1 + (wv >> 2) * 16384, 32768, lb + L_W0 + wv * 4096);
  };
  u32x4 v[8], wxf[8];
  int win;
  {
    const u64 gt = __ballot(cj * 8 + (lane & 7) > ci * 8 + (lane >> 3));
    const u32 rk = __builtin_popcount((u32)(gt >> (lane & 56)) & 0xffu);
    const int wl = __builtin_ctzll(__ballot(rk == (u32)(wv < 4 ? wv : 11 - wv)));
    win = wl >> 3;
    const int cntw = ovf ? 1 : __builtin_amdgcn_readlane(ci, wl);
    const int nsteps = (cntw + 15) >> 4;
    const u32 lo = r * 4;
    u32 srca = L_SRC + win * CAPW * 4 + hh * 32;
    u32 GA[8], GB[8], off[8];
    const bool cfon = rp == hh;
    u32 cfa = cfon ? (L_CF + (win * CAPW + 8 * rh + rj) * 16) : L_ZERO;
    const u32 cfs = cfon ? 256u : 0u;
    u32 sma = L_SM + (win * CAPW + 8 * hh + (c16 >> 2)) * 40 + (g4 & 1) * 32 + (lane & 3) * 8;
    u32x4 cf, kbA, kbB, sf = {0, 0, 0, 0}, mB0 = {0, 0, 0, 0}, mB1 = {0, 0, 0, 0}, mA0, mA1;
    f32x16 yA, yB;
    {
      const u32x4 i0 = *(const u32x4*)(lds + srca), i1 = *(const u32x4*)(lds + srca + 16);
      const u32x4 i2 = *(const u32x4*)(lds + srca + 64), i3 = *(const u32x4*)(lds + srca + 80);
      cf = *(const u32x4*)(lds + cfa);
      kbA = *(const u32x4*)(lds + L_KB + win * 2048 + (2 * r + hh) * 16);
      kbB = *(const u32x4*)(lds + L_KB + win * 2048 + (64 + 2 * r + hh) * 16);
      srca += 128;
      SB();
#pragma unroll
      for (int j = 0; j < 4; j++) { off[j] = i0[j] + lo; off[4 + j] = i1[j] + lo; }
#pragma unroll
      for (int j = 0; j < 8; j++) GA[j] = __builtin_amdgcn_raw_buffer_load_b32(r_bt, off[j], 0, SC1);
      SB();
#pragma unroll
      for (int j = 0; j < 8; j++) GB[j] = __builtin_amdgcn_raw_buffer_load_b32(r_bt, off[j] + 128, 0, SC1);
      SB();
      asm volatile("" :: "v"(pw[0]), "v"(pw[1]), "v"(pw[2]), "v"(pw[3]));
      yA = mfma32(cf, kbA, zero);
#pragma unroll
      for (int j = 0; j < 4; j++) { off[j] = i2[j] + lo; off[4 + j] = i3[j] + lo; }
      yA = mfma32(sel0, u32x4{GA[0], GA[1], GA[2], GA[3]}, yA);
      yA = mfma32(sel1, u32x4{GA[4], GA[5], GA[6], GA[7]}, yA);
      PIN(yA);
      SB();
#pragma unroll
      for (int j = 0; j < 8; j++) GA[j] = __builtin_amdgcn_raw_buffer_load_b32(r_bt, off[j], 0, SC1);
      SB();
    }
#define MF(d, a, bb, c) do { SB(); d = mfma32(a, bb, c); PIN(d); SB(); } while (0)
#define SA(y, i, j) do { p_ = f32x2{__builtin_amdgcn_exp2f(-y[i]), __builtin_amdgcn_exp2f(-y[(i) + 1])}; \
                         q_ = f32x2{__builtin_amdgcn_exp2f(-y[j]), __builtin_amdgcn_exp2f(-y[(j) + 1])}; SB(); p_ += 1.0f; q_ += 1.0f; SB(); } while (0)
#define SBB(y, i, j, o0, o1) do { p_ = f32x2{__builtin_amdgcn_rcpf(p_[0]), __builtin_amdgcn_rcpf(p_[1])}; \
                         q_ = f32x2{__builtin_amdgcn_rcpf(q_[0]), __builtin_amdgcn_rcpf(q_[1])}; SB(); \
                         p_ *= f32x2{y[i], y[(i) + 1]}; q_ *= f32x2{y[j], y[(j) + 1]}; SB(); \
                         u32 t0_ = pk_bf16(p_[0], p_[1]), t1_ = pk_bf16(q_[0], q_[1]); \
                         asm volatile("" : "+v"(t0_), "+v"(t1_)); o0 = t0_; o1 = t1_; SB(); } while (0)
#define S4(y, i, j, o0, o1) do { SA(y, i, j); SBB(y, i, j, o0, o1); } while (0)
#define LD2(Gx, j, add) do { Gx[j] = __builtin_amdgcn_raw_buffer_load_b32(r_bt, off[j] + (add), 0, SC1); \
                             Gx[(j) + 1] = __builtin_amdgcn_raw_buffer_load_b32(r_bt, off[(j) + 1] + (add), 0, SC1); SB(); } while (0)
    f32x2 p_, q_;
    for (int s = 1; s < nsteps; s++) {
      MF(yB, cf, kbB, zero);
      S4(yA, 0, 2, mA0[0], mA0[1]);
      MF(yB, sel0, (u32x4{GB[0], GB[1], GB[2], GB[3]}), yB);
      cfa += cfs;
      cf = *(const u32x4*)(lds + cfa);
      SA(yA, 4, 6);
      LD2(GB, 0, 128);
      SBB(yA, 4, 6, mA0[2], mA0[3]);
      LD2(GB, 2, 128);
      MF(yB, sel1, (u32x4{GB[4], GB[5], GB[6], GB[7]}), yB);
      SA(yA, 8, 10);
      LD2(GB, 4, 128);
      MF(aB0, mB0, sf, aB0);
      SBB(yA, 8, 10, mA1[0], mA1[1]);
      LD2(GB, 6, 128);
      const u32x4 i0 = *(const u32x4*)(lds + srca), i1 = *(const u32x4*)(lds + srca + 16);
      srca += 64;
      SA(yA, 12, 14);
      MF(aB1, mB1, sf, aB1);
      sf = s_frag(lds + sma);
      sma += 640;
      SBB(yA, 12, 14, mA1[2], mA1[3]);
      MF(yA, cf, kbA, zero);
      S4(yB, 0, 2, mB0[0], mB0[1]);
      MF(yA, sel0, (u32x4{GA[0], GA[1], GA[2], GA[3]}), yA);
#pragma unroll
      for (int j = 0; j < 4; j++) { off[j] = i0[j] + lo; off[4 + j] = i1[j] + lo; }
      SA(yB, 4, 6);
      LD2(GA, 0, 0);
      SBB(yB, 4, 6, mB0[2], mB0[3]);
      LD2(GA, 2, 0);
      MF(yA, sel1, (u32x4{GA[4], GA[5], GA[6], GA[7]}), yA);
      SA(yB, 8, 10);
      LD2(GA, 4, 0);
      MF(aA0, mA0, sf, aA0);
      SBB(yB, 8, 10, mB1[0], mB1[1]);
      LD2(GA, 6, 0);
      SA(yB, 12, 14);
      MF(aA1, mA1, sf, aA1);
      SBB(yB, 12, 14, mB1[2], mB1[3]);
    }
    {
      const int rem = cntw - 16 * (nsteps - 1);
      load_wh2();
      MF(yB, cf, kbB, zero);
      mA0 = u32x4{0, 0, 0, 0}; mA1 = u32x4{0, 0, 0, 0};
      S4(yA, 0, 8, mA0[0], mA1[0]);
      MF(yB, sel0, (u32x4{GB[0], GB[1], GB[2], GB[3]}), yB);
      if (rem > 4) S4(yA, 2, 10, mA0[1], mA1[1]);
      MF(yB, sel1, (u32x4{GB[4], GB[5], GB[6], GB[7]}), yB);
      if (rem > 8) S4(yA, 4, 12, mA0[2], mA1[2]);
      MF(aB0, mB0, sf, aB0);
      if (rem > 12) S4(yA, 6, 14, mA0[3], mA1[3]);
      MF(aB1, mB1, sf, aB1);
      sf = s_frag(lds + sma);
      mB0 = u32x4{0, 0, 0, 0}; mB1 = u32x4{0, 0, 0, 0};
      S4(yB, 0, 8, mB0[0], mB1[0]);
      MF(aA0, mA0, sf, aA0);
      if (rem > 4) S4(yB, 2, 10, mB0[1], mB1[1]);
      MF(aA1, mA1, sf, aA1);
      if (rem > 8) S4(yB, 4, 12, mB0[2], mB1[2]);
      if (rem > 12) S4(yB, 6, 14, mB0[3], mB1[3]);
      MF(aB0, mB0, sf, aB0);
      MF(aB1, mB1, sf, aB1);
    }
#pragma unroll
    for (int q8 = 0; q8 < 8; q8++) {
      const f32x16& a0 = q8 < 4 ? aA0 : aB0;
      const f32x16& a1 = q8 < 4 ? aA1 : aB1;
#pragma unroll
      for (int q = 0; q < 4; q++) v[q8][q] = pk_bf16(a0[4 * (q8 & 3) + q], a1[4 * (q8 & 3) + q]);
    }
    if (r < 20 && (r & 3) == 0 && !ovf) {
      char* mi = lds + mi_row(r * 2 + win) + 16 * hh;
#pragma unroll
      for (int q8 = 0; q8 < 8; q8++) *(u32x4*)(mi + 32 * q8) = v[q8];
    }
  }
  if (__builtin_expect(ovf, 0)) {
    float* macc = (float*)(lds + L_SM);
    float* pacc = (float*)(lds + L_SM + NPB * D * 4);
    __syncthreads();
    for (int i = tid; i < NPB * D + NPB * 4; i += NT) macc[i] = 0.f;
    __syncthreads();
    const u32 wxp = *(const u32*)(lds + L_WX + lane * 4);
    const float wx0 = bf2f(wxp), wx1 = bfhi(wxp);
    const u32x4 kc0 = *(const u32x4*)(lds + L_KB + (2 * lane) * 16), kc1 = *(const u32x4*)(lds + L_KB + (2 * lane + 1) * 16);
    const float wd0 = bf2f(kc0[3]) + bfhi(kc0[3]), wd1 = bf2f(kc1[3]) + bfhi(kc1[3]);
    const float wa0 = bfhi(kc0[2]), wa1 = bfhi(kc1[2]);
    u32 rot = 0;
    for (int c = 0; c < G; c++) {
      const u32 oo = RFL(*(const u32*)(lds + L_BC + c * 4));
      const u32 s0 = oo >> 10, n = oo & 1023u;
      for (u32 k = (wv - rot) & 7u; k < n; k += 8) {
        u32x4 rr = __builtin_amdgcn_raw_buffer_load_b128(r_rec, (c * CH + s0 + k) * 16, 0, SC1);
        rr = settle(rr, true, r_rec, (c * CH + s0 + k) * 16, tg);
        const u32 nl = (rr[0] >> 14) & 0x3fu, sl = nl >> 3, wn = nl & 7u;
        const u32 bv = __builtin_amdgcn_raw_buffer_load_b32(r_bt, ((rr[0] & 0x3fffu) << 8) + lane * 4, 0, SC1);
        const float d2 = bfhi(dist2(rr[2], rr[3])), eav = bf2f(rr[1]);
        const float a0 = bf2f(*(const u16*)(lds + L_KB + wn * 2048 + (2 * lane) * 16 + sl * 2));
        const float a1 = bf2f(*(const u16*)(lds + L_KB + wn * 2048 + (2 * lane + 1) * 16 + sl * 2));
        const float y0 = a0 + bf2f(bv) + d2 * wd0 + eav * wa0, y1 = a1 + bfhi(bv) + d2 * wd1 + eav * wa1;
        const u32 mp = silu2(y0, y1);
        const float m0 = bf2f(mp), m1 = bfhi(mp);
        atomicAdd(&macc[nl * D + 2 * lane], m0);
        atomicAdd(&macc[nl * D + 2 * lane + 1], m1);
        float csv = m0 * wx0 + m1 * wx1;
#pragma unroll
        for (int o = 32; o > 0; o >>= 1) csv += __shfl_xor(csv, o, 64);
        if (lane < 3) atomicAdd(&pacc[nl * 4 + lane], csv * (lane == 0 ? bf2f(rr[2]) : lane == 1 ? bfhi(rr[2]) : bf2f(rr[3])));
      }
      rot += n;
    }
    __syncthreads();
    for (int i = tid; i < NPB * D / 2; i += NT) {
      const int n = i >> 6, f = (i & 63) * 2;
      *(u32*)(lds + mi_row(n) + f * 2) = pk_bf16(macc[n * D + f], macc[n * D + f + 1]);
    }
    if (tid < NPB * 3) {
      const int n = tid / 3, c = tid - n * 3;
      if (nb0 + n < N) out[(size_t)N * D + (size_t)nb0 * 3 + tid] = (u16)pk_bf16(bf2f(*(const u16*)(lds + L_PD + tid * 2)) + LN2 * pacc[n * 4 + c], 0.f);
    }
  }
  u32x2 hr[3];
  u32x4 f2[4];
#pragma unroll
  for (int t = 0; t < 3; t++) {
    const int n = tile_node(t, c16);
    hr[t] = *(const u32x2*)(lds + L_HS + n * 256 + 16 * ((2 * wv + (g4 >> 1)) ^ hswz(n)) + 8 * (g4 & 1));
  }
#pragma unroll
  for (int q8 = 0; q8 < 8; q8++) wxf[q8] = *(const u32x4*)(lds + L_WX + (16 * q8 + 8 * hh) * 2);
  const int kind = lane & 3, pnl = (lane >> 2) * 8 + win;
  const bool pon = kind != 0 && lane < 20 && !ovf;
  const u32 p0 = *(const u16*)(lds + L_PD + (pon ? pnl * 6 + kind * 2 - 2 : 0));
  const int mia = mi_row(tile_node(0, c16)) + g4 * 16, mib = mi_row(tile_node(2, c16)) + g4 * 16;
  asm volatile("s_waitcnt vmcnt(0)" ::: "memory");
  __syncthreads();
  {
    u32x4 mb[3][4];
#pragma unroll
    for (int s = 0; s < 4; s++) {
      f2[s] = tr_frag(lds + L_W0, s, lane, 2 * wv + ((lane & 3) >> 1), lane & 1);
      mb[0][s] = *(const u32x4*)(lds + mia + s * 64);
      mb[1][s] = *(const u32x4*)(lds + mia + 8192 + s * 64);
      mb[2][s] = *(const u32x4*)(lds + mib + s * 64);
    }
    SB();
#pragma unroll
    for (int s = 0; s < 4; s++)
#pragma unroll
      for (int t = 0; t < 3; t++) cp[t] = mfma16(f2[s], mb[t][s], cp[t]);
    f32x16 z = zero, z1 = zero;
#pragma unroll
    for (int t = 0; t < 3; t++) {
      const f32x4 y = cp[t];
      f32x4 e;
#pragma unroll
      for (int i = 0; i < 4; i++) e[i] = __builtin_amdgcn_exp2f(-y[i]);
      SB();
      if (t & 1) { z1 = mfma32(wxf[3 * t], v[3 * t], z1); PIN(z1); } else { z = mfma32(wxf[3 * t], v[3 * t], z); PIN(z); }
      SB();
      e += 1.0f;
#pragma unroll
      for (int i = 0; i < 4; i++) e[i] = __builtin_amdgcn_rcpf(e[i]);
      SB();
      if (t & 1) { z = mfma32(wxf[3 * t + 1], v[3 * t + 1], z); PIN(z); } else { z1 = mfma32(wxf[3 * t + 1], v[3 * t + 1], z1); PIN(z1); }
      SB();
      const f32x4 o = f32x4{bf2f(hr[t][0]), bfhi(hr[t][0]), bf2f(hr[t][1]), bfhi(hr[t][1])} + (y * LN2) * e;
      *(u32x2*)(lds + L_SM + tile_node(t, c16) * 272 + wv * 32 + g4 * 8) = u32x2{pk_bf16(o[0], o[1]), pk_bf16(o[2], o[3])};
      SB();
      if (t == 0) { z = mfma32(wxf[2], v[2], z); PIN(z); }
      if (t == 1) { z1 = mfma32(wxf[5], v[5], z1); PIN(z1); }
      SB();
    }
    __syncthreads();
    const __amdgpu_buffer_rsrc_t r_out = rsrc(out, N * 262);
    const u32x4 o0 = *(const u32x4*)(lds + L_SM + (tid >> 4) * 272 + (tid & 15) * 16);
    const u32x4 o1 = *(const u32x4*)(lds + L_SM + (32 + ((tid >> 4) & 7)) * 272 + (tid & 15) * 16);
    const u32 oo = nb0 * 256 + tid * 16;
    __builtin_amdgcn_raw_buffer_store_b128(o0, r_out, oo < N * 256 ? oo : OOB, 0, 0);
    __builtin_amdgcn_raw_buffer_store_b128(o1, r_out, tid < 128 && oo + 8192 < N * 256 ? oo + 8192 : OOB, 0, 0);
    __builtin_amdgcn_raw_buffer_store_b16((u16)pk_bf16(bf2f(p0) + LN2 * (z[0] + z1[0]), 0.f), r_out,
                                          pon ? N * 256 + ((nb0 + pnl) * 3 + kind - 1) * 2 : OOB, 0, 0);
  }
  { const int dm = *(const int*)(lds + L_SY + 28); while (__builtin_amdgcn_s_memrealtime() - tk0 + dm < 1800) __builtin_amdgcn_s_sleep(1); }
  if (b == 0 && tid == 0) __hip_atomic_store((gu64*)(ws + WS_EP), ep + 1, RLX_AGENT);
}

extern "C" void kernel_launch(void* const* d_in, const int* in_sizes, int n_in, void* d_out, int out_size, void* d_ws,
                              size_t ws_size, hipStream_t stream) {
  const int N = in_sizes[0] / D, E = in_sizes[3];
  const int NB = (N + NPB - 1) / NPB, C = (E + CH - 1) / CH;
  const int G = NB > C ? NB : C;
  k_egnn<<<G, NT, 0, stream>>>((const u16*)d_in[0], (const u16*)d_in[1], (const u16*)d_in[2], (const int*)d_in[3],
                               (const int*)d_in[4], (const u16*)d_in[5], (const u16*)d_in[6], (const u16*)d_in[7],
                               (char*)d_ws, (u16*)d_out, N, E, G);
}
```

```cpp
#include <hip/hip_runtime.h>
#include <hip/hip_bf16.h>

typedef unsigned int u32;
typedef unsigned short u16;
typedef unsigned long long u64;
typedef u32 u32x2 __attribute__((ext_vector_type(2)));
typedef u32 u32x4 __attribute__((ext_vector_type(4)));
typedef short s16x4 __attribute__((ext_vector_type(4)));
typedef short bf16x8 __attribute__((ext_vector_type(8)));
typedef float f32x2 __attribute__((ext_vector_type(2)));
typedef float f32x4 __attribute__((ext_vector_type(4)));
typedef float f32x16 __attribute__((ext_vector_type(16)));
typedef __bf16 bf16x2_t __attribute__((ext_vector_type(2)));
typedef __attribute__((address_space(1))) u64 gu64;
typedef __attribute__((address_space(3))) s16x4 lds_s16x4;
typedef __attribute__((address_space(3))) char lds_char;
#define DI __device__ __forceinline__
#define RLX_AGENT __ATOMIC_RELAXED, __HIP_MEMORY_SCOPE_AGENT
#define SC1 16
#define SB() __builtin_amdgcn_sched_barrier(0)
#define PIN(x) asm volatile("" : "+v"(x))
#define RFL(x) __builtin_amdgcn_readfirstlane(x)

constexpr int D = 128;
constexpr int NT = 512;
constexpr int NPB = 40;
constexpr int CH = 640;
constexpr int MAXG = 256;
constexpr int CAPW = 128;
constexpr int MAXPOS = 61440;
constexpr u32 PFR = 16;
constexpr u32 SPINS = 1u << 16;
constexpr float LOG2E = 1.4426950408889634f;
constexpr float LN2 = 0.6931471805599453f;
constexpr u32 OOB = 0xfffff000u;

constexpr size_t WS_EP = 0;
constexpr size_t WS_SLOT0 = (size_t)2 << 20, WS_SLOT = (size_t)6 << 20;
constexpr size_t WS_GR = 0;
constexpr size_t WS_BT = WS_GR + (size_t)MAXG * MAXG * 8;
constexpr size_t WS_REC = WS_BT + (size_t)MAXG * NPB * 256;
constexpr size_t WS_FL = WS_REC + (size_t)MAXG * CH * 16;
static_assert(WS_FL + MAXG * 8 <= WS_SLOT, "slot");

DI float bf2f(u32 b) { return __builtin_bit_cast(float, b << 16); }
DI float bfhi(u32 b) { return __builtin_bit_cast(float, b & 0xffff0000u); }
DI u32 pk_bf16(float lo, float hi) {
  f32x2 v = {lo, hi};
  bf16x2_t r = __builtin_convertvector(v, bf16x2_t);
  return __builtin_bit_cast(u32, r);
}
DI f32x4 mfma16(u32x4 a, u32x4 b, f32x4 c) {
  return __builtin_amdgcn_mfma_f32_16x16x32_bf16(__builtin_bit_cast(bf16x8, a), __builtin_bit_cast(bf16x8, b), c, 0, 0, 0);
}
DI f32x16 mfma32(u32x4 a, u32x4 b, f32x16 c) {
  return __builtin_amdgcn_mfma_f32_32x32x16_bf16(__builtin_bit_cast(bf16x8, a), __builtin_bit_cast(bf16x8, b), c, 0, 0, 0);
}
DI __amdgpu_buffer_rsrc_t rsrc(const void* p, u32 bytes) { return __builtin_amdgcn_make_buffer_rsrc((void*)p, 0, bytes, 0x00020000); }
DI u32x4 srd(const void* p, u32 bytes) { const u64 a = (u64)p; return u32x4{(u32)a, (u32)(a >> 32) & 0xffffu, bytes, 0x00020000u}; }
DI void dma4(u32x4 sd, u32 v0, u32 v1, u32 soff, u32 ldsa) {
  u32 keep;
  asm volatile("s_mov_b32 %0, m0\n\ts_mov_b32 m0, %1\n\ts_nop 0\n\t"
               "buffer_load_dwordx4 %2, %4, %5 offen lds\n\tbuffer_load_dwordx4 %2, %4, %5 offen offset:1024 lds\n\t"
               "buffer_load_dwordx4 %3, %4, %5 offen offset:2048 lds\n\tbuffer_load_dwordx4 %3, %4, %5 offen offset:3072 lds\n\t"
               "s_mov_b32 m0, %0"
               : "=&s"(keep) : "s"(RFL(ldsa)), "v"(v0), "v"(v1), "s"(sd), "s"(soff) : "memory");
}
DI void dma2(u32x4 sd, u32 v0, u32 soff, u32 ldsa) {
  u32 keep;
  asm volatile("s_mov_b32 %0, m0\n\ts_mov_b32 m0, %1\n\ts_nop 0\n\t"
               "buffer_load_dwordx4 %2, %3, %4 offen lds\n\tbuffer_load_dwordx4 %2, %3, %4 offen offset:1024 lds\n\t"
               "s_mov_b32 m0, %0"
               : "=&s"(keep) : "s"(RFL(ldsa)), "v"(v0), "s"(sd), "s"(soff) : "memory");
}
DI u32 scan64(u32 x) {
  x += __builtin_amdgcn_update_dpp(0u, x, 0x111, 0xf, 0xf, false);
  x += __builtin_amdgcn_update_dpp(0u, x, 0x112, 0xf, 0xf, false);
  x += __builtin_amdgcn_update_dpp(0u, x, 0x114, 0xf, 0xf, false);
  x += __builtin_amdgcn_update_dpp(0u, x, 0x118, 0xf, 0xf, false);
  x += __builtin_amdgcn_update_dpp(0u, x, 0x142, 0xa, 0xf, false);
  x += __builtin_amdgcn_update_dpp(0u, x, 0x143, 0xc, 0xf, false);
  return x;
}

constexpr int L_ZERO = 0;
constexpr int L_CNT = L_ZERO + 16;
constexpr int L_BC = L_CNT + 48;
constexpr int L_SY = L_BC + 1024;
constexpr int L_SM = L_SY + 32;
constexpr int L_CF = L_SM + 8 * 8 * 640;
constexpr int L_SRC = L_CF + 8 * CAPW * 16;
constexpr int L_KB = L_SRC + 8 * CAPW * 4 + 64;
constexpr int L_W1 = L_KB + 8 * D * 16;
constexpr int L_W0 = L_W1 + 32768;
constexpr int L_HS = L_W0 + 32768;
constexpr int L_WX = L_HS + NPB * 256;
constexpr int L_PD = L_WX + 256;
constexpr int L_LS = L_PD + 256;
constexpr int L_END = L_LS + 8 * 768;
constexpr int L_RS = L_W0 - CH * 16;
constexpr int L_POS = L_RS - MAXPOS;
static_assert(L_END <= 160 * 1024, "lds");
static_assert(L_POS >= L_SM + 36864 && L_POS <= L_CF && (L_POS % 16) == 0, "pos table");
static_assert((L_SM % 16) == 0 && (L_BC % 16) == 0 && (L_SRC % 16) == 0 && (L_KB % 16) == 0 && (L_W0 % 16) == 0 && (L_HS % 16) == 0 && (L_WX % 16) == 0 && (L_LS % 16) == 0, "align");

DI int tile_node(int t, int c) { return t < 2 ? ((c & 3) * 8 + 4 * t + (c >> 2)) : (32 + (c & 7)); }
DI int mi_row(int nl) { return L_KB + (nl & 7) * 2048 + (nl & 3) * 64 + (nl >> 3) * 272; }
DI int wswz(int k) { return ((k & 3) << 2) | ((k >> 2) & 2); }
DI int hswz(int n) { return ((n & 3) << 2) | ((n >> 3) & 3); }
DI u32x4 tr_frag(const char* img, int s, int lane, int ch, int hf) {
  const int k0 = 8 * (lane >> 4) + ((lane & 15) >> 2);
  const char* p = img + (32 * s + k0) * 256 + 16 * (ch ^ wswz(k0)) + 8 * hf;
  const s16x4 a = __builtin_amdgcn_ds_read_tr16_b64_v4i16((lds_s16x4*)p);
  const s16x4 b = __builtin_amdgcn_ds_read_tr16_b64_v4i16((lds_s16x4*)(p + 1024));
  const u32x2 x = __builtin_bit_cast(u32x2, a), y = __builtin_bit_cast(u32x2, b);
  return u32x4{x[0], x[1], y[0], y[1]};
}
DI u32x4 s_frag(const char* p) {
  const u32x2 x = __builtin_bit_cast(u32x2, __builtin_amdgcn_ds_read_tr16_b64_v4i16((lds_s16x4*)p));
  const u32x2 y = __builtin_bit_cast(u32x2, __builtin_amdgcn_ds_read_tr16_b64_v4i16((lds_s16x4*)(p + 160)));
  return u32x4{x[0], x[1], y[0], y[1]};
}
DI u32x4 h_frag(const char* lds, int t, int s, int lane) {
  const int n = tile_node(t, lane & 15);
  return *(const u32x4*)(lds + L_HS + n * 256 + 16 * ((4 * s + (lane >> 4)) ^ hswz(n)));
}
DI u32 dist2(u32 w2, u32 w3) {
  const float dx = bf2f(w2), dy = bfhi(w2), dz = bf2f(w3);
  const u32 q0 = pk_bf16(dx * dx, dy * dy), q1 = pk_bf16(dz * dz, 0.f);
  const float s2 = bf2f(q0) + bfhi(q0) + bf2f(q1);
  return pk_bf16(s2, s2);
}
DI u32 take_rank(char* lds, u32x4 rc) { return atomicAdd((u32*)(lds + L_CNT) + ((rc[0] >> 14) & 7u), 1u); }
DI void put_rec(char* lds, u32x4 rc, u32 rank) {
  const u32 win = (rc[0] >> 14) & 7u, slot = (rc[0] >> 17) & 7u;
  const u64 oh = 0x3f80ull << (slot * 16);
  const u32x4 cf = {slot < 4 ? (u32)oh : 0u, slot < 4 ? (u32)(oh >> 32) : 0u, (slot == 4 ? 0x3f80u : 0u) | (rc[1] << 16), dist2(rc[2], rc[3])};
  if (rank < CAPW) {
    const u32 k = ((rank & 1u) << 3) + ((rank & 15u) >> 1), sp = win * CAPW + (rank & ~15u) + k;
    *(u32*)(lds + L_SRC + sp * 4) = (rc[0] & 0x3fffu) << 8;
    *(u32x4*)(lds + L_CF + sp * 16) = cf;
    *(u32x2*)(lds + L_SM + sp * 40 + slot * 8) = u32x2{0x3f80u | (rc[2] << 16), __builtin_amdgcn_alignbit(rc[3], rc[2], 16)};
  } else *(u32*)(lds + L_CNT + 32) = 1;
}
DI void rec_tags(u64 tag, u32 (&t)[3]) {
  t[0] = (u32)tag << 20; t[1] = ((u32)tag >> 12) << 16; t[2] = ((u32)(tag >> 28) ^ (u32)tag) << 16;
}
DI bool rec_stale(u32x4 rc, const u32 (&t)[3]) { return (((rc[0] ^ t[0]) >> 20) | (((rc[1] ^ t[1]) | (rc[3] ^ t[2])) >> 16)) != 0; }
DI u32x4 settle(u32x4 rc, bool on, __amdgpu_buffer_rsrc_t r, u32 off, const u32 (&t)[3]) {
  for (u32 spins = 0; __any(on && rec_stale(rc, t)) && spins < SPINS; spins++) {
    __builtin_amdgcn_s_sleep(4);
    if (on && rec_stale(rc, t)) rc = __builtin_amdgcn_raw_buffer_load_b128(r, off, 0, SC1);
  }
  return rc;
}
DI void arrive(u32 lb, int i) {
  asm volatile("s_mov_b64 exec, 1\n\tds_add_u32 %0, %1 offset:%2\n\ts_mov_b64 exec, -1" :: "v"(lb), "v"(1u), "i"(L_SY + 4 * i) : "memory");
}
DI void await(char* lds, int i, u32 n) {
  asm volatile("" ::: "memory");
  while ((u32)RFL(__hip_atomic_load((u32*)(lds + L_SY) + i, __ATOMIC_RELAXED, __HIP_MEMORY_SCOPE_WORKGROUP)) < n) { }
  asm volatile("" ::: "memory");
}
DI u32 silu2(float a, float b) {
  f32x2 yy = {a, b};
  f32x2 e = {__builtin_amdgcn_exp2f(-a), __builtin_amdgcn_exp2f(-b)};
  e += 1.0f;
  f32x2 rc = {__builtin_amdgcn_rcpf(e[0]), __builtin_amdgcn_rcpf(e[1])};
  yy *= rc;
  return pk_bf16(yy[0], yy[1]);
}

__global__ __launch_bounds__(NT, 2) void k_egnn(const u16* __restrict__ h, const u16* __restrict__ pos,
                                                const u16* __restrict__ ea, const int* __restrict__ src,
                                                const int* __restrict__ dst, const u16* __restrict__ w_e,
                                                const u16* __restrict__ w_x, const u16* __restrict__ w_h,
                                                char* __restrict__ ws, u16* __restrict__ out, int N, int E, int G) {
  __shared__ __attribute__((aligned(16))) char lds[L_END];
  const int tid = threadIdx.x, lane = tid & 63, b = blockIdx.x;
  const int wv = RFL(tid >> 6);
  const int u = wv & 3, c16 = lane & 15, g4 = lane >> 4;
  const bool role1 = wv >= 4;
  const u32 lb = (u32)(size_t)(lds_char*)lds;
  if (tid < L_SM / 4) *(u32*)(lds + tid * 4) = 0;
  __syncthreads();
  const int ne = RFL(max(0, min(CH, E - b * CH)));
  u32 ed[3], es[3], ee[3];
  u32x2 epoch;
  {
    const u32x4 s_d = srd(dst + b * CH, ne * 4), s_s = srd(src + b * CH, ne * 4), s_e = srd(ea + b * CH, ne * 2);
    asm volatile("s_cmp_lt_u32 %15, 4\n\ts_cbranch_scc1 1f\n\t"
                 "buffer_load_dword %0, %10, %12, 0 offen\n\tbuffer_load_dword %1, %10, %12, 0 offen offset:1024\n\t"
                 "buffer_load_dword %2, %10, %12, 0 offen offset:2048\n\tbuffer_load_dword %3, %10, %13, 0 offen\n\t"
                 "buffer_load_dword %4, %10, %13, 0 offen offset:1024\n\tbuffer_load_dword %5, %10, %13, 0 offen offset:2048\n\t"
                 "buffer_load_ushort %6, %11, %14, 0 offen\n\tbuffer_load_ushort %7, %11, %14, 0 offen offset:512\n\t"
                 "buffer_load_ushort %8, %11, %14, 0 offen offset:1024\n"
                 "1:\n\tglobal_load_dwordx2 %9, %16, %17 sc1"
                 : "=&v"(ed[0]), "=&v"(ed[1]), "=&v"(ed[2]), "=&v"(es[0]), "=&v"(es[1]), "=&v"(es[2]), "=&v"(ee[0]), "=&v"(ee[1]), "=&v"(ee[2]), "=&v"(epoch)
                 : "v"((tid & 255) * 4), "v"((tid & 255) * 2), "s"(s_d), "s"(s_s), "s"(s_e), "s"(wv), "v"(0u), "s"(ws + WS_EP) : "memory", "scc");
  }
  int bk;
  { const int q = G >> 3, r = G & 7, x = b & 7, i = b >> 3;
    bk = (x < r ? x * (q + 1) : r * (q + 1) + (x - r) * q) + i; }
  const int nb0 = bk * NPB;
  const int r = lane & 31, hh = lane >> 5;
  const int rh = (r >> 2) & 1, rp = r >> 4, rj = (r & 3) | (((r >> 3) & 1) << 2);
  f32x16 aA0, aA1, aB0, aB1, zero;
  u32x4 sel0, sel1;
  auto setup = [&]() {
#pragma unroll
    for (int i = 0; i < 16; i++) zero[i] = 0.f;
    u32x4 z4 = {0, 0, 0, 0};
    PIN(z4);
    aA0 = mfma32(z4, z4, zero); aA1 = mfma32(z4, z4, zero); aB0 = mfma32(z4, z4, zero); aB1 = mfma32(z4, z4, zero);
    const u32 one = rh == hh ? 0x3f80u << (rp * 16) : 0u;
#pragma unroll
    for (int q = 0; q < 4; q++) { sel0[q] = (rj == q) ? one : 0u; sel1[q] = (rj == 4 + q) ? one : 0u; }
    PIN(aA0); PIN(aA1); PIN(aB0); PIN(aB1); PIN(sel0); PIN(sel1);
  };
  u32 ep_lo, ep_hi;
  u32x4 hb[3][4];
  const u32 wv0 = g4 * 256 + 16 * (c16 ^ (g4 << 2)) + u * 4096, wv1 = wv0 ^ 32;
  if (!role1) {
    const u32x4 s_pos = srd(pos, N * 6), s_h = srd(h, N * 256), s_we = srd(w_e, 258 * 256), s_wh = srd(w_h, 65536);
#pragma unroll
    for (int i = 0; i < 4; i++)
      if (i < 3 || u < 3) dma4(s_pos, lane * 16 + u * 4096, lane * 16 + u * 4096, i * 16384, lb + L_POS + i * 16384 + u * 4096);
    dma2(s_h, g4 * 256 + 16 * (c16 ^ ((g4 << 2) | u)) + u * 2048, nb0 * 256, lb + L_HS + u * 2048);
    if (u == 0) dma2(s_h, g4 * 256 + 16 * (c16 ^ (g4 << 2)) + 8192, nb0 * 256, lb + L_HS + 8192);
    dma4(s_we, wv0, wv1, 32768, lb + L_W0 + u * 4096);
    dma4(s_we, wv0, wv1, 32768 + 16384, lb + L_W0 + 16384 + u * 4096);
    u32 wdv, wav, wxv, pf1, pf2;
    {
      const u32x4 s_wx = srd(w_x, 256);
      const u32 xi = (b >> 3) & 31;
      asm volatile("buffer_load_ushort %0, %5, %6, %7 offen\n\tbuffer_load_ushort %1, %5, %6, %7 offen offset:256\n\t"
                   "buffer_load_ushort %2, %5, %8, 0 offen\n\tbuffer_load_dword %3, %9, %6, 0 offen\n\t"
                   "buffer_load_dword %4, %10, %11, 0 offen"
                   : "=&v"(wdv), "=&v"(wav), "=&v"(wxv), "=&v"(pf1), "=&v"(pf2)
                   : "v"((tid & 127) * 2), "s"(s_we), "s"(65536u), "s"(s_wx), "v"(tid < 8 ? (xi * 8 + tid) * 128 : OOB),
                     "v"(tid - 64 < 16u ? (xi * 16 + tid - 64) * 128 : OOB), "s"(s_wh) : "memory");
    }
    asm volatile("s_waitcnt vmcnt(15)\n\tv_readfirstlane_b32 %0, %2\n\tv_readfirstlane_b32 %1, %3\n\ts_nop 1"
                 : "=s"(ep_lo), "=s"(ep_hi) : "v"(epoch[0]), "v"(epoch[1]) : "memory");
    arrive(lb, 0);
    setup();
    asm volatile("s_waitcnt vmcnt(5)" ::: "memory");
    arrive(lb, 1);
    char* const slot = ws + WS_SLOT0 + (size_t)(RFL(ep_lo) & 3u) * WS_SLOT;
    await(lds, 1, 4);
#pragma unroll
    for (int s = 0; s < 4; s++)
#pragma unroll
      for (int t = 0; t < 3; t++) hb[t][s] = h_frag(lds, t, s, lane);
    f32x4 acc[3][2];
#pragma unroll
    for (int t = 0; t < 3; t++) { acc[t][0] = f32x4{0, 0, 0, 0}; acc[t][1] = f32x4{0, 0, 0, 0}; }
#pragma unroll
    for (int s = 0; s < 4; s++) {
      const u32x4 fa = tr_frag(lds + L_W0, s, lane, 4 * u + (lane & 3), 0), fb = tr_frag(lds + L_W0, s, lane, 4 * u + (lane & 3), 1);
#pragma unroll
      for (int t = 0; t < 3; t++) { acc[t][0] = mfma16(fa, hb[t][s], acc[t][0]); acc[t][1] = mfma16(fb, hb[t][s], acc[t][1]); }
    }
    asm volatile("s_waitcnt vmcnt(0)" : "+v"(wdv), "+v"(wav), "+v"(wxv), "+v"(pf1), "+v"(pf2), "+v"(acc[2][1]) :: "memory");
    arrive(lb, 3);
    const __amdgpu_buffer_rsrc_t r_bw = rsrc(slot + WS_BT, N * 256);
#pragma unroll
    for (int t = 0; t < 3; t++) {
      const f32x4 x = acc[t][0] * LOG2E, y = acc[t][1] * LOG2E;
      const u32x4 o = {pk_bf16(x[0], x[1]), pk_bf16(x[2], x[3]), pk_bf16(y[0], y[1]), pk_bf16(y[2], y[3])};
      __builtin_amdgcn_raw_buffer_store_b128(o, r_bw, (t < 2 || c16 < 8) ? (nb0 + tile_node(t, c16)) * 256 + u * 64 + g4 * 16 : OOB, 0, SC1);
    }
    await(lds, 3, 4);
    dma4(s_wh, wv0, wv1, 0, lb + L_W0 + u * 4096);
    dma4(s_wh, wv0, wv1, 16384, lb + L_W0 + 16384 + u * 4096);
    await(lds, 4, 4);
    dma4(s_we, wv0, wv1, 0, lb + L_W1 + u * 4096);
    dma4(s_we, wv0, wv1, 16384, lb + L_W1 + 16384 + u * 4096);
    {
      const float wd = bf2f(wdv) * LOG2E;
      const u32 wdh = pk_bf16(wd, 0.f);
      const u32 wdp = wdh | (pk_bf16(wd - bf2f(wdh), 0.f) << 16), wap = pk_bf16(bf2f(wav) * LOG2E, 0.f);
#pragma unroll
      for (int i = 0; i < 4; i++) {
        char* kb = lds + L_KB + (i * 256 + tid) * 16;
        *(u32*)(kb + 12) = wdp;
        *(u16*)(kb + 10) = (u16)wap;
      }
      if (tid < D) *(u16*)(lds + L_WX + tid * 2) = (u16)wxv;
    }
    asm volatile("s_waitcnt vmcnt(0)" ::: "memory");
    if (lane == 0 && __hip_atomic_fetch_add((u32*)(lds + L_SY) + 5, 1u, __ATOMIC_RELAXED, __HIP_MEMORY_SCOPE_WORKGROUP) == 3)
      __hip_atomic_store((gu64*)(slot + WS_FL) + b, ((((u64)ep_hi << 32) | ep_lo) + 1) & 0xfffffffffffull, RLX_AGENT);
  } else {
    const int t1 = tid - 256;
    __builtin_amdgcn_s_setprio(2);
    const u32x4 z = {0, 0, 0, 0};
    setup();
    asm volatile("s_waitcnt vmcnt(7)" : "+v"(ed[0]), "+v"(ed[1]), "+v"(ed[2]) :: "memory");
    u32 rank[3], ebk[3];
#pragma unroll
    for (int i = 0; i < 3; i++) {
      ebk[i] = ed[i] / NPB;
      rank[i] = 0;
      if (t1 + 256 * i < ne) rank[i] = atomicAdd((u32*)(lds + L_BC) + ebk[i], 1u);
    }
    arrive(lb, 0);
    asm volatile("s_waitcnt vmcnt(0)\n\tv_readfirstlane_b32 %0, %8\n\tv_readfirstlane_b32 %1, %9\n\ts_nop 1"
                 : "=s"(ep_lo), "=s"(ep_hi), "+v"(es[0]), "+v"(es[1]), "+v"(es[2]), "+v"(ee[0]), "+v"(ee[1]), "+v"(ee[2])
                 : "v"(epoch[0]), "v"(epoch[1]) : "memory");
    ep_lo = RFL(ep_lo); ep_hi = RFL(ep_hi);
    char* const slot = ws + WS_SLOT0 + (size_t)(ep_lo & 3u) * WS_SLOT;
    const u64 tag = ((((u64)ep_hi << 32) | ep_lo) + 1) & 0xfffffffffffull;
    u32 tg[3];
    rec_tags(tag, tg);
    await(lds, 0, 8);
    const u32x4 c4 = *(const u32x4*)(lds + L_BC + lane * 16);
    const u32 cown = *(const u32*)(lds + L_BC + t1 * 4);
    u32x2 pd[3], ps[3];
#pragma unroll
    for (int i = 0; i < 3; i++) {
      const u32* qd = (const u32*)(lds + L_POS + ((ed[i] * 6) & ~3u));
      const u32* qs = (const u32*)(lds + L_POS + ((es[i] * 6) & ~3u));
      pd[i] = u32x2{qd[0], qd[1]}; ps[i] = u32x2{qs[0], qs[1]};
    }
    const u32 pown = *(const u16*)(lds + L_POS + min((nb0 * 3 + t1) * 2, MAXPOS - 2));
    const u32 tot = c4[0] + c4[1] + c4[2] + c4[3];
    const u32 ex = scan64(tot) - tot;
    u32* mine = (u32*)(lds + L_LS + u * 1024);
    *(u32x4*)(mine + lane * 4) = u32x4{ex, ex + c4[0], ex + c4[0] + c4[1], ex + c4[0] + c4[1] + c4[2]};
    __builtin_amdgcn_wave_barrier();
    u32 at[3];
#pragma unroll
    for (int i = 0; i < 3; i++) at[i] = L_RS + (mine[ebk[i]] + rank[i]) * 16;
    const u32 gword = (mine[t1] << 10) | cown;
#pragma unroll
    for (int i = 0; i < 3; i++) {
      const u32 shd = (ed[i] & 1u) * 16, shs = (es[i] & 1u) * 16;
      const u32 dxy = __builtin_amdgcn_alignbit(pd[i][1], pd[i][0], shd), sxy = __builtin_amdgcn_alignbit(ps[i][1], ps[i][0], shs);
      const u32 xy = pk_bf16(bf2f(dxy) - bf2f(sxy), bfhi(dxy) - bfhi(sxy));
      const u32 zz = pk_bf16(bf2f(pd[i][1] >> shd) - bf2f(ps[i][1] >> shs), 0.f);
      u32x4 rc = {es[i] | ((ed[i] - ebk[i] * NPB) << 14) | tg[0], ee[i] | tg[1], xy, zz | tg[2]};
      PIN(rc);
      if (t1 + 256 * i < ne) *(u32x4*)(lds + at[i]) = rc;
    }
    if (t1 < NPB * 3) *(u16*)(lds + L_PD + t1 * 2) = (u16)pown;
    arrive(lb, 2);
    await(lds, 2, 4);
    const __amdgpu_buffer_rsrc_t r_rw = rsrc(slot + WS_REC + (size_t)b * CH * 16, CH * 16);
    u32x4 rv[3];
#pragma unroll
    for (int i = 0; i < 3; i++) rv[i] = *(const u32x4*)(lds + L_RS + (t1 + 256 * i) * 16);
#pragma unroll
    for (int i = 0; i < 3; i++) __builtin_amdgcn_raw_buffer_store_b128(rv[i], r_rw, t1 * 16, i * 4096, SC1);
    arrive(lb, 4);
    __hip_atomic_store((gu64*)(slot + WS_GR) + (size_t)b * MAXG + t1, (tag << 20) | gword, RLX_AGENT);
    __builtin_amdgcn_s_setprio(0);
#pragma unroll
    for (int i = 0; i < 10; i++) if (i < 9 || L_SM + i * 4096 + t1 * 16 < L_POS) *(u32x4*)(lds + L_SM + i * 4096 + t1 * 16) = z;
#pragma unroll
    for (int i = 0; i < 5; i++) { const int o = L_POS + i * 4096 + t1 * 16; if (i < 4 || o < L_SRC) *(u32x4*)(lds + o) = z; }
    *(u32x4*)(lds + L_SRC + t1 * 16) = u32x4{OOB, OOB, OOB, OOB};
    if (t1 < 16) *(u32*)(lds + L_SRC + 4096 + t1 * 4) = OOB;
    await(lds, 1, 4);
#pragma unroll
    for (int s = 0; s < 4; s++)
#pragma unroll
      for (int t = 0; t < 3; t++) hb[t][s] = h_frag(lds, t, s, lane);
  }
  __syncthreads();
  ep_lo = RFL(ep_lo); ep_hi = RFL(ep_hi);
  const u64 ep = ((u64)ep_hi << 32) | ep_lo, tag = (ep + 1) & 0xfffffffffffull;
  char* const slot = ws + WS_SLOT0 + (size_t)(ep_lo & 3u) * WS_SLOT;
  const __amdgpu_buffer_rsrc_t r_bt = rsrc(slot + WS_BT, N * 256), r_rec = rsrc(slot + WS_REC, G * CH * 16);
  u32 tg[3];
  rec_tags(tag, tg);
  f32x4 cp[3];
  u32 pw[4] = {0, 0, 0, 0};
  {
    const int cs = wv * 32 + (lane & 31), csc = cs < G ? cs : G - 1;
    gu64* const gp = (gu64*)(slot + WS_GR) + (size_t)csc * MAXG + bk;
    gu64* const fp = (gu64*)(slot + WS_FL) + csc;
    u64 gv = __hip_atomic_load(gp, RLX_AGENT);
    SB();
    {
      f32x4 acc[3] = {f32x4{0, 0, 0, 0}, f32x4{0, 0, 0, 0}, f32x4{0, 0, 0, 0}};
#pragma unroll
      for (int s = 0; s < 4; s++) {
        const u32x4 fw = tr_frag(lds + L_W1, s, lane, 2 * wv + ((lane & 3) >> 1), lane & 1);
#pragma unroll
        for (int t = 0; t < 3; t++) acc[t] = mfma16(hb[t][s], fw, acc[t]);
      }
      char* kb = lds + L_KB + (16 * wv + c16) * 16;
#pragma unroll
      for (int t = 0; t < 2; t++) {
        const f32x4 a = acc[t] * LOG2E;
        *(u32x2*)(kb + (4 * t + g4) * 2048) = u32x2{pk_bf16(a[0], a[1]), pk_bf16(a[2], a[3])};
      }
      if (g4 < 2) {
#pragma unroll
        for (int i = 0; i < 4; i++) *(u16*)(kb + (4 * g4 + i) * 2048 + 8) = (u16)pk_bf16(acc[2][i] * LOG2E, 0.f);
      }
    }
    SB();
    for (u32 spins = 0; !__all((gv >> 20) == tag) && spins < SPINS; spins++) {
      __builtin_amdgcn_s_sleep(4);
      if ((gv >> 20) != tag) gv = __hip_atomic_load(gp, RLX_AGENT);
    }
    const bool on = lane < 32 && cs < G;
    const u32 cnt = on ? ((u32)gv & 1023u) : 0u, st = ((u32)gv >> 10) & 1023u;
    const u32 inc = scan64(cnt);
    const u32 total = __builtin_amdgcn_readlane(inc, 63);
    u32* list = (u32*)(lds + L_LS + wv * 768);
    const u32 base = (cs * CH + st) * 16, p0 = inc - cnt;
    const u32 lim = min(cnt, p0 < 128 ? 128 - p0 : 0u);
#pragma unroll
    for (u32 k = 0; k < 6; k++) list[k < lim ? p0 + k : 128 + lane] = base + k * 16;
    if (__any(lim > 6)) {
#pragma unroll 1
      for (u32 k = 6; k < lim; k++) list[p0 + k] = base + k * 16;
    }
    __builtin_amdgcn_wave_barrier();
    {
      const u32 a0 = list[lane], a1 = list[64 + lane];
      const bool v0 = lane < total, v1 = 64 + lane < total;
      u32x4 r0 = __builtin_amdgcn_raw_buffer_load_b128(r_rec, v0 ? a0 : OOB, 0, SC1);
      u32x4 r1 = __builtin_amdgcn_raw_buffer_load_b128(r_rec, v1 ? a1 : OOB, 0, SC1);
      u64 fv = __hip_atomic_load(fp, RLX_AGENT);
      if (on) *(u32*)(lds + L_BC + cs * 4) = (u32)gv & 0xfffffu;
      SB();
#pragma unroll
      for (int t = 0; t < 3; t++) cp[t] = f32x4{0, 0, 0, 0};
#pragma unroll
      for (int s = 0; s < 4; s++) {
        const u32x4 f1 = tr_frag(lds + L_W0, s, lane, 2 * wv + ((lane & 3) >> 1), lane & 1);
#pragma unroll
        for (int t = 0; t < 3; t++) cp[t] = mfma16(f1, hb[t][s], cp[t]);
      }
#pragma unroll
      for (int t = 0; t < 3; t++) { cp[t] *= LOG2E; PIN(cp[t]); }
      SB();
      r0 = settle(r0, v0, r_rec, a0, tg);
      r1 = settle(r1, v1, r_rec, a1, tg);
      u32 k0 = 0, k1 = 0;
      if (v0) k0 = take_rank(lds, r0);
      if (v1) k1 = take_rank(lds, r1);
      for (u32 spins = 0; !__all(fv == tag) && spins < SPINS; spins++) {
        __builtin_amdgcn_s_sleep(4);
        fv = __hip_atomic_load(fp, RLX_AGENT);
      }
      __builtin_amdgcn_s_waitcnt(0x0f70);
      pw[0] = __builtin_amdgcn_raw_buffer_load_b32(r_bt, v0 && k0 < PFR ? (r0[0] & 0x3fffu) << 8 : OOB, 0, SC1);
      pw[1] = __builtin_amdgcn_raw_buffer_load_b32(r_bt, v0 && k0 < PFR ? ((r0[0] & 0x3fffu) << 8) + 128 : OOB, 0, SC1);
      pw[2] = __builtin_amdgcn_raw_buffer_load_b32(r_bt, v1 && k1 < PFR ? (r1[0] & 0x3fffu) << 8 : OOB, 0, SC1);
      pw[3] = __builtin_amdgcn_raw_buffer_load_b32(r_bt, v1 && k1 < PFR ? ((r1[0] & 0x3fffu) << 8) + 128 : OOB, 0, SC1);
      SB();
      if (v0) put_rec(lds, r0, k0);
      if (v1) put_rec(lds, r1, k1);
    }
    if (__builtin_expect(total > 128, 0)) {
#pragma unroll 1
      for (u32 done = 128; done < total; done += 64) {
        if (__hip_atomic_load((u32*)(lds + L_CNT + 32), __ATOMIC_RELAXED, __HIP_MEMORY_SCOPE_WORKGROUP)) break;
        __builtin_amdgcn_wave_barrier();
#pragma unroll 1
        for (u32 k = p0 < done ? done - p0 : 0u; k < cnt && p0 + k < done + 64; k++) list[p0 + k - done] = base + k * 16;
        __builtin_amdgcn_wave_barrier();
        const bool v0 = done + lane < total;
        const u32 a0 = list[lane];
        u32x4 r0 = __builtin_amdgcn_raw_buffer_load_b128(r_rec, v0 ? a0 : OOB, 0, SC1);
        r0 = settle(r0, v0, r_rec, a0, tg);
        if (v0) put_rec(lds, r0, take_rank(lds, r0));
      }
      __builtin_amdgcn_s_waitcnt(0x0f70);
    }
  }
  __syncthreads();
  const u32* wcnt = (const u32*)(lds + L_CNT);
  const u32 ci = wcnt[lane >> 3], cj = wcnt[lane & 7];
  const int ovf = RFL(wcnt[8]);
  auto load_wh2 = [&]() {
    const u32x4 s_wh = srd(w_h, 65536);
    dma4(s_wh, wv0 + (wv >> 2) * 16384, wv1 + (wv >> 2) * 16384, 32768, lb + L_W0 + wv * 4096);
  };
  u32x4 v[8], wxf[8];
  int win;
  {
    const u64 gt = __ballot(cj * 8 + (lane & 7) > ci * 8 + (lane >> 3));
    const u32 rk = __builtin_popcount((u32)(gt >> (lane & 56)) & 0xffu);
    const int wl = __builtin_ctzll(__ballot(rk == (u32)(wv < 4 ? wv : 11 - wv)));
    win = wl >> 3;
    const int cntw = ovf ? 1 : __builtin_amdgcn_readlane(ci, wl);
    const int nsteps = (cntw + 15) >> 4;
    const u32 lo = r * 4;
    u32 srca = L_SRC + win * CAPW * 4 + hh * 32;
    u32 GA[8], GB[8], off[8];
    const bool cfon = rp == hh;
    u32 cfa = cfon ? (L_CF + (win * CAPW + 8 * rh + rj) * 16) : L_ZERO;
    const u32 cfs = cfon ? 256u : 0u;
    u32 sma = L_SM + (win * CAPW + 8 * hh + (c16 >> 2)) * 40 + (g4 & 1) * 32 + (lane & 3) * 8;
    u32x4 cf, kbA, kbB, sf = {0, 0, 0, 0}, mB0 = {0, 0, 0, 0}, mB1 = {0, 0, 0, 0}, mA0, mA1;
    f32x16 yA, yB;
    {
      const u32x4 i0 = *(const u32x4*)(lds + srca), i1 = *(const u32x4*)(lds + srca + 16);
      const u32x4 i2 = *(const u32x4*)(lds + srca + 64), i3 = *(const u32x4*)(lds + srca + 80);
      cf = *(const u32x4*)(lds + cfa);
      kbA = *(const u32x4*)(lds + L_KB + win * 2048 + (2 * r + hh) * 16);
      kbB = *(const u32x4*)(lds + L_KB + win * 2048 + (64 + 2 * r + hh) * 16);
      srca += 128;
      SB();
#pragma unroll
      for (int j = 0; j < 4; j++) { off[j] = i0[j] + lo; off[4 + j] = i1[j] + lo; }
#pragma unroll
      for (int j = 0; j < 8; j++) GA[j] = __builtin_amdgcn_raw_buffer_load_b32(r_bt, off[j], 0, SC1);
      SB();
#pragma unroll
      for (int j = 0; j < 8; j++) GB[j] = __builtin_amdgcn_raw_buffer_load_b32(r_bt, off[j] + 128, 0, SC1);
      SB();
      asm volatile("" :: "v"(pw[0]), "v"(pw[1]), "v"(pw[2]), "v"(pw[3]));
      yA = mfma32(cf, kbA, zero);
#pragma unroll
      for (int j = 0; j < 4; j++) { off[j] = i2[j] + lo; off[4 + j] = i3[j] + lo; }
      yA = mfma32(sel0, u32x4{GA[0], GA[1], GA[2], GA[3]}, yA);
      yA = mfma32(sel1, u32x4{GA[4], GA[5], GA[6], GA[7]}, yA);
      PIN(yA);
      SB();
#pragma unroll
      for (int j = 0; j < 8; j++) GA[j] = __builtin_amdgcn_raw_buffer_load_b32(r_bt, off[j], 0, SC1);
      SB();
    }
#define MF(d, a, bb, c) do { SB(); d = mfma32(a, bb, c); PIN(d); SB(); } while (0)
#define SA(y, i, j) do { p_ = f32x2{__builtin_amdgcn_exp2f(-y[i]), __builtin_amdgcn_exp2f(-y[(i) + 1])}; \
                         q_ = f32x2{__builtin_amdgcn_exp2f(-y[j]), __builtin_amdgcn_exp2f(-y[(j) + 1])}; SB(); p_ += 1.0f; q_ += 1.0f; SB(); } while (0)
#define SBB(y, i, j, o0, o1) do { p_ = f32x2{__builtin_amdgcn_rcpf(p_[0]), __builtin_amdgcn_rcpf(p_[1])}; \
                         q_ = f32x2{__builtin_amdgcn_rcpf(q_[0]), __builtin_amdgcn_rcpf(q_[1])}; SB(); \
                         p_ *= f32x2{y[i], y[(i) + 1]}; q_ *= f32x2{y[j], y[(j) + 1]}; SB(); \
                         u32 t0_ = pk_bf16(p_[0], p_[1]), t1_ = pk_bf16(q_[0], q_[1]); \
                         asm volatile("" : "+v"(t0_), "+v"(t1_)); o0 = t0_; o1 = t1_; SB(); } while (0)
#define S4(y, i, j, o0, o1) do { SA(y, i, j); SBB(y, i, j, o0, o1); } while (0)
#define LD2(Gx, j, add) do { Gx[j] = __builtin_amdgcn_raw_buffer_load_b32(r_bt, off[j] + (add), 0, SC1); \
                             Gx[(j) + 1] = __builtin_amdgcn_raw_buffer_load_b32(r_bt, off[(j) + 1] + (add), 0, SC1); SB(); } while (0)
    f32x2 p_, q_;
    for (int s = 1; s < nsteps; s++) {
      MF(yB, cf, kbB, zero);
      S4(yA, 0, 2, mA0[0], mA0[1]);
      MF(yB, sel0, (u32x4{GB[0], GB[1], GB[2], GB[3]}), yB);
      cfa += cfs;
      cf = *(const u32x4*)(lds + cfa);
      SA(yA, 4, 6);
      LD2(GB, 0, 128);
      SBB(yA, 4, 6, mA0[2], mA0[3]);
      LD2(GB, 2, 128);
      MF(yB, sel1, (u32x4{GB[4], GB[5], GB[6], GB[7]}), yB);
      SA(yA, 8, 10);
      LD2(GB, 4, 128);
      MF(aB0, mB0, sf, aB0);
      SBB(yA, 8, 10, mA1[0], mA1[1]);
      LD2(GB, 6, 128);
      const u32x4 i0 = *(const u32x4*)(lds + srca), i1 = *(const u32x4*)(lds + srca + 16);
      srca += 64;
      SA(yA, 12, 14);
      MF(aB1, mB1, sf, aB1);
      sf = s_frag(lds + sma);
      sma += 640;
      SBB(yA, 12, 14, mA1[2], mA1[3]);
      MF(yA, cf, kbA, zero);
      S4(yB, 0, 2, mB0[0], mB0[1]);
      MF(yA, sel0, (u32x4{GA[0], GA[1], GA[2], GA[3]}), yA);
#pragma unroll
      for (int j = 0; j < 4; j++) { off[j] = i0[j] + lo; off[4 + j] = i1[j] + lo; }
      SA(yB, 4, 6);
      LD2(GA, 0, 0);
      SBB(yB, 4, 6, mB0[2], mB0[3]);
      LD2(GA, 2, 0);
      MF(yA, sel1, (u32x4{GA[4], GA[5], GA[6], GA[7]}), yA);
      SA(yB, 8, 10);
      LD2(GA, 4, 0);
      MF(aA0, mA0, sf, aA0);
      SBB(yB, 8, 10, mB1[0], mB1[1]);
      LD2(GA, 6, 0);
      SA(yB, 12, 14);
      MF(aA1, mA1, sf, aA1);
      SBB(yB, 12, 14, mB1[2], mB1[3]);
    }
    {
      const int rem = cntw - 16 * (nsteps - 1);
      load_wh2();
      MF(yB, cf, kbB, zero);
      mA0 = u32x4{0, 0, 0, 0}; mA1 = u32x4{0, 0, 0, 0};
      S4(yA, 0, 8, mA0[0], mA1[0]);
      MF(yB, sel0, (u32x4{GB[0], GB[1], GB[2], GB[3]}), yB);
      if (rem > 4) S4(yA, 2, 10, mA0[1], mA1[1]);
      MF(yB, sel1, (u32x4{GB[4], GB[5], GB[6], GB[7]}), yB);
      if (rem > 8) S4(yA, 4, 12, mA0[2], mA1[2]);
      MF(aB0, mB0, sf, aB0);
      if (rem > 12) S4(yA, 6, 14, mA0[3], mA1[3]);
      MF(aB1, mB1, sf, aB1);
      sf = s_frag(lds + sma);
      mB0 = u32x4{0, 0, 0, 0}; mB1 = u32x4{0, 0, 0, 0};
      S4(yB, 0, 8, mB0[0], mB1[0]);
      MF(aA0, mA0, sf, aA0);
      if (rem > 4) S4(yB, 2, 10, mB0[1], mB1[1]);
      MF(aA1, mA1, sf, aA1);
      if (rem > 8) S4(yB, 4, 12, mB0[2], mB1[2]);
      if (rem > 12) S4(yB, 6, 14, mB0[3], mB1[3]);
      MF(aB0, mB0, sf, aB0);
      MF(aB1, mB1, sf, aB1);
    }
#pragma unroll
    for (int q8 = 0; q8 < 8; q8++) {
      const f32x16& a0 = q8 < 4 ? aA0 : aB0;
      const f32x16& a1 = q8 < 4 ? aA1 : aB1;
#pragma unroll
      for (int q = 0; q < 4; q++) v[q8][q] = pk_bf16(a0[4 * (q8 & 3) + q], a1[4 * (q8 & 3) + q]);
    }
    if (r < 20 && (r & 3) == 0 && !ovf) {
      char* mi = lds + mi_row(r * 2 + win) + 16 * hh;
#pragma unroll
      for (int q8 = 0; q8 < 8; q8++) *(u32x4*)(mi + 32 * q8) = v[q8];
    }
  }
  if (__builtin_expect(ovf, 0)) {
    float* macc = (float*)(lds + L_SM);
    float* pacc = (float*)(lds + L_SM + NPB * D * 4);
    __syncthreads();
    for (int i = tid; i < NPB * D + NPB * 4; i += NT) macc[i] = 0.f;
    __syncthreads();
    const u32 wxp = *(const u32*)(lds + L_WX + lane * 4);
    const float wx0 = bf2f(wxp), wx1 = bfhi(wxp);
    const u32x4 kc0 = *(const u32x4*)(lds + L_KB + (2 * lane) * 16), kc1 = *(const u32x4*)(lds + L_KB + (2 * lane + 1) * 16);
    const float wd0 = bf2f(kc0[3]) + bfhi(kc0[3]), wd1 = bf2f(kc1[3]) + bfhi(kc1[3]);
    const float wa0 = bfhi(kc0[2]), wa1 = bfhi(kc1[2]);
    u32 rot = 0;
    for (int c = 0; c < G; c++) {
      const u32 oo = RFL(*(const u32*)(lds + L_BC + c * 4));
      const u32 s0 = oo >> 10, n = oo & 1023u;
      for (u32 k = (wv - rot) & 7u; k < n; k += 8) {
        u32x4 rr = __builtin_amdgcn_raw_buffer_load_b128(r_rec, (c * CH + s0 + k) * 16, 0, SC1);
        rr = settle(rr, true, r_rec, (c * CH + s0 + k) * 16, tg);
        const u32 nl = (rr[0] >> 14) & 0x3fu, sl = nl >> 3, wn = nl & 7u;
        const u32 bv = __builtin_amdgcn_raw_buffer_load_b32(r_bt, ((rr[0] & 0x3fffu) << 8) + lane * 4, 0, SC1);
        const float d2 = bfhi(dist2(rr[2], rr[3])), eav = bf2f(rr[1]);
        const float a0 = bf2f(*(const u16*)(lds + L_KB + wn * 2048 + (2 * lane) * 16 + sl * 2));
        const float a1 = bf2f(*(const u16*)(lds + L_KB + wn * 2048 + (2 * lane + 1) * 16 + sl * 2));
        const float y0 = a0 + bf2f(bv) + d2 * wd0 + eav * wa0, y1 = a1 + bfhi(bv) + d2 * wd1 + eav * wa1;
        const u32 mp = silu2(y0, y1);
        const float m0 = bf2f(mp), m1 = bfhi(mp);
        atomicAdd(&macc[nl * D + 2 * lane], m0);
        atomicAdd(&macc[nl * D + 2 * lane + 1], m1);
        float csv = m0 * wx0 + m1 * wx1;
#pragma unroll
        for (int o = 32; o > 0; o >>= 1) csv += __shfl_xor(csv, o, 64);
        if (lane < 3) atomicAdd(&pacc[nl * 4 + lane], csv * (lane == 0 ? bf2f(rr[2]) : lane == 1 ? bfhi(rr[2]) : bf2f(rr[3])));
      }
      rot += n;
    }
    __syncthreads();
    for (int i = tid; i < NPB * D / 2; i += NT) {
      const int n = i >> 6, f = (i & 63) * 2;
      *(u32*)(lds + mi_row(n) + f * 2) = pk_bf16(macc[n * D + f], macc[n * D + f + 1]);
    }
    if (tid < NPB * 3) {
      const int n = tid / 3, c = tid - n * 3;
      if (nb0 + n < N) out[(size_t)N * D + (size_t)nb0 * 3 + tid] = (u16)pk_bf16(bf2f(*(const u16*)(lds + L_PD + tid * 2)) + LN2 * pacc[n * 4 + c], 0.f);
    }
  }
  u32x2 hr[3];
  u32x4 f2[4];
#pragma unroll
  for (int t = 0; t < 3; t++) {
    const int n = tile_node(t, c16);
    hr[t] = *(const u32x2*)(lds + L_HS + n * 256 + 16 * ((2 * wv + (g4 >> 1)) ^ hswz(n)) + 8 * (g4 & 1));
  }
#pragma unroll
  for (int q8 = 0; q8 < 8; q8++) wxf[q8] = *(const u32x4*)(lds + L_WX + (16 * q8 + 8 * hh) * 2);
  const int kind = lane & 3, pnl = (lane >> 2) * 8 + win;
  const bool pon = kind != 0 && lane < 20 && !ovf;
  const u32 p0 = *(const u16*)(lds + L_PD + (pon ? pnl * 6 + kind * 2 - 2 : 0));
  const int mia = mi_row(tile_node(0, c16)) + g4 * 16, mib = mi_row(tile_node(2, c16)) + g4 * 16;
  asm volatile("s_waitcnt vmcnt(0)" ::: "memory");
  __syncthreads();
  {
    u32x4 mb[3][4];
#pragma unroll
    for (int s = 0; s < 4; s++) {
      f2[s] = tr_frag(lds + L_W0, s, lane, 2 * wv + ((lane & 3) >> 1), lane & 1);
      mb[0][s] = *(const u32x4*)(lds + mia + s * 64);
      mb[1][s] = *(const u32x4*)(lds + mia + 8192 + s * 64);
      mb[2][s] = *(const u32x4*)(lds + mib + s * 64);
    }
    SB();
#pragma unroll
    for (int s = 0; s < 4; s++)
#pragma unroll
      for (int t = 0; t < 3; t++) cp[t] = mfma16(f2[s], mb[t][s], cp[t]);
    f32x16 z = zero, z1 = zero;
#pragma unroll
    for (int t = 0; t < 3; t++) {
      const f32x4 y = cp[t];
      f32x4 e;
#pragma unroll
      for (int i = 0; i < 4; i++) e[i] = __builtin_amdgcn_exp2f(-y[i]);
      SB();
      if (t & 1) { z1 = mfma32(wxf[3 * t], v[3 * t], z1); PIN(z1); } else { z = mfma32(wxf[3 * t], v[3 * t], z); PIN(z); }
      SB();
      e += 1.0f;
#pragma unroll
      for (int i = 0; i < 4; i++) e[i] = __builtin_amdgcn_rcpf(e[i]);
      SB();
      if (t & 1) { z = mfma32(wxf[3 * t + 1], v[3 * t + 1], z); PIN(z); } else { z1 = mfma32(wxf[3 * t + 1], v[3 * t + 1], z1); PIN(z1); }
      SB();
      const f32x4 o = f32x4{bf2f(hr[t][0]), bfhi(hr[t][0]), bf2f(hr[t][1]), bfhi(hr[t][1])} + (y * LN2) * e;
      *(u32x2*)(lds + L_SM + tile_node(t, c16) * 272 + wv * 32 + g4 * 8) = u32x2{pk_bf16(o[0], o[1]), pk_bf16(o[2], o[3])};
      SB();
      if (t == 0) { z = mfma32(wxf[2], v[2], z); PIN(z); }
      if (t == 1) { z1 = mfma32(wxf[5], v[5], z1); PIN(z1); }
      SB();
    }
    __syncthreads();
    const __amdgpu_buffer_rsrc_t r_out = rsrc(out, N * 262);
    const u32x4 o0 = *(const u32x4*)(lds + L_SM + (tid >> 4) * 272 + (tid & 15) * 16);
    const u32x4 o1 = *(const u32x4*)(lds + L_SM + (32 + ((tid >> 4) & 7)) * 272 + (tid & 15) * 16);
    const u32 oo = nb0 * 256 + tid * 16;
    __builtin_amdgcn_raw_buffer_store_b128(o0, r_out, oo < N * 256 ? oo : OOB, 0, 0);
    __builtin_amdgcn_raw_buffer_store_b128(o1, r_out, tid < 128 && oo + 8192 < N * 256 ? oo + 8192 : OOB, 0, 0);
    __builtin_amdgcn_raw_buffer_store_b16((u16)pk_bf16(bf2f(p0) + LN2 * (z[0] + z1[0]), 0.f), r_out,
                                          pon ? N * 256 + ((nb0 + pnl) * 3 + kind - 1) * 2 : OOB, 0, 0);
  }
  if (b == 0 && tid == 0) __hip_atomic_store((gu64*)(ws + WS_EP), ep + 1, RLX_AGENT);
}

__global__ __launch_bounds__(128) void k_edge_any(const u16* h, const u16* pos, const u16* ea, const int* src, const int* dst,
                                                  const u16* w_e, const u16* w_x, float* acc, int N) {
  __shared__ float x[2 * D + 2], red[2];
  const int e = blockIdx.x, f = threadIdx.x, s = src[e], d = dst[e];
  const u32 xy = pk_bf16(bf2f(pos[d * 3]) - bf2f(pos[s * 3]), bf2f(pos[d * 3 + 1]) - bf2f(pos[s * 3 + 1]));
  const u32 zz = pk_bf16(bf2f(pos[d * 3 + 2]) - bf2f(pos[s * 3 + 2]), 0.f);
  x[f] = bf2f(h[(size_t)d * D + f]); x[D + f] = bf2f(h[(size_t)s * D + f]);
  if (f == 0) { x[2 * D] = bfhi(dist2(xy, zz)); x[2 * D + 1] = bf2f(ea[e]); }
  __syncthreads();
  float y = 0.f;
  for (int k = 0; k < 2 * D + 2; k++) y += x[k] * bf2f(w_e[k * D + f]);
  const float m = LN2 * bf2f(silu2(y * LOG2E, 0.f));
  atomicAdd(&acc[(size_t)d * D + f], m);
  float c = m * bf2f(w_x[f]);
  for (int o = 32; o > 0; o >>= 1) c += __shfl_xor(c, o, 64);
  if ((f & 63) == 0) red[f >> 6] = c;
  __syncthreads();
  if (f < 3) atomicAdd(&acc[(size_t)N * D + d * 3 + f], (red[0] + red[1]) * (f == 0 ? bf2f(xy) : f == 1 ? bfhi(xy) : bf2f(zz)));
}
__global__ __launch_bounds__(128) void k_node_any(const u16* h, const u16* pos, const u16* w_h, const float* acc, u16* out, int N) {
  __shared__ float x[2 * D];
  const int n = blockIdx.x, f = threadIdx.x;
  x[f] = bf2f(h[(size_t)n * D + f]); x[D + f] = bf2f(pk_bf16(acc[(size_t)n * D + f], 0.f));
  __syncthreads();
  float y = 0.f;
  for (int k = 0; k < 2 * D; k++) y += x[k] * bf2f(w_h[k * D + f]);
  out[(size_t)n * D + f] = (u16)pk_bf16(x[f] + y * __builtin_amdgcn_rcpf(1.f + __builtin_amdgcn_exp2f(-y * LOG2E)), 0.f);
  if (f < 3) out[(size_t)N * D + n * 3 + f] = (u16)pk_bf16(bf2f(pos[n * 3 + f]) + acc[(size_t)N * D + n * 3 + f], 0.f);
}

extern "C" void kernel_launch(void* const* d_in, const int* in_sizes, int n_in, void* d_out, int out_size, void* d_ws,
                              size_t ws_size, hipStream_t stream) {
  const u16 *h = (const u16*)d_in[0], *pos = (const u16*)d_in[1], *ea = (const u16*)d_in[2];
  const int *src = (const int*)d_in[3], *dst = (const int*)d_in[4];
  const u16 *w_e = (const u16*)d_in[5], *w_x = (const u16*)d_in[6], *w_h = (const u16*)d_in[7];
  const int N = in_sizes[0] / D, E = in_sizes[3];
  const int NB = (N + NPB - 1) / NPB, C = (E + CH - 1) / CH;
  const int G = NB > C ? NB : C;
  if (G <= MAXG && N * 6 <= MAXPOS && (N & 7) == 0 && ws_size >= WS_SLOT0 + 4 * WS_SLOT) {
    k_egnn<<<G, NT, 0, stream>>>(h, pos, ea, src, dst, w_e, w_x, w_h, (char*)d_ws, (u16*)d_out, N, E, G);
  } else {
    (void)hipMemsetAsync(d_ws, 0, (size_t)N * (D + 3) * sizeof(float), stream);
    if (E > 0) k_edge_any<<<E, 128, 0, stream>>>(h, pos, ea, src, dst, w_e, w_x, (float*)d_ws, N);
    k_node_any<<<N, 128, 0, stream>>>(h, pos, w_h, (const float*)d_ws, (u16*)d_out, N);
  }
}
```

```cpp
#include <hip/hip_runtime.h>
#include <hip/hip_bf16.h>

typedef unsigned int u32;
typedef unsigned short u16;
typedef unsigned long long u64;
typedef u32 u32x2 __attribute__((ext_vector_type(2)));
typedef u32 u32x4 __attribute__((ext_vector_type(4)));
typedef short s16x4 __attribute__((ext_vector_type(4)));
typedef short bf16x8 __attribute__((ext_vector_type(8)));
typedef float f32x2 __attribute__((ext_vector_type(2)));
typedef float f32x4 __attribute__((ext_vector_type(4)));
typedef float f32x16 __attribute__((ext_vector_type(16)));
typedef __bf16 bf16x2_t __attribute__((ext_vector_type(2)));
typedef __attribute__((address_space(1))) u64 gu64;
typedef __attribute__((address_space(3))) s16x4 lds_s16x4;
typedef __attribute__((address_space(3))) char lds_char;
#define DI __device__ __forceinline__
#define RLX_AGENT __ATOMIC_RELAXED, __HIP_MEMORY_SCOPE_AGENT
#define SC1 16
#define SB() __builtin_amdgcn_sched_barrier(0)
#define PIN(x) asm volatile("" : "+v"(x))
#define RFL(x) __builtin_amdgcn_readfirstlane(x)

constexpr int D = 128;
constexpr int NT = 512;
constexpr int NPB = 40;
constexpr int CH = 640;
constexpr int MAXG = 256;
constexpr int CAPW = 128;
constexpr int MAXPOS = 61440;
constexpr u32 PFR = 16;
constexpr u32 SPINS = 1u << 16;
constexpr u32 CODE_BYTES = 16384, CPL = 16;
constexpr float LOG2E = 1.4426950408889634f;
constexpr float LN2 = 0.6931471805599453f;
constexpr u32 OOB = 0xfffff000u;

constexpr size_t WS_EP = 0;
constexpr size_t WS_SLOT0 = (size_t)2 << 20, WS_SLOT = (size_t)6 << 20;
constexpr size_t WS_GR = 0;
constexpr size_t WS_BT = WS_GR + (size_t)MAXG * MAXG * 8;
constexpr size_t WS_REC = WS_BT + (size_t)MAXG * NPB * 256;
constexpr size_t WS_FL = WS_REC + (size_t)MAXG * CH * 16;
static_assert(WS_FL + MAXG * 8 <= WS_SLOT, "slot");

DI float bf2f(u32 b) { return __builtin_bit_cast(float, b << 16); }
DI float bfhi(u32 b) { return __builtin_bit_cast(float, b & 0xffff0000u); }
DI u32 pk_bf16(float lo, float hi) {
  f32x2 v = {lo, hi};
  bf16x2_t r = __builtin_convertvector(v, bf16x2_t);
  return __builtin_bit_cast(u32, r);
}
DI f32x4 mfma16(u32x4 a, u32x4 b, f32x4 c) {
  return __builtin_amdgcn_mfma_f32_16x16x32_bf16(__builtin_bit_cast(bf16x8, a), __builtin_bit_cast(bf16x8, b), c, 0, 0, 0);
}
DI f32x16 mfma32(u32x4 a, u32x4 b, f32x16 c) {
  return __builtin_amdgcn_mfma_f32_32x32x16_bf16(__builtin_bit_cast(bf16x8, a), __builtin_bit_cast(bf16x8, b), c, 0, 0, 0);
}
DI __amdgpu_buffer_rsrc_t rsrc(const void* p, u32 bytes) { return __builtin_amdgcn_make_buffer_rsrc((void*)p, 0, bytes, 0x00020000); }
DI u32x4 srd(const void* p, u32 bytes) { const u64 a = (u64)p; return u32x4{(u32)a, (u32)(a >> 32) & 0xffffu, bytes, 0x00020000u}; }
DI void dma4(u32x4 sd, u32 v0, u32 v1, u32 soff, u32 ldsa) {
  u32 keep;
  asm volatile("s_mov_b32 %0, m0\n\ts_mov_b32 m0, %1\n\ts_nop 0\n\t"
               "buffer_load_dwordx4 %2, %4, %5 offen lds\n\tbuffer_load_dwordx4 %2, %4, %5 offen offset:1024 lds\n\t"
               "buffer_load_dwordx4 %3, %4, %5 offen offset:2048 lds\n\tbuffer_load_dwordx4 %3, %4, %5 offen offset:3072 lds\n\t"
               "s_mov_b32 m0, %0"
               : "=&s"(keep) : "s"(RFL(ldsa)), "v"(v0), "v"(v1), "s"(sd), "s"(soff) : "memory");
}
DI void dma2(u32x4 sd, u32 v0, u32 soff, u32 ldsa) {
  u32 keep;
  asm volatile("s_mov_b32 %0, m0\n\ts_mov_b32 m0, %1\n\ts_nop 0\n\t"
               "buffer_load_dwordx4 %2, %3, %4 offen lds\n\tbuffer_load_dwordx4 %2, %3, %4 offen offset:1024 lds\n\t"
               "s_mov_b32 m0, %0"
               : "=&s"(keep) : "s"(RFL(ldsa)), "v"(v0), "s"(sd), "s"(soff) : "memory");
}
DI u32 scan64(u32 x) {
  x += __builtin_amdgcn_update_dpp(0u, x, 0x111, 0xf, 0xf, false);
  x += __builtin_amdgcn_update_dpp(0u, x, 0x112, 0xf, 0xf, false);
  x += __builtin_amdgcn_update_dpp(0u, x, 0x114, 0xf, 0xf, false);
  x += __builtin_amdgcn_update_dpp(0u, x, 0x118, 0xf, 0xf, false);
  x += __builtin_amdgcn_update_dpp(0u, x, 0x142, 0xa, 0xf, false);
  x += __builtin_amdgcn_update_dpp(0u, x, 0x143, 0xc, 0xf, false);
  return x;
}

constexpr int L_ZERO = 0;
constexpr int L_CNT = L_ZERO + 16;
constexpr int L_BC = L_CNT + 48;
constexpr int L_SY = L_BC + 1024;
constexpr int L_SM = L_SY + 32;
constexpr int L_CF = L_SM + 8 * 8 * 640;
constexpr int L_SRC = L_CF + 8 * CAPW * 16;
constexpr int L_KB = L_SRC + 8 * CAPW * 4 + 64;
constexpr int L_W1 = L_KB + 8 * D * 16;
constexpr int L_W0 = L_W1 + 32768;
constexpr int L_HS = L_W0 + 32768;
constexpr int L_WX = L_HS + NPB * 256;
constexpr int L_PD = L_WX + 256;
constexpr int L_LS = L_PD + 256;
constexpr int L_END = L_LS + 8 * 768;
constexpr int L_RS = L_W0 - CH * 16;
constexpr int L_POS = L_RS - MAXPOS;
static_assert(L_END <= 160 * 1024, "lds");
static_assert(L_POS >= L_SM + 36864 && L_POS <= L_CF && (L_POS % 16) == 0, "pos table");
static_assert((L_SM % 16) == 0 && (L_BC % 16) == 0 && (L_SRC % 16) == 0 && (L_KB % 16) == 0 && (L_W0 % 16) == 0 && (L_HS % 16) == 0 && (L_WX % 16) == 0 && (L_LS % 16) == 0, "align");

DI int tile_node(int t, int c) { return t < 2 ? ((c & 3) * 8 + 4 * t + (c >> 2)) : (32 + (c & 7)); }
DI int mi_row(int nl) { return L_KB + (nl & 7) * 2048 + (nl & 3) * 64 + (nl >> 3) * 272; }
DI int wswz(int k) { return ((k & 3) << 2) | ((k >> 2) & 2); }
DI int hswz(int n) { return ((n & 3) << 2) | ((n >> 3) & 3); }
DI u32x4 tr_frag(const char* img, int s, int lane, int ch, int hf) {
  const int k0 = 8 * (lane >> 4) + ((lane & 15) >> 2);
  const char* p = img + (32 * s + k0) * 256 + 16 * (ch ^ wswz(k0)) + 8 * hf;
  const s16x4 a = __builtin_amdgcn_ds_read_tr16_b64_v4i16((lds_s16x4*)p);
  const s16x4 b = __builtin_amdgcn_ds_read_tr16_b64_v4i16((lds_s16x4*)(p + 1024));
  const u32x2 x = __builtin_bit_cast(u32x2, a), y = __builtin_bit_cast(u32x2, b);
  return u32x4{x[0], x[1], y[0], y[1]};
}
DI u32x4 s_frag(const char* p) {
  const u32x2 x = __builtin_bit_cast(u32x2, __builtin_amdgcn_ds_read_tr16_b64_v4i16((lds_s16x4*)p));
  const u32x2 y = __builtin_bit_cast(u32x2, __builtin_amdgcn_ds_read_tr16_b64_v4i16((lds_s16x4*)(p + 160)));
  return u32x4{x[0], x[1], y[0], y[1]};
}
DI u32x4 h_frag(const char* lds, int t, int s, int lane) {
  const int n = tile_node(t, lane & 15);
  return *(const u32x4*)(lds + L_HS + n * 256 + 16 * ((4 * s + (lane >> 4)) ^ hswz(n)));
}
DI u32 dist2(u32 w2, u32 w3) {
  const float dx = bf2f(w2), dy = bfhi(w2), dz = bf2f(w3);
  const u32 q0 = pk_bf16(dx * dx, dy * dy), q1 = pk_bf16(dz * dz, 0.f);
  const float s2 = bf2f(q0) + bfhi(q0) + bf2f(q1);
  return pk_bf16(s2, s2);
}
DI u32 take_rank(char* lds, u32x4 rc) { return atomicAdd((u32*)(lds + L_CNT) + ((rc[0] >> 14) & 7u), 1u); }
DI void put_rec(char* lds, u32x4 rc, u32 rank) {
  const u32 win = (rc[0] >> 14) & 7u, slot = (rc[0] >> 17) & 7u;
  const u64 oh = 0x3f80ull << (slot * 16);
  const u32x4 cf = {slot < 4 ? (u32)oh : 0u, slot < 4 ? (u32)(oh >> 32) : 0u, (slot == 4 ? 0x3f80u : 0u) | (rc[1] << 16), dist2(rc[2], rc[3])};
  if (rank < CAPW) {
    const u32 k = ((rank & 1u) << 3) + ((rank & 15u) >> 1), sp = win * CAPW + (rank & ~15u) + k;
    *(u32*)(lds + L_SRC + sp * 4) = (rc[0] & 0x3fffu) << 8;
    *(u32x4*)(lds + L_CF + sp * 16) = cf;
    *(u32x2*)(lds + L_SM + sp * 40 + slot * 8) = u32x2{0x3f80u | (rc[2] << 16), __builtin_amdgcn_alignbit(rc[3], rc[2], 16)};
  } else *(u32*)(lds + L_CNT + 32) = 1;
}
DI void rec_tags(u64 tag, u32 (&t)[3]) {
  t[0] = (u32)tag << 20; t[1] = ((u32)tag >> 12) << 16; t[2] = ((u32)(tag >> 28) ^ (u32)tag) << 16;
}
DI bool rec_stale(u32x4 rc, const u32 (&t)[3]) { return (((rc[0] ^ t[0]) >> 20) | (((rc[1] ^ t[1]) | (rc[3] ^ t[2])) >> 16)) != 0; }
DI u32x4 settle(u32x4 rc, bool on, __amdgpu_buffer_rsrc_t r, u32 off, const u32 (&t)[3]) {
  for (u32 spins = 0; __any(on && rec_stale(rc, t)) && spins < SPINS; spins++) {
    __builtin_amdgcn_s_sleep(4);
    if (on && rec_stale(rc, t)) rc = __builtin_amdgcn_raw_buffer_load_b128(r, off, 0, SC1);
  }
  return rc;
}
DI void arrive(u32 lb, int i) {
  asm volatile("s_mov_b64 exec, 1\n\tds_add_u32 %0, %1 offset:%2\n\ts_mov_b64 exec, -1" :: "v"(lb), "v"(1u), "i"(L_SY + 4 * i) : "memory");
}
DI void await(char* lds, int i, u32 n) {
  asm volatile("" ::: "memory");
  while ((u32)RFL(__hip_atomic_load((u32*)(lds + L_SY) + i, __ATOMIC_RELAXED, __HIP_MEMORY_SCOPE_WORKGROUP)) < n) { }
  asm volatile("" ::: "memory");
}
DI u32 silu2(float a, float b) {
  f32x2 yy = {a, b};
  f32x2 e = {__builtin_amdgcn_exp2f(-a), __builtin_amdgcn_exp2f(-b)};
  e += 1.0f;
  f32x2 rc = {__builtin_amdgcn_rcpf(e[0]), __builtin_amdgcn_rcpf(e[1])};
  yy *= rc;
  return pk_bf16(yy[0], yy[1]);
}

__global__ __launch_bounds__(NT, 2) void k_egnn(const u16* __restrict__ h, const u16* __restrict__ pos,
                                                const u16* __restrict__ ea, const int* __restrict__ src,
                                                const int* __restrict__ dst, const u16* __restrict__ w_e,
                                                const u16* __restrict__ w_x, const u16* __restrict__ w_h,
                                                char* __restrict__ ws, u16* __restrict__ out, int N, int E, int G) {
  __shared__ __attribute__((aligned(16))) char lds[L_END];
  const int tid = threadIdx.x, lane = tid & 63, b = blockIdx.x;
  const int wv = RFL(tid >> 6);
  const int u = wv & 3, c16 = lane & 15, g4 = lane >> 4;
  const bool role1 = wv >= 4;
  const u32 lb = (u32)(size_t)(lds_char*)lds;
  if (tid < L_SM / 4) *(u32*)(lds + tid * 4) = 0;
  __syncthreads();
  const int ne = RFL(max(0, min(CH, E - b * CH)));
  u32 ed[3], es[3], ee[3];
  u32x2 epoch;
  {
    const u32x4 s_d = srd(dst + b * CH, ne * 4), s_s = srd(src + b * CH, ne * 4), s_e = srd(ea + b * CH, ne * 2);
    asm volatile("s_cmp_lt_u32 %15, 4\n\ts_cbranch_scc1 1f\n\t"
                 "buffer_load_dword %0, %10, %12, 0 offen\n\tbuffer_load_dword %1, %10, %12, 0 offen offset:1024\n\t"
                 "buffer_load_dword %2, %10, %12, 0 offen offset:2048\n\tbuffer_load_dword %3, %10, %13, 0 offen\n\t"
                 "buffer_load_dword %4, %10, %13, 0 offen offset:1024\n\tbuffer_load_dword %5, %10, %13, 0 offen offset:2048\n\t"
                 "buffer_load_ushort %6, %11, %14, 0 offen\n\tbuffer_load_ushort %7, %11, %14, 0 offen offset:512\n\t"
                 "buffer_load_ushort %8, %11, %14, 0 offen offset:1024\n"
                 "1:\n\tglobal_load_dwordx2 %9, %16, %17 sc1"
                 : "=&v"(ed[0]), "=&v"(ed[1]), "=&v"(ed[2]), "=&v"(es[0]), "=&v"(es[1]), "=&v"(es[2]), "=&v"(ee[0]), "=&v"(ee[1]), "=&v"(ee[2]), "=&v"(epoch)
                 : "v"((tid & 255) * 4), "v"((tid & 255) * 2), "s"(s_d), "s"(s_s), "s"(s_e), "s"(wv), "v"(0u), "s"(ws + WS_EP) : "memory", "scc");
  }
  int bk;
  { const int q = G >> 3, r = G & 7, x = b & 7, i = b >> 3;
    bk = (x < r ? x * (q + 1) : r * (q + 1) + (x - r) * q) + i; }
  const int nb0 = bk * NPB;
  const int r = lane & 31, hh = lane >> 5;
  const int rh = (r >> 2) & 1, rp = r >> 4, rj = (r & 3) | (((r >> 3) & 1) << 2);
  f32x16 aA0, aA1, aB0, aB1, zero;
  u32x4 sel0, sel1;
  auto setup = [&]() {
#pragma unroll
    for (int i = 0; i < 16; i++) zero[i] = 0.f;
    u32x4 z4 = {0, 0, 0, 0};
    PIN(z4);
    aA0 = mfma32(z4, z4, zero); aA1 = mfma32(z4, z4, zero); aB0 = mfma32(z4, z4, zero); aB1 = mfma32(z4, z4, zero);
    const u32 one = rh == hh ? 0x3f80u << (rp * 16) : 0u;
#pragma unroll
    for (int q = 0; q < 4; q++) { sel0[q] = (rj == q) ? one : 0u; sel1[q] = (rj == 4 + q) ? one : 0u; }
    PIN(aA0); PIN(aA1); PIN(aB0); PIN(aB1); PIN(sel0); PIN(sel1);
  };
  u32 ep_lo, ep_hi;
  u32x4 hb[3][4];
  const u32 wv0 = g4 * 256 + 16 * (c16 ^ (g4 << 2)) + u * 4096, wv1 = wv0 ^ 32;
  if (!role1) {
    const u32x4 s_pos = srd(pos, N * 6), s_h = srd(h, N * 256), s_we = srd(w_e, 258 * 256), s_wh = srd(w_h, 65536);
#pragma unroll
    for (int i = 0; i < 4; i++)
      if (i < 3 || u < 3) dma4(s_pos, lane * 16 + u * 4096, lane * 16 + u * 4096, i * 16384, lb + L_POS + i * 16384 + u * 4096);
    dma2(s_h, g4 * 256 + 16 * (c16 ^ ((g4 << 2) | u)) + u * 2048, nb0 * 256, lb + L_HS + u * 2048);
    if (u == 0) dma2(s_h, g4 * 256 + 16 * (c16 ^ (g4 << 2)) + 8192, nb0 * 256, lb + L_HS + 8192);
    dma4(s_we, wv0, wv1, 32768, lb + L_W0 + u * 4096);
    dma4(s_we, wv0, wv1, 32768 + 16384, lb + L_W0 + 16384 + u * 4096);
    u32 wdv, wav, wxv, pf1, pf2, pf3;
    {
      const u32x4 s_wx = srd(w_x, 256), s_c = srd((const void*)(__builtin_amdgcn_s_getpc() & ~127ull), CODE_BYTES);
      const u32 xi = (b >> 3) & 31;
      asm volatile("buffer_load_dword %5, %13, %14, 0 offen\n\t"
                   "buffer_load_ushort %0, %6, %7, %8 offen\n\tbuffer_load_ushort %1, %6, %7, %8 offen offset:256\n\t"
                   "buffer_load_ushort %2, %6, %9, 0 offen\n\tbuffer_load_dword %3, %10, %7, 0 offen\n\t"
                   "buffer_load_dword %4, %11, %12, 0 offen"
                   : "=&v"(wdv), "=&v"(wav), "=&v"(wxv), "=&v"(pf1), "=&v"(pf2), "=&v"(pf3)
                   : "v"((tid & 127) * 2), "s"(s_we), "s"(65536u), "s"(s_wx), "v"(tid < 8 ? (xi * 8 + tid) * 128 : OOB),
                     "v"(tid - 64 < 16u ? (xi * 16 + tid - 64) * 128 : OOB), "s"(s_wh),
                     "v"(tid - 128 < CPL ? ((xi & 7) * CPL + tid - 128) * 128 : OOB), "s"(s_c) : "memory");
    }
    asm volatile("s_waitcnt vmcnt(16)\n\tv_readfirstlane_b32 %0, %2\n\tv_readfirstlane_b32 %1, %3\n\ts_nop 1"
                 : "=s"(ep_lo), "=s"(ep_hi) : "v"(epoch[0]), "v"(epoch[1]) : "memory");
    arrive(lb, 0);
    setup();
    asm volatile("s_waitcnt vmcnt(6)" ::: "memory");
    arrive(lb, 1);
    char* const slot = ws + WS_SLOT0 + (size_t)(RFL(ep_lo) & 3u) * WS_SLOT;
    await(lds, 1, 4);
#pragma unroll
    for (int s = 0; s < 4; s++)
#pragma unroll
      for (int t = 0; t < 3; t++) hb[t][s] = h_frag(lds, t, s, lane);
    f32x4 acc[3][2];
#pragma unroll
    for (int t = 0; t < 3; t++) { acc[t][0] = f32x4{0, 0, 0, 0}; acc[t][1] = f32x4{0, 0, 0, 0}; }
#pragma unroll
    for (int s = 0; s < 4; s++) {
      const u32x4 fa = tr_frag(lds + L_W0, s, lane, 4 * u + (lane & 3), 0), fb = tr_frag(lds + L_W0, s, lane, 4 * u + (lane & 3), 1);
#pragma unroll
      for (int t = 0; t < 3; t++) { acc[t][0] = mfma16(fa, hb[t][s], acc[t][0]); acc[t][1] = mfma16(fb, hb[t][s], acc[t][1]); }
    }
    asm volatile("s_waitcnt vmcnt(0)" : "+v"(wdv), "+v"(wav), "+v"(wxv), "+v"(pf1), "+v"(pf2), "+v"(pf3), "+v"(acc[2][1]) :: "memory");
    arrive(lb, 3);
    const __amdgpu_buffer_rsrc_t r_bw = rsrc(slot + WS_BT, N * 256);
#pragma unroll
    for (int t = 0; t < 3; t++) {
      const f32x4 x = acc[t][0] * LOG2E, y = acc[t][1] * LOG2E;
      const u32x4 o = {pk_bf16(x[0], x[1]), pk_bf16(x[2], x[3]), pk_bf16(y[0], y[1]), pk_bf16(y[2], y[3])};
      __builtin_amdgcn_raw_buffer_store_b128(o, r_bw, (t < 2 || c16 < 8) ? (nb0 + tile_node(t, c16)) * 256 + u * 64 + g4 * 16 : OOB, 0, SC1);
    }
    await(lds, 3, 4);
    dma4(s_wh, wv0, wv1, 0, lb + L_W0 + u * 4096);
    dma4(s_wh, wv0, wv1, 16384, lb + L_W0 + 16384 + u * 4096);
    await(lds, 4, 4);
    dma4(s_we, wv0, wv1, 0, lb + L_W1 + u * 4096);
    dma4(s_we, wv0, wv1, 16384, lb + L_W1 + 16384 + u * 4096);
    {
      const float wd = bf2f(wdv) * LOG2E;
      const u32 wdh = pk_bf16(wd, 0.f);
      const u32 wdp = wdh | (pk_bf16(wd - bf2f(wdh), 0.f) << 16), wap = pk_bf16(bf2f(wav) * LOG2E, 0.f);
#pragma unroll
      for (int i = 0; i < 4; i++) {
        char* kb = lds + L_KB + (i * 256 + tid) * 16;
        *(u32*)(kb + 12) = wdp;
        *(u16*)(kb + 10) = (u16)wap;
      }
      if (tid < D) *(u16*)(lds + L_WX + tid * 2) = (u16)wxv;
    }
    asm volatile("s_waitcnt vmcnt(0)" ::: "memory");
    if (lane == 0 && __hip_atomic_fetch_add((u32*)(lds + L_SY) + 5, 1u, __ATOMIC_RELAXED, __HIP_MEMORY_SCOPE_WORKGROUP) == 3)
      __hip_atomic_store((gu64*)(slot + WS_FL) + b, ((((u64)ep_hi << 32) | ep_lo) + 1) & 0xfffffffffffull, RLX_AGENT);
  } else {
    const int t1 = tid - 256;
    __builtin_amdgcn_s_setprio(2);
    const u32x4 z = {0, 0, 0, 0};
    setup();
    asm volatile("s_waitcnt vmcnt(7)" : "+v"(ed[0]), "+v"(ed[1]), "+v"(ed[2]) :: "memory");
    u32 rank[3], ebk[3];
#pragma unroll
    for (int i = 0; i < 3; i++) {
      ebk[i] = ed[i] / NPB;
      rank[i] = 0;
      if (t1 + 256 * i < ne) rank[i] = atomicAdd((u32*)(lds + L_BC) + ebk[i], 1u);
    }
    arrive(lb, 0);
    asm volatile("s_waitcnt vmcnt(0)\n\tv_readfirstlane_b32 %0, %8\n\tv_readfirstlane_b32 %1, %9\n\ts_nop 1"
                 : "=s"(ep_lo), "=s"(ep_hi), "+v"(es[0]), "+v"(es[1]), "+v"(es[2]), "+v"(ee[0]), "+v"(ee[1]), "+v"(ee[2])
                 : "v"(epoch[0]), "v"(epoch[1]) : "memory");
    ep_lo = RFL(ep_lo); ep_hi = RFL(ep_hi);
    char* const slot = ws + WS_SLOT0 + (size_t)(ep_lo & 3u) * WS_SLOT;
    const u64 tag = ((((u64)ep_hi << 32) | ep_lo) + 1) & 0xfffffffffffull;
    u32 tg[3];
    rec_tags(tag, tg);
    await(lds, 0, 8);
    const u32x4 c4 = *(const u32x4*)(lds + L_BC + lane * 16);
    const u32 cown = *(const u32*)(lds + L_BC + t1 * 4);
    u32x2 pd[3], ps[3];
#pragma unroll
    for (int i = 0; i < 3; i++) {
      const u32* qd = (const u32*)(lds + L_POS + ((ed[i] * 6) & ~3u));
      const u32* qs = (const u32*)(lds + L_POS + ((es[i] * 6) & ~3u));
      pd[i] = u32x2{qd[0], qd[1]}; ps[i] = u32x2{qs[0], qs[1]};
    }
    const u32 pown = *(const u16*)(lds + L_POS + min((nb0 * 3 + t1) * 2, MAXPOS - 2));
    const u32 tot = c4[0] + c4[1] + c4[2] + c4[3];
    const u32 ex = scan64(tot) - tot;
    u32* mine = (u32*)(lds + L_LS + u * 1024);
    *(u32x4*)(mine + lane * 4) = u32x4{ex, ex + c4[0], ex + c4[0] + c4[1], ex + c4[0] + c4[1] + c4[2]};
    __builtin_amdgcn_wave_barrier();
    u32 at[3];
#pragma unroll
    for (int i = 0; i < 3; i++) at[i] = L_RS + (mine[ebk[i]] + rank[i]) * 16;
    const u32 gword = (mine[t1] << 10) | cown;
#pragma unroll
    for (int i = 0; i < 3; i++) {
      const u32 shd = (ed[i] & 1u) * 16, shs = (es[i] & 1u) * 16;
      const u32 dxy = __builtin_amdgcn_alignbit(pd[i][1], pd[i][0], shd), sxy = __builtin_amdgcn_alignbit(ps[i][1], ps[i][0], shs);
      const u32 xy = pk_bf16(bf2f(dxy) - bf2f(sxy), bfhi(dxy) - bfhi(sxy));
      const u32 zz = pk_bf16(bf2f(pd[i][1] >> shd) - bf2f(ps[i][1] >> shs), 0.f);
      u32x4 rc = {es[i] | ((ed[i] - ebk[i] * NPB) << 14) | tg[0], ee[i] | tg[1], xy, zz | tg[2]};
      PIN(rc);
      if (t1 + 256 * i < ne) *(u32x4*)(lds + at[i]) = rc;
    }
    if (t1 < NPB * 3) *(u16*)(lds + L_PD + t1 * 2) = (u16)pown;
    arrive(lb, 2);
    await(lds, 2, 4);
    const __amdgpu_buffer_rsrc_t r_rw = rsrc(slot + WS_REC + (size_t)b * CH * 16, CH * 16);
    u32x4 rv[3];
#pragma unroll
    for (int i = 0; i < 3; i++) rv[i] = *(const u32x4*)(lds + L_RS + (t1 + 256 * i) * 16);
#pragma unroll
    for (int i = 0; i < 3; i++) __builtin_amdgcn_raw_buffer_store_b128(rv[i], r_rw, t1 * 16, i * 4096, SC1);
    arrive(lb, 4);
    __hip_atomic_store((gu64*)(slot + WS_GR) + (size_t)b * MAXG + t1, (tag << 20) | gword, RLX_AGENT);
    __builtin_amdgcn_s_setprio(0);
#pragma unroll
    for (int i = 0; i < 10; i++) if (i < 9 || L_SM + i * 4096 + t1 * 16 < L_POS) *(u32x4*)(lds + L_SM + i * 4096 + t1 * 16) = z;
#pragma unroll
    for (int i = 0; i < 5; i++) { const int o = L_POS + i * 4096 + t1 * 16; if (i < 4 || o < L_SRC) *(u32x4*)(lds + o) = z; }
    *(u32x4*)(lds + L_SRC + t1 * 16) = u32x4{OOB, OOB, OOB, OOB};
    if (t1 < 16) *(u32*)(lds + L_SRC + 4096 + t1 * 4) = OOB;
    await(lds, 1, 4);
#pragma unroll
    for (int s = 0; s < 4; s++)
#pragma unroll
      for (int t = 0; t < 3; t++) hb[t][s] = h_frag(lds, t, s, lane);
  }
  __syncthreads();
  ep_lo = RFL(ep_lo); ep_hi = RFL(ep_hi);
  const u64 ep = ((u64)ep_hi << 32) | ep_lo, tag = (ep + 1) & 0xfffffffffffull;
  char* const slot = ws + WS_SLOT0 + (size_t)(ep_lo & 3u) * WS_SLOT;
  const __amdgpu_buffer_rsrc_t r_bt = rsrc(slot + WS_BT, N * 256), r_rec = rsrc(slot + WS_REC, G * CH * 16);
  u32 tg[3];
  rec_tags(tag, tg);
  f32x4 cp[3];
  u32 pw[4] = {0, 0, 0, 0};
  {
    const int cs = wv * 32 + (lane & 31), csc = cs < G ? cs : G - 1;
    gu64* const gp = (gu64*)(slot + WS_GR) + (size_t)csc * MAXG + bk;
    gu64* const fp = (gu64*)(slot + WS_FL) + csc;
    u64 gv = __hip_atomic_load(gp, RLX_AGENT);
    SB();
    {
      f32x4 acc[3] = {f32x4{0, 0, 0, 0}, f32x4{0, 0, 0, 0}, f32x4{0, 0, 0, 0}};
#pragma unroll
      for (int s = 0; s < 4; s++) {
        const u32x4 fw = tr_frag(lds + L_W1, s, lane, 2 * wv + ((lane & 3) >> 1), lane & 1);
#pragma unroll
        for (int t = 0; t < 3; t++) acc[t] = mfma16(hb[t][s], fw, acc[t]);
      }
      char* kb = lds + L_KB + (16 * wv + c16) * 16;
#pragma unroll
      for (int t = 0; t < 2; t++) {
        const f32x4 a = acc[t] * LOG2E;
        *(u32x2*)(kb + (4 * t + g4) * 2048) = u32x2{pk_bf16(a[0], a[1]), pk_bf16(a[2], a[3])};
      }
      if (g4 < 2) {
#pragma unroll
        for (int i = 0; i < 4; i++) *(u16*)(kb + (4 * g4 + i) * 2048 + 8) = (u16)pk_bf16(acc[2][i] * LOG2E, 0.f);
      }
    }
    SB();
    for (u32 spins = 0; !__all((gv >> 20) == tag) && spins < SPINS; spins++) {
      __builtin_amdgcn_s_sleep(4);
      if ((gv >> 20) != tag) gv = __hip_atomic_load(gp, RLX_AGENT);
    }
    const bool on = lane < 32 && cs < G;
    const u32 cnt = on ? ((u32)gv & 1023u) : 0u, st = ((u32)gv >> 10) & 1023u;
    const u32 inc = scan64(cnt);
    const u32 total = __builtin_amdgcn_readlane(inc, 63);
    u32* list = (u32*)(lds + L_LS + wv * 768);
    const u32 base = (cs * CH + st) * 16, p0 = inc - cnt;
    const u32 lim = min(cnt, p0 < 128 ? 128 - p0 : 0u);
#pragma unroll
    for (u32 k = 0; k < 6; k++) list[k < lim ? p0 + k : 128 + lane] = base + k * 16;
    if (__any(lim > 6)) {
#pragma unroll 1
      for (u32 k = 6; k < lim; k++) list[p0 + k] = base + k * 16;
    }
    __builtin_amdgcn_wave_barrier();
    {
      const u32 a0 = list[lane], a1 = list[64 + lane];
      const bool v0 = lane < total, v1 = 64 + lane < total;
      u32x4 r0 = __builtin_amdgcn_raw_buffer_load_b128(r_rec, v0 ? a0 : OOB, 0, SC1);
      u32x4 r1 = __builtin_amdgcn_raw_buffer_load_b128(r_rec, v1 ? a1 : OOB, 0, SC1);
      u64 fv = __hip_atomic_load(fp, RLX_AGENT);
      if (on) *(u32*)(lds + L_BC + cs * 4) = (u32)gv & 0xfffffu;
      SB();
#pragma unroll
      for (int t = 0; t < 3; t++) cp[t] = f32x4{0, 0, 0, 0};
#pragma unroll
      for (int s = 0; s < 4; s++) {
        const u32x4 f1 = tr_frag(lds + L_W0, s, lane, 2 * wv + ((lane & 3) >> 1), lane & 1);
#pragma unroll
        for (int t = 0; t < 3; t++) cp[t] = mfma16(f1, hb[t][s], cp[t]);
      }
#pragma unroll
      for (int t = 0; t < 3; t++) { cp[t] *= LOG2E; PIN(cp[t]); }
      SB();
      r0 = settle(r0, v0, r_rec, a0, tg);
      r1 = settle(r1, v1, r_rec, a1, tg);
      u32 k0 = 0, k1 = 0;
      if (v0) k0 = take_rank(lds, r0);
      if (v1) k1 = take_rank(lds, r1);
      for (u32 spins = 0; !__all(fv == tag) && spins < SPINS; spins++) {
        __builtin_amdgcn_s_sleep(4);
        fv = __hip_atomic_load(fp, RLX_AGENT);
      }
      __builtin_amdgcn_s_waitcnt(0x0f70);
      pw[0] = __builtin_amdgcn_raw_buffer_load_b32(r_bt, v0 && k0 < PFR ? (r0[0] & 0x3fffu) << 8 : OOB, 0, SC1);
      pw[1] = __builtin_amdgcn_raw_buffer_load_b32(r_bt, v0 && k0 < PFR ? ((r0[0] & 0x3fffu) << 8) + 128 : OOB, 0, SC1);
      pw[2] = __builtin_amdgcn_raw_buffer_load_b32(r_bt, v1 && k1 < PFR ? (r1[0] & 0x3fffu) << 8 : OOB, 0, SC1);
      pw[3] = __builtin_amdgcn_raw_buffer_load_b32(r_bt, v1 && k1 < PFR ? ((r1[0] & 0x3fffu) << 8) + 128 : OOB, 0, SC1);
      SB();
      if (v0) put_rec(lds, r0, k0);
      if (v1) put_rec(lds, r1, k1);
    }
    if (__builtin_expect(total > 128, 0)) {
#pragma unroll 1
      for (u32 done = 128; done < total; done += 64) {
        if (__hip_atomic_load((u32*)(lds + L_CNT + 32), __ATOMIC_RELAXED, __HIP_MEMORY_SCOPE_WORKGROUP)) break;
        __builtin_amdgcn_wave_barrier();
#pragma unroll 1
        for (u32 k = p0 < done ? done - p0 : 0u; k < cnt && p0 + k < done + 64; k++) list[p0 + k - done] = base + k * 16;
        __builtin_amdgcn_wave_barrier();
        const bool v0 = done + lane < total;
        const u32 a0 = list[lane];
        u32x4 r0 = __builtin_amdgcn_raw_buffer_load_b128(r_rec, v0 ? a0 : OOB, 0, SC1);
        r0 = settle(r0, v0, r_rec, a0, tg);
        if (v0) put_rec(lds, r0, take_rank(lds, r0));
      }
      __builtin_amdgcn_s_waitcnt(0x0f70);
    }
  }
  __syncthreads();
  const u32* wcnt = (const u32*)(lds + L_CNT);
  const u32 ci = wcnt[lane >> 3], cj = wcnt[lane & 7];
  const int ovf = RFL(wcnt[8]);
  auto load_wh2 = [&]() {
    const u32x4 s_wh = srd(w_h, 65536);
    dma4(s_wh, wv0 + (wv >> 2) * 16384, wv1 + (wv >> 2) * 16384, 32768, lb + L_W0 + wv * 4096);
  };
  u32x4 v[8], wxf[8];
  int win;
  {
    const u64 gt = __ballot(cj * 8 + (lane & 7) > ci * 8 + (lane >> 3));
    const u32 rk = __builtin_popcount((u32)(gt >> (lane & 56)) & 0xffu);
    const int wl = __builtin_ctzll(__ballot(rk == (u32)(wv < 4 ? wv : 11 - wv)));
    win = wl >> 3;
    const int cntw = ovf ? 1 : __builtin_amdgcn_readlane(ci, wl);
    const int nsteps = (cntw + 15) >> 4;
    const u32 lo = r * 4;
    u32 srca = L_SRC + win * CAPW * 4 + hh * 32;
    u32 GA[8], GB[8], off[8];
    const bool cfon = rp == hh;
    u32 cfa = cfon ? (L_CF + (win * CAPW + 8 * rh + rj) * 16) : L_ZERO;
    const u32 cfs = cfon ? 256u : 0u;
    u32 sma = L_SM + (win * CAPW + 8 * hh + (c16 >> 2)) * 40 + (g4 & 1) * 32 + (lane & 3) * 8;
    u32x4 cf, kbA, kbB, sf = {0, 0, 0, 0}, mB0 = {0, 0, 0, 0}, mB1 = {0, 0, 0, 0}, mA0, mA1;
    f32x16 yA, yB;
    {
      const u32x4 i0 = *(const u32x4*)(lds + srca), i1 = *(const u32x4*)(lds + srca + 16);
      const u32x4 i2 = *(const u32x4*)(lds + srca + 64), i3 = *(const u32x4*)(lds + srca + 80);
      cf = *(const u32x4*)(lds + cfa);
      kbA = *(const u32x4*)(lds + L_KB + win * 2048 + (2 * r + hh) * 16);
      kbB = *(const u32x4*)(lds + L_KB + win * 2048 + (64 + 2 * r + hh) * 16);
      srca += 128;
      SB();
#pragma unroll
      for (int j = 0; j < 4; j++) { off[j] = i0[j] + lo; off[4 + j] = i1[j] + lo; }
#pragma unroll
      for (int j = 0; j < 8; j++) GA[j] = __builtin_amdgcn_raw_buffer_load_b32(r_bt, off[j], 0, SC1);
      SB();
#pragma unroll
      for (int j = 0; j < 8; j++) GB[j] = __builtin_amdgcn_raw_buffer_load_b32(r_bt, off[j] + 128, 0, SC1);
      SB();
      asm volatile("" :: "v"(pw[0]), "v"(pw[1]), "v"(pw[2]), "v"(pw[3]));
      yA = mfma32(cf, kbA, zero);
#pragma unroll
      for (int j = 0; j < 4; j++) { off[j] = i2[j] + lo; off[4 + j] = i3[j] + lo; }
      yA = mfma32(sel0, u32x4{GA[0], GA[1], GA[2], GA[3]}, yA);
      yA = mfma32(sel1, u32x4{GA[4], GA[5], GA[6], GA[7]}, yA);
      PIN(yA);
      SB();
#pragma unroll
      for (int j = 0; j < 8; j++) GA[j] = __builtin_amdgcn_raw_buffer_load_b32(r_bt, off[j], 0, SC1);
      SB();
    }
#define MF(d, a, bb, c) do { SB(); d = mfma32(a, bb, c); PIN(d); SB(); } while (0)
#define SA(y, i, j) do { p_ = f32x2{__builtin_amdgcn_exp2f(-y[i]), __builtin_amdgcn_exp2f(-y[(i) + 1])}; \
                         q_ = f32x2{__builtin_amdgcn_exp2f(-y[j]), __builtin_amdgcn_exp2f(-y[(j) + 1])}; SB(); p_ += 1.0f; q_ += 1.0f; SB(); } while (0)
#define SBB(y, i, j, o0, o1) do { p_ = f32x2{__builtin_amdgcn_rcpf(p_[0]), __builtin_amdgcn_rcpf(p_[1])}; \
                         q_ = f32x2{__builtin_amdgcn_rcpf(q_[0]), __builtin_amdgcn_rcpf(q_[1])}; SB(); \
                         p_ *= f32x2{y[i], y[(i) + 1]}; q_ *= f32x2{y[j], y[(j) + 1]}; SB(); \
                         u32 t0_ = pk_bf16(p_[0], p_[1]), t1_ = pk_bf16(q_[0], q_[1]); \
                         asm volatile("" : "+v"(t0_), "+v"(t1_)); o0 = t0_; o1 = t1_; SB(); } while (0)
#define S4(y, i, j, o0, o1) do { SA(y, i, j); SBB(y, i, j, o0, o1); } while (0)
#define LD2(Gx, j, add) do { Gx[j] = __builtin_amdgcn_raw_buffer_load_b32(r_bt, off[j] + (add), 0, SC1); \
                             Gx[(j) + 1] = __builtin_amdgcn_raw_buffer_load_b32(r_bt, off[(j) + 1] + (add), 0, SC1); SB(); } while (0)
    f32x2 p_, q_;
    for (int s = 1; s < nsteps; s++) {
      MF(yB, cf, kbB, zero);
      S4(yA, 0, 2, mA0[0], mA0[1]);
      MF(yB, sel0, (u32x4{GB[0], GB[1], GB[2], GB[3]}), yB);
      cfa += cfs;
      cf = *(const u32x4*)(lds + cfa);
      SA(yA, 4, 6);
      LD2(GB, 0, 128);
      SBB(yA, 4, 6, mA0[2], mA0[3]);
      LD2(GB, 2, 128);
      MF(yB, sel1, (u32x4{GB[4], GB[5], GB[6], GB[7]}), yB);
      SA(yA, 8, 10);
      LD2(GB, 4, 128);
      MF(aB0, mB0, sf, aB0);
      SBB(yA, 8, 10, mA1[0], mA1[1]);
      LD2(GB, 6, 128);
      const u32x4 i0 = *(const u32x4*)(lds + srca), i1 = *(const u32x4*)(lds + srca + 16);
      srca += 64;
      SA(yA, 12, 14);
      MF(aB1, mB1, sf, aB1);
      sf = s_frag(lds + sma);
      sma += 640;
      SBB(yA, 12, 14, mA1[2], mA1[3]);
      MF(yA, cf, kbA, zero);
      S4(yB, 0, 2, mB0[0], mB0[1]);
      MF(yA, sel0, (u32x4{GA[0], GA[1], GA[2], GA[3]}), yA);
#pragma unroll
      for (int j = 0; j < 4; j++) { off[j] = i0[j] + lo; off[4 + j] = i1[j] + lo; }
      SA(yB, 4, 6);
      LD2(GA, 0, 0);
      SBB(yB, 4, 6, mB0[2], mB0[3]);
      LD2(GA, 2, 0);
      MF(yA, sel1, (u32x4{GA[4], GA[5], GA[6], GA[7]}), yA);
      SA(yB, 8, 10);
      LD2(GA, 4, 0);
      MF(aA0, mA0, sf, aA0);
      SBB(yB, 8, 10, mB1[0], mB1[1]);
      LD2(GA, 6, 0);
      SA(yB, 12, 14);
      MF(aA1, mA1, sf, aA1);
      SBB(yB, 12, 14, mB1[2], mB1[3]);
    }
    {
      const int rem = cntw - 16 * (nsteps - 1);
      load_wh2();
      MF(yB, cf, kbB, zero);
      mA0 = u32x4{0, 0, 0, 0}; mA1 = u32x4{0, 0, 0, 0};
      S4(yA, 0, 8, mA0[0], mA1[0]);
      MF(yB, sel0, (u32x4{GB[0], GB[1], GB[2], GB[3]}), yB);
      if (rem > 4) S4(yA, 2, 10, mA0[1], mA1[1]);
      MF(yB, sel1, (u32x4{GB[4], GB[5], GB[6], GB[7]}), yB);
      if (rem > 8) S4(yA, 4, 12, mA0[2], mA1[2]);
      MF(aB0, mB0, sf, aB0);
      if (rem > 12) S4(yA, 6, 14, mA0[3], mA1[3]);
      MF(aB1, mB1, sf, aB1);
      sf = s_frag(lds + sma);
      mB0 = u32x4{0, 0, 0, 0}; mB1 = u32x4{0, 0, 0, 0};
      S4(yB, 0, 8, mB0[0], mB1[0]);
      MF(aA0, mA0, sf, aA0);
      if (rem > 4) S4(yB, 2, 10, mB0[1], mB1[1]);
      MF(aA1, mA1, sf, aA1);
      if (rem > 8) S4(yB, 4, 12, mB0[2], mB1[2]);
      if (rem > 12) S4(yB, 6, 14, mB0[3], mB1[3]);
      MF(aB0, mB0, sf, aB0);
      MF(aB1, mB1, sf, aB1);
    }
#pragma unroll
    for (int q8 = 0; q8 < 8; q8++) {
      const f32x16& a0 = q8 < 4 ? aA0 : aB0;
      const f32x16& a1 = q8 < 4 ? aA1 : aB1;
#pragma unroll
      for (int q = 0; q < 4; q++) v[q8][q] = pk_bf16(a0[4 * (q8 & 3) + q], a1[4 * (q8 & 3) + q]);
    }
    if (r < 20 && (r & 3) == 0 && !ovf) {
      char* mi = lds + mi_row(r * 2 + win) + 16 * hh;
#pragma unroll
      for (int q8 = 0; q8 < 8; q8++) *(u32x4*)(mi + 32 * q8) = v[q8];
    }
  }
  if (__builtin_expect(ovf, 0)) {
    float* macc = (float*)(lds + L_SM);
    float* pacc = (float*)(lds + L_SM + NPB * D * 4);
    __syncthreads();
    for (int i = tid; i < NPB * D + NPB * 4; i += NT) macc[i] = 0.f;
    __syncthreads();
    const u32 wxp = *(const u32*)(lds + L_WX + lane * 4);
    const float wx0 = bf2f(wxp), wx1 = bfhi(wxp);
    const u32x4 kc0 = *(const u32x4*)(lds + L_KB + (2 * lane) * 16), kc1 = *(const u32x4*)(lds + L_KB + (2 * lane + 1) * 16);
    const float wd0 = bf2f(kc0[3]) + bfhi(kc0[3]), wd1 = bf2f(kc1[3]) + bfhi(kc1[3]);
    const float wa0 = bfhi(kc0[2]), wa1 = bfhi(kc1[2]);
    u32 rot = 0;
    for (int c = 0; c < G; c++) {
      const u32 oo = RFL(*(const u32*)(lds + L_BC + c * 4));
      const u32 s0 = oo >> 10, n = oo & 1023u;
      for (u32 k = (wv - rot) & 7u; k < n; k += 8) {
        u32x4 rr = __builtin_amdgcn_raw_buffer_load_b128(r_rec, (c * CH + s0 + k) * 16, 0, SC1);
        rr = settle(rr, true, r_rec, (c * CH + s0 + k) * 16, tg);
        const u32 nl = (rr[0] >> 14) & 0x3fu, sl = nl >> 3, wn = nl & 7u;
        const u32 bv = __builtin_amdgcn_raw_buffer_load_b32(r_bt, ((rr[0] & 0x3fffu) << 8) + lane * 4, 0, SC1);
        const float d2 = bfhi(dist2(rr[2], rr[3])), eav = bf2f(rr[1]);
        const float a0 = bf2f(*(const u16*)(lds + L_KB + wn * 2048 + (2 * lane) * 16 + sl * 2));
        const float a1 = bf2f(*(const u16*)(lds + L_KB + wn * 2048 + (2 * lane + 1) * 16 + sl * 2));
        const float y0 = a0 + bf2f(bv) + d2 * wd0 + eav * wa0, y1 = a1 + bfhi(bv) + d2 * wd1 + eav * wa1;
        const u32 mp = silu2(y0, y1);
        const float m0 = bf2f(mp), m1 = bfhi(mp);
        atomicAdd(&macc[nl * D + 2 * lane], m0);
        atomicAdd(&macc[nl * D + 2 * lane + 1], m1);
        float csv = m0 * wx0 + m1 * wx1;
#pragma unroll
        for (int o = 32; o > 0; o >>= 1) csv += __shfl_xor(csv, o, 64);
        if (lane < 3) atomicAdd(&pacc[nl * 4 + lane], csv * (lane == 0 ? bf2f(rr[2]) : lane == 1 ? bfhi(rr[2]) : bf2f(rr[3])));
      }
      rot += n;
    }
    __syncthreads();
    for (int i = tid; i < NPB * D / 2; i += NT) {
      const int n = i >> 6, f = (i & 63) * 2;
      *(u32*)(lds + mi_row(n) + f * 2) = pk_bf16(macc[n * D + f], macc[n * D + f + 1]);
    }
    if (tid < NPB * 3) {
      const int n = tid / 3, c = tid - n * 3;
      if (nb0 + n < N) out[(size_t)N * D + (size_t)nb0 * 3 + tid] = (u16)pk_bf16(bf2f(*(const u16*)(lds + L_PD + tid * 2)) + LN2 * pacc[n * 4 + c], 0.f);
    }
  }
  u32x2 hr[3];
  u32x4 f2[4];
#pragma unroll
  for (int t = 0; t < 3; t++) {
    const int n = tile_node(t, c16);
    hr[t] = *(const u32x2*)(lds + L_HS + n * 256 + 16 * ((2 * wv + (g4 >> 1)) ^ hswz(n)) + 8 * (g4 & 1));
  }
#pragma unroll
  for (int q8 = 0; q8 < 8; q8++) wxf[q8] = *(const u32x4*)(lds + L_WX + (16 * q8 + 8 * hh) * 2);
  const int kind = lane & 3, pnl = (lane >> 2) * 8 + win;
  const bool pon = kind != 0 && lane < 20 && !ovf;
  const u32 p0 = *(const u16*)(lds + L_PD + (pon ? pnl * 6 + kind * 2 - 2 : 0));
  const int mia = mi_row(tile_node(0, c16)) + g4 * 16, mib = mi_row(tile_node(2, c16)) + g4 * 16;
  asm volatile("s_waitcnt vmcnt(0)" ::: "memory");
  __syncthreads();
  {
    u32x4 mb[3][4];
#pragma unroll
    for (int s = 0; s < 4; s++) {
      f2[s] = tr_frag(lds + L_W0, s, lane, 2 * wv + ((lane & 3) >> 1), lane & 1);
      mb[0][s] = *(const u32x4*)(lds + mia + s * 64);
      mb[1][s] = *(const u32x4*)(lds + mia + 8192 + s * 64);
      mb[2][s] = *(const u32x4*)(lds + mib + s * 64);
    }
    SB();
#pragma unroll
    for (int s = 0; s < 4; s++)
#pragma unroll
      for (int t = 0; t < 3; t++) cp[t] = mfma16(f2[s], mb[t][s], cp[t]);
    f32x16 z = zero, z1 = zero;
#pragma unroll
    for (int t = 0; t < 3; t++) {
      const f32x4 y = cp[t];
      f32x4 e;
#pragma unroll
      for (int i = 0; i < 4; i++) e[i] = __builtin_amdgcn_exp2f(-y[i]);
      SB();
      if (t & 1) { z1 = mfma32(wxf[3 * t], v[3 * t], z1); PIN(z1); } else { z = mfma32(wxf[3 * t], v[3 * t], z); PIN(z); }
      SB();
      e += 1.0f;
#pragma unroll
      for (int i = 0; i < 4; i++) e[i] = __builtin_amdgcn_rcpf(e[i]);
      SB();
      if (t & 1) { z = mfma32(wxf[3 * t + 1], v[3 * t + 1], z); PIN(z); } else { z1 = mfma32(wxf[3 * t + 1], v[3 * t + 1], z1); PIN(z1); }
      SB();
      const f32x4 o = f32x4{bf2f(hr[t][0]), bfhi(hr[t][0]), bf2f(hr[t][1]), bfhi(hr[t][1])} + (y * LN2) * e;
      *(u32x2*)(lds + L_SM + tile_node(t, c16) * 272 + wv * 32 + g4 * 8) = u32x2{pk_bf16(o[0], o[1]), pk_bf16(o[2], o[3])};
      SB();
      if (t == 0) { z = mfma32(wxf[2], v[2], z); PIN(z); }
      if (t == 1) { z1 = mfma32(wxf[5], v[5], z1); PIN(z1); }
      SB();
    }
    __syncthreads();
    const __amdgpu_buffer_rsrc_t r_out = rsrc(out, N * 262);
    const u32x4 o0 = *(const u32x4*)(lds + L_SM + (tid >> 4) * 272 + (tid & 15) * 16);
    const u32x4 o1 = *(const u32x4*)(lds + L_SM + (32 + ((tid >> 4) & 7)) * 272 + (tid & 15) * 16);
    const u32 oo = nb0 * 256 + tid * 16;
    __builtin_amdgcn_raw_buffer_store_b128(o0, r_out, oo < N * 256 ? oo : OOB, 0, 0);
    __builtin_amdgcn_raw_buffer_store_b128(o1, r_out, tid < 128 && oo + 8192 < N * 256 ? oo + 8192 : OOB, 0, 0);
    __builtin_amdgcn_raw_buffer_store_b16((u16)pk_bf16(bf2f(p0) + LN2 * (z[0] + z1[0]), 0.f), r_out,
                                          pon ? N * 256 + ((nb0 + pnl) * 3 + kind - 1) * 2 : OOB, 0, 0);
  }
  if (b == 0 && tid == 0) __hip_atomic_store((gu64*)(ws + WS_EP), ep + 1, RLX_AGENT);
}

__global__ __launch_bounds__(128) void k_edge_any(const u16* h, const u16* pos, const u16* ea, const int* src, const int* dst,
                                                  const u16* w_e, const u16* w_x, float* acc, int N) {
  __shared__ float x[2 * D + 2], red[2];
  const int e = blockIdx.x, f = threadIdx.x, s = src[e], d = dst[e];
  const u32 xy = pk_bf16(bf2f(pos[d * 3]) - bf2f(pos[s * 3]), bf2f(pos[d * 3 + 1]) - bf2f(pos[s * 3 + 1]));
  const u32 zz = pk_bf16(bf2f(pos[d * 3 + 2]) - bf2f(pos[s * 3 + 2]), 0.f);
  x[f] = bf2f(h[(size_t)d * D + f]); x[D + f] = bf2f(h[(size_t)s * D + f]);
  if (f == 0) { x[2 * D] = bfhi(dist2(xy, zz)); x[2 * D + 1] = bf2f(ea[e]); }
  __syncthreads();
  float y = 0.f;
  for (int k = 0; k < 2 * D + 2; k++) y += x[k] * bf2f(w_e[k * D + f]);
  const float m = LN2 * bf2f(silu2(y * LOG2E, 0.f));
  atomicAdd(&acc[(size_t)d * D + f], m);
  float c = m * bf2f(w_x[f]);
  for (int o = 32; o > 0; o >>= 1) c += __shfl_xor(c, o, 64);
  if ((f & 63) == 0) red[f >> 6] = c;
  __syncthreads();
  if (f < 3) atomicAdd(&acc[(size_t)N * D + d * 3 + f], (red[0] + red[1]) * (f == 0 ? bf2f(xy) : f == 1 ? bfhi(xy) : bf2f(zz)));
}
__global__ __launch_bounds__(128) void k_node_any(const u16* h, const u16* pos, const u16* w_h, const float* acc, u16* out, int N) {
  __shared__ float x[2 * D];
  const int n = blockIdx.x, f = threadIdx.x;
  x[f] = bf2f(h[(size_t)n * D + f]); x[D + f] = bf2f(pk_bf16(acc[(size_t)n * D + f], 0.f));
  __syncthreads();
  float y = 0.f;
  for (int k = 0; k < 2 * D; k++) y += x[k] * bf2f(w_h[k * D + f]);
  out[(size_t)n * D + f] = (u16)pk_bf16(x[f] + y * __builtin_amdgcn_rcpf(1.f + __builtin_amdgcn_exp2f(-y * LOG2E)), 0.f);
  if (f < 3) out[(size_t)N * D + n * 3 + f] = (u16)pk_bf16(bf2f(pos[n * 3 + f]) + acc[(size_t)N * D + n * 3 + f], 0.f);
}

extern "C" void kernel_launch(void* const* d_in, const int* in_sizes, int n_in, void* d_out, int out_size, void* d_ws,
                              size_t ws_size, hipStream_t stream) {
  const u16 *h = (const u16*)d_in[0], *pos = (const u16*)d_in[1], *ea = (const u16*)d_in[2];
  const int *src = (const int*)d_in[3], *dst = (const int*)d_in[4];
  const u16 *w_e = (const u16*)d_in[5], *w_x = (const u16*)d_in[6], *w_h = (const u16*)d_in[7];
  const int N = in_sizes[0] / D, E = in_sizes[3];
  const int NB = (N + NPB - 1) / NPB, C = (E + CH - 1) / CH;
  const int G = NB > C ? NB : C;
  if (G <= MAXG && N * 6 <= MAXPOS && (N & 7) == 0 && ws_size >= WS_SLOT0 + 4 * WS_SLOT) {
    k_egnn<<<G, NT, 0, stream>>>(h, pos, ea, src, dst, w_e, w_x, w_h, (char*)d_ws, (u16*)d_out, N, E, G);
  } else {
    (void)hipMemsetAsync(d_ws, 0, (size_t)N * (D + 3) * sizeof(float), stream);
    if (E > 0) k_edge_any<<<E, 128, 0, stream>>>(h, pos, ea, src, dst, w_e, w_x, (float*)d_ws, N);
    k_node_any<<<N, 128, 0, stream>>>(h, pos, w_h, (const float*)d_ws, (u16*)d_out, N);
  }
}
```

```cpp
#include <hip/hip_runtime.h>
#include <hip/hip_bf16.h>

typedef unsigned int u32;
typedef unsigned short u16;
typedef unsigned long long u64;
typedef u32 u32x2 __attribute__((ext_vector_type(2)));
typedef u32 u32x4 __attribute__((ext_vector_type(4)));
typedef short s16x4 __attribute__((ext_vector_type(4)));
typedef short bf16x8 __attribute__((ext_vector_type(8)));
typedef float f32x2 __attribute__((ext_vector_type(2)));
typedef float f32x4 __attribute__((ext_vector_type(4)));
typedef float f32x16 __attribute__((ext_vector_type(16)));
typedef __bf16 bf16x2_t __attribute__((ext_vector_type(2)));
typedef __attribute__((address_space(1))) u64 gu64;
typedef __attribute__((address_space(3))) s16x4 lds_s16x4;
typedef __attribute__((address_space(3))) char lds_char;
#define DI __device__ __forceinline__
#define RLX_AGENT __ATOMIC_RELAXED, __HIP_MEMORY_SCOPE_AGENT
#define SC1 16
#define SB() __builtin_amdgcn_sched_barrier(0)
#define PIN(x) asm volatile("" : "+v"(x))
#define RFL(x) __builtin_amdgcn_readfirstlane(x)

constexpr int D = 128;
constexpr int NT = 512;
constexpr int NPB = 40;
constexpr int CH = 640;
constexpr int MAXG = 256;
constexpr int CAPW = 128;
constexpr int MAXPOS = 61440;
constexpr u32 PFR = 16;
constexpr u32 SPINS = 1u << 16;
constexpr float LOG2E = 1.4426950408889634f;
constexpr float LN2 = 0.6931471805599453f;
constexpr u32 OOB = 0xfffff000u;

constexpr size_t WS_EP = 0;
constexpr size_t WS_SLOT0 = (size_t)2 << 20, WS_SLOT = (size_t)6 << 20;
constexpr size_t WS_GR = 0;
constexpr size_t WS_BT = WS_GR + (size_t)MAXG * MAXG * 8;
constexpr size_t WS_REC = WS_BT + (size_t)MAXG * NPB * 256;
constexpr size_t WS_FL = WS_REC + (size_t)MAXG * CH * 16;
static_assert(WS_FL + MAXG * 8 <= WS_SLOT, "slot");

DI float bf2f(u32 b) { return __builtin_bit_cast(float, b << 16); }
DI float bfhi(u32 b) { return __builtin_bit_cast(float, b & 0xffff0000u); }
DI u32 pk_bf16(float lo, float hi) {
  f32x2 v = {lo, hi};
  bf16x2_t r = __builtin_convertvector(v, bf16x2_t);
  return __builtin_bit_cast(u32, r);
}
DI f32x4 mfma16(u32x4 a, u32x4 b, f32x4 c) {
  return __builtin_amdgcn_mfma_f32_16x16x32_bf16(__builtin_bit_cast(bf16x8, a), __builtin_bit_cast(bf16x8, b), c, 0, 0, 0);
}
DI f32x16 mfma32(u32x4 a, u32x4 b, f32x16 c) {
  return __builtin_amdgcn_mfma_f32_32x32x16_bf16(__builtin_bit_cast(bf16x8, a), __builtin_bit_cast(bf16x8, b), c, 0, 0, 0);
}
DI __amdgpu_buffer_rsrc_t rsrc(const void* p, u32 bytes) { return __builtin_amdgcn_make_buffer_rsrc((void*)p, 0, bytes, 0x00020000); }
DI u32x4 srd(const void* p, u32 bytes) { const u64 a = (u64)p; return u32x4{(u32)a, (u32)(a >> 32) & 0xffffu, bytes, 0x00020000u}; }
DI void dma4(u32x4 sd, u32 v0, u32 v1, u32 soff, u32 ldsa) {
  u32 keep;
  asm volatile("s_mov_b32 %0, m0\n\ts_mov_b32 m0, %1\n\ts_nop 0\n\t"
               "buffer_load_dwordx4 %2, %4, %5 offen lds\n\tbuffer_load_dwordx4 %2, %4, %5 offen offset:1024 lds\n\t"
               "buffer_load_dwordx4 %3, %4, %5 offen offset:2048 lds\n\tbuffer_load_dwordx4 %3, %4, %5 offen offset:3072 lds\n\t"
               "s_mov_b32 m0, %0"
               : "=&s"(keep) : "s"(RFL(ldsa)), "v"(v0), "v"(v1), "s"(sd), "s"(soff) : "memory");
}
DI void dma2(u32x4 sd, u32 v0, u32 soff, u32 ldsa) {
  u32 keep;
  asm volatile("s_mov_b32 %0, m0\n\ts_mov_b32 m0, %1\n\ts_nop 0\n\t"
               "buffer_load_dwordx4 %2, %3, %4 offen lds\n\tbuffer_load_dwordx4 %2, %3, %4 offen offset:1024 lds\n\t"
               "s_mov_b32 m0, %0"
               : "=&s"(keep) : "s"(RFL(ldsa)), "v"(v0), "s"(sd), "s"(soff) : "memory");
}
DI u32 scan64(u32 x) {
  x += __builtin_amdgcn_update_dpp(0u, x, 0x111, 0xf, 0xf, false);
  x += __builtin_amdgcn_update_dpp(0u, x, 0x112, 0xf, 0xf, false);
  x += __builtin_amdgcn_update_dpp(0u, x, 0x114, 0xf, 0xf, false);
  x += __builtin_amdgcn_update_dpp(0u, x, 0x118, 0xf, 0xf, false);
  x += __builtin_amdgcn_update_dpp(0u, x, 0x142, 0xa, 0xf, false);
  x += __builtin_amdgcn_update_dpp(0u, x, 0x143, 0xc, 0xf, false);
  return x;
}

constexpr int L_ZERO = 0;
constexpr int L_CNT = L_ZERO + 16;
constexpr int L_BC = L_CNT + 48;
constexpr int L_SY = L_BC + 1024;
constexpr int L_SM = L_SY + 32;
constexpr int L_CF = L_SM + 8 * 8 * 640;
constexpr int L_SRC = L_CF + 8 * CAPW * 16;
constexpr int L_KB = L_SRC + 8 * CAPW * 4 + 64;
constexpr int L_W1 = L_KB + 8 * D * 16;
constexpr int L_W0 = L_W1 + 32768;
constexpr int L_HS = L_W0 + 32768;
constexpr int L_WX = L_HS + NPB * 256;
constexpr int L_PD = L_WX + 256;
constexpr int L_LS = L_PD + 256;
constexpr int L_END = L_LS + 8 * 768;
constexpr int L_RS = L_W0 - CH * 16;
constexpr int L_POS = L_RS - MAXPOS;
static_assert(L_END <= 160 * 1024, "lds");
static_assert(L_POS >= L_SM + 36864 && L_POS <= L_CF && (L_POS % 16) == 0, "pos table");
static_assert((L_SM % 16) == 0 && (L_BC % 16) == 0 && (L_SRC % 16) == 0 && (L_KB % 16) == 0 && (L_W0 % 16) == 0 && (L_HS % 16) == 0 && (L_WX % 16) == 0 && (L_LS % 16) == 0, "align");

DI int tile_node(int t, int c) { return t < 2 ? ((c & 3) * 8 + 4 * t + (c >> 2)) : (32 + (c & 7)); }
DI int mi_row(int nl) { return L_KB + (nl & 7) * 2048 + (nl & 3) * 64 + (nl >> 3) * 272; }
DI int wswz(int k) { return ((k & 3) << 2) | ((k >> 2) & 2); }
DI int hswz(int n) { return ((n & 3) << 2) | ((n >> 3) & 3); }
DI u32x4 tr_frag(const char* img, int s, int lane, int ch, int hf) {
  const int k0 = 8 * (lane >> 4) + ((lane & 15) >> 2);
  const char* p = img + (32 * s + k0) * 256 + 16 * (ch ^ wswz(k0)) + 8 * hf;
  const s16x4 a = __builtin_amdgcn_ds_read_tr16_b64_v4i16((lds_s16x4*)p);
  const s16x4 b = __builtin_amdgcn_ds_read_tr16_b64_v4i16((lds_s16x4*)(p + 1024));
  const u32x2 x = __builtin_bit_cast(u32x2, a), y = __builtin_bit_cast(u32x2, b);
  return u32x4{x[0], x[1], y[0], y[1]};
}
DI u32x4 s_frag(const char* p) {
  const u32x2 x = __builtin_bit_cast(u32x2, __builtin_amdgcn_ds_read_tr16_b64_v4i16((lds_s16x4*)p));
  const u32x2 y = __builtin_bit_cast(u32x2, __builtin_amdgcn_ds_read_tr16_b64_v4i16((lds_s16x4*)(p + 160)));
  return u32x4{x[0], x[1], y[0], y[1]};
}
DI u32x4 h_frag(const char* lds, int t, int s, int lane) {
  const int n = tile_node(t, lane & 15);
  return *(const u32x4*)(lds + L_HS + n * 256 + 16 * ((4 * s + (lane >> 4)) ^ hswz(n)));
}
DI u32 dist2(u32 w2, u32 w3) {
  const float dx = bf2f(w2), dy = bfhi(w2), dz = bf2f(w3);
  const u32 q0 = pk_bf16(dx * dx, dy * dy), q1 = pk_bf16(dz * dz, 0.f);
  const float s2 = bf2f(q0) + bfhi(q0) + bf2f(q1);
  return pk_bf16(s2, s2);
}
DI u32 take_rank(char* lds, u32x4 rc) { return atomicAdd((u32*)(lds + L_CNT) + ((rc[0] >> 14) & 7u), 1u); }
DI void put_rec(char* lds, u32x4 rc, u32 rank) {
  const u32 win = (rc[0] >> 14) & 7u, slot = (rc[0] >> 17) & 7u;
  const u64 oh = 0x3f80ull << (slot * 16);
  const u32x4 cf = {slot < 4 ? (u32)oh : 0u, slot < 4 ? (u32)(oh >> 32) : 0u, (slot == 4 ? 0x3f80u : 0u) | (rc[1] << 16), dist2(rc[2], rc[3])};
  if (rank < CAPW) {
    const u32 k = ((rank & 1u) << 3) + ((rank & 15u) >> 1), sp = win * CAPW + (rank & ~15u) + k;
    *(u32*)(lds + L_SRC + sp * 4) = (rc[0] & 0x3fffu) << 8;
    *(u32x4*)(lds + L_CF + sp * 16) = cf;
    *(u32x2*)(lds + L_SM + sp * 40 + slot * 8) = u32x2{0x3f80u | (rc[2] << 16), __builtin_amdgcn_alignbit(rc[3], rc[2], 16)};
  } else *(u32*)(lds + L_CNT + 32) = 1;
}
DI void rec_tags(u64 tag, u32 (&t)[3]) {
  t[0] = (u32)tag << 20; t[1] = ((u32)tag >> 12) << 16; t[2] = ((u32)(tag >> 28) ^ (u32)tag) << 16;
}
DI bool rec_stale(u32x4 rc, const u32 (&t)[3]) { return (((rc[0] ^ t[0]) >> 20) | (((rc[1] ^ t[1]) | (rc[3] ^ t[2])) >> 16)) != 0; }
DI u32x4 settle(u32x4 rc, bool on, __amdgpu_buffer_rsrc_t r, u32 off, const u32 (&t)[3]) {
  for (u32 spins = 0; __any(on && rec_stale(rc, t)) && spins < SPINS; spins++) {
    __builtin_amdgcn_s_sleep(4);
    if (on && rec_stale(rc, t)) rc = __builtin_amdgcn_raw_buffer_load_b128(r, off, 0, SC1);
  }
  return rc;
}
DI void arrive(u32 lb, int i) {
  asm volatile("s_mov_b64 exec, 1\n\tds_add_u32 %0, %1 offset:%2\n\ts_mov_b64 exec, -1" :: "v"(lb), "v"(1u), "i"(L_SY + 4 * i) : "memory");
}
DI void await(char* lds, int i, u32 n) {
  asm volatile("" ::: "memory");
  while ((u32)RFL(__hip_atomic_load((u32*)(lds + L_SY) + i, __ATOMIC_RELAXED, __HIP_MEMORY_SCOPE_WORKGROUP)) < n) { }
  asm volatile("" ::: "memory");
}
DI u32 silu2(float a, float b) {
  f32x2 yy = {a, b};
  f32x2 e = {__builtin_amdgcn_exp2f(-a), __builtin_amdgcn_exp2f(-b)};
  e += 1.0f;
  f32x2 rc = {__builtin_amdgcn_rcpf(e[0]), __builtin_amdgcn_rcpf(e[1])};
  yy *= rc;
  return pk_bf16(yy[0], yy[1]);
}

__global__ __launch_bounds__(NT, 2) void k_egnn(const u16* __restrict__ h, const u16* __restrict__ pos,
                                                const u16* __restrict__ ea, const int* __restrict__ src,
                                                const int* __restrict__ dst, const u16* __restrict__ w_e,
                                                const u16* __restrict__ w_x, const u16* __restrict__ w_h,
                                                char* __restrict__ ws, u16* __restrict__ out, int N, int E, int G) {
  __shared__ __attribute__((aligned(16))) char lds[L_END];
  const int tid = threadIdx.x, lane = tid & 63, b = blockIdx.x;
  const int wv = RFL(tid >> 6);
  const int u = wv & 3, c16 = lane & 15, g4 = lane >> 4;
  const bool role1 = wv >= 4;
  const u32 lb = (u32)(size_t)(lds_char*)lds;
  if (tid < L_SM / 4) *(u32*)(lds + tid * 4) = 0;
  __syncthreads();
  const int ne = RFL(max(0, min(CH, E - b * CH)));
  u32 ed[3], es[3], ee[3];
  u32x2 epoch;
  {
    const u32x4 s_d = srd(dst + b * CH, ne * 4), s_s = srd(src + b * CH, ne * 4), s_e = srd(ea + b * CH, ne * 2);
    asm volatile("s_cmp_lt_u32 %15, 4\n\ts_cbranch_scc1 1f\n\t"
                 "buffer_load_dword %0, %10, %12, 0 offen\n\tbuffer_load_dword %1, %10, %12, 0 offen offset:1024\n\t"
                 "buffer_load_dword %2, %10, %12, 0 offen offset:2048\n\tbuffer_load_dword %3, %10, %13, 0 offen\n\t"
                 "buffer_load_dword %4, %10, %13, 0 offen offset:1024\n\tbuffer_load_dword %5, %10, %13, 0 offen offset:2048\n\t"
                 "buffer_load_ushort %6, %11, %14, 0 offen\n\tbuffer_load_ushort %7, %11, %14, 0 offen offset:512\n\t"
                 "buffer_load_ushort %8, %11, %14, 0 offen offset:1024\n"
                 "1:\n\tglobal_load_dwordx2 %9, %16, %17 sc1"
                 : "=&v"(ed[0]), "=&v"(ed[1]), "=&v"(ed[2]), "=&v"(es[0]), "=&v"(es[1]), "=&v"(es[2]), "=&v"(ee[0]), "=&v"(ee[1]), "=&v"(ee[2]), "=&v"(epoch)
                 : "v"((tid & 255) * 4), "v"((tid & 255) * 2), "s"(s_d), "s"(s_s), "s"(s_e), "s"(wv), "v"(0u), "s"(ws + WS_EP) : "memory", "scc");
  }
  int bk;
  { const int q = G >> 3, r = G & 7, x = b & 7, i = b >> 3;
    bk = (x < r ? x * (q + 1) : r * (q + 1) + (x - r) * q) + i; }
  const int nb0 = bk * NPB;
  const int r = lane & 31, hh = lane >> 5;
  const int rh = (r >> 2) & 1, rp = r >> 4, rj = (r & 3) | (((r >> 3) & 1) << 2);
  f32x16 aA0, aA1, aB0, aB1, zero;
  u32x4 sel0, sel1;
  auto setup = [&]() {
#pragma unroll
    for (int i = 0; i < 16; i++) zero[i] = 0.f;
    u32x4 z4 = {0, 0, 0, 0};
    PIN(z4);
    aA0 = mfma32(z4, z4, zero); aA1 = mfma32(z4, z4, zero); aB0 = mfma32(z4, z4, zero); aB1 = mfma32(z4, z4, zero);
    const u32 one = rh == hh ? 0x3f80u << (rp * 16) : 0u;
#pragma unroll
    for (int q = 0; q < 4; q++) { sel0[q] = (rj == q) ? one : 0u; sel1[q] = (rj == 4 + q) ? one : 0u; }
    PIN(aA0); PIN(aA1); PIN(aB0); PIN(aB1); PIN(sel0); PIN(sel1);
  };
  u32 ep_lo, ep_hi;
  u32x4 hb[3][4];
  const u32 wv0 = g4 * 256 + 16 * (c16 ^ (g4 << 2)) + u * 4096, wv1 = wv0 ^ 32;
  if (!role1) {
    const u32x4 s_pos = srd(pos, N * 6), s_h = srd(h, N * 256), s_we = srd(w_e, 258 * 256), s_wh = srd(w_h, 65536);
#pragma unroll
    for (int i = 0; i < 4; i++)
      if (i < 3 || u < 3) dma4(s_pos, lane * 16 + u * 4096, lane * 16 + u * 4096, i * 16384, lb + L_POS + i * 16384 + u * 4096);
    dma2(s_h, g4 * 256 + 16 * (c16 ^ ((g4 << 2) | u)) + u * 2048, nb0 * 256, lb + L_HS + u * 2048);
    if (u == 0) dma2(s_h, g4 * 256 + 16 * (c16 ^ (g4 << 2)) + 8192, nb0 * 256, lb + L_HS + 8192);
    dma4(s_we, wv0, wv1, 32768, lb + L_W0 + u * 4096);
    dma4(s_we, wv0, wv1, 32768 + 16384, lb + L_W0 + 16384 + u * 4096);
    u32 wdv, wav, wxv, pf1, pf2;
    {
      const u32x4 s_wx = srd(w_x, 256);
      const u32 xi = (b >> 3) & 31;
      asm volatile("buffer_load_ushort %0, %5, %6, %7 offen\n\tbuffer_load_ushort %1, %5, %6, %7 offen offset:256\n\t"
                   "buffer_load_ushort %2, %5, %8, 0 offen\n\tbuffer_load_dword %3, %9, %6, 0 offen\n\t"
                   "buffer_load_dword %4, %10, %11, 0 offen"
                   : "=&v"(wdv), "=&v"(wav), "=&v"(wxv), "=&v"(pf1), "=&v"(pf2)
                   : "v"((tid & 127) * 2), "s"(s_we), "s"(65536u), "s"(s_wx), "v"(tid < 8 ? (xi * 8 + tid) * 128 : OOB),
                     "v"(tid - 64 < 16u ? (xi * 16 + tid - 64) * 128 : OOB), "s"(s_wh) : "memory");
    }
    asm volatile("s_waitcnt vmcnt(15)\n\tv_readfirstlane_b32 %0, %2\n\tv_readfirstlane_b32 %1, %3\n\ts_nop 1"
                 : "=s"(ep_lo), "=s"(ep_hi) : "v"(epoch[0]), "v"(epoch[1]) : "memory");
    arrive(lb, 0);
    setup();
    asm volatile("s_waitcnt vmcnt(5)" ::: "memory");
    arrive(lb, 1);
    char* const slot = ws + WS_SLOT0 + (size_t)(RFL(ep_lo) & 3u) * WS_SLOT;
    await(lds, 1, 4);
#pragma unroll
    for (int s = 0; s < 4; s++)
#pragma unroll
      for (int t = 0; t < 3; t++) hb[t][s] = h_frag(lds, t, s, lane);
    f32x4 acc[3][2];
#pragma unroll
    for (int t = 0; t < 3; t++) { acc[t][0] = f32x4{0, 0, 0, 0}; acc[t][1] = f32x4{0, 0, 0, 0}; }
#pragma unroll
    for (int s = 0; s < 4; s++) {
      const u32x4 fa = tr_frag(lds + L_W0, s, lane, 4 * u + (lane & 3), 0), fb = tr_frag(lds + L_W0, s, lane, 4 * u + (lane & 3), 1);
#pragma unroll
      for (int t = 0; t < 3; t++) { acc[t][0] = mfma16(fa, hb[t][s], acc[t][0]); acc[t][1] = mfma16(fb, hb[t][s], acc[t][1]); }
    }
    asm volatile("s_waitcnt vmcnt(0)" : "+v"(wdv), "+v"(wav), "+v"(wxv), "+v"(pf1), "+v"(pf2), "+v"(acc[2][1]) :: "memory");
    arrive(lb, 3);
    const __amdgpu_buffer_rsrc_t r_bw = rsrc(slot + WS_BT, N * 256);
#pragma unroll
    for (int t = 0; t < 3; t++) {
      const f32x4 x = acc[t][0] * LOG2E, y = acc[t][1] * LOG2E;
      const u32x4 o = {pk_bf16(x[0], x[1]), pk_bf16(x[2], x[3]), pk_bf16(y[0], y[1]), pk_bf16(y[2], y[3])};
      __builtin_amdgcn_raw_buffer_store_b128(o, r_bw, (t < 2 || c16 < 8) ? (nb0 + tile_node(t, c16)) * 256 + u * 64 + g4 * 16 : OOB, 0, SC1);
    }
    await(lds, 3, 4);
    dma4(s_wh, wv0, wv1, 0, lb + L_W0 + u * 4096);
    dma4(s_wh, wv0, wv1, 16384, lb + L_W0 + 16384 + u * 4096);
    await(lds, 4, 4);
    dma4(s_we, wv0, wv1, 0, lb + L_W1 + u * 4096);
    dma4(s_we, wv0, wv1, 16384, lb + L_W1 + 16384 + u * 4096);
    {
      const float wd = bf2f(wdv) * LOG2E;
      const u32 wdh = pk_bf16(wd, 0.f);
      const u32 wdp = wdh | (pk_bf16(wd - bf2f(wdh), 0.f) << 16), wap = pk_bf16(bf2f(wav) * LOG2E, 0.f);
#pragma unroll
      for (int i = 0; i < 4; i++) {
        char* kb = lds + L_KB + (i * 256 + tid) * 16;
        *(u32*)(kb + 12) = wdp;
        *(u16*)(kb + 10) = (u16)wap;
      }
      if (tid < D) *(u16*)(lds + L_WX + tid * 2) = (u16)wxv;
    }
    asm volatile("s_waitcnt vmcnt(0)" ::: "memory");
    if (lane == 0 && __hip_atomic_fetch_add((u32*)(lds + L_SY) + 5, 1u, __ATOMIC_RELAXED, __HIP_MEMORY_SCOPE_WORKGROUP) == 3)
      __hip_atomic_store((gu64*)(slot + WS_FL) + b, ((((u64)ep_hi << 32) | ep_lo) + 1) & 0xfffffffffffull, RLX_AGENT);
  } else {
    const int t1 = tid - 256;
    __builtin_amdgcn_s_setprio(2);
    const u32x4 z = {0, 0, 0, 0};
    setup();
    asm volatile("s_waitcnt vmcnt(7)" : "+v"(ed[0]), "+v"(ed[1]), "+v"(ed[2]) :: "memory");
    u32 rank[3], ebk[3];
#pragma unroll
    for (int i = 0; i < 3; i++) {
      ebk[i] = ed[i] / NPB;
      rank[i] = 0;
      if (t1 + 256 * i < ne) rank[i] = atomicAdd((u32*)(lds + L_BC) + ebk[i], 1u);
    }
    arrive(lb, 0);
    asm volatile("s_waitcnt vmcnt(0)\n\tv_readfirstlane_b32 %0, %8\n\tv_readfirstlane_b32 %1, %9\n\ts_nop 1"
                 : "=s"(ep_lo), "=s"(ep_hi), "+v"(es[0]), "+v"(es[1]), "+v"(es[2]), "+v"(ee[0]), "+v"(ee[1]), "+v"(ee[2])
                 : "v"(epoch[0]), "v"(epoch[1]) : "memory");
    ep_lo = RFL(ep_lo); ep_hi = RFL(ep_hi);
    char* const slot = ws + WS_SLOT0 + (size_t)(ep_lo & 3u) * WS_SLOT;
    const u64 tag = ((((u64)ep_hi << 32) | ep_lo) + 1) & 0xfffffffffffull;
    u32 tg[3];
    rec_tags(tag, tg);
    await(lds, 0, 8);
    const u32x4 c4 = *(const u32x4*)(lds + L_BC + lane * 16);
    const u32 cown = *(const u32*)(lds + L_BC + t1 * 4);
    u32x2 pd[3], ps[3];
#pragma unroll
    for (int i = 0; i < 3; i++) {
      const u32* qd = (const u32*)(lds + L_POS + ((ed[i] * 6) & ~3u));
      const u32* qs = (const u32*)(lds + L_POS + ((es[i] * 6) & ~3u));
      pd[i] = u32x2{qd[0], qd[1]}; ps[i] = u32x2{qs[0], qs[1]};
    }
    const u32 pown = *(const u16*)(lds + L_POS + min((nb0 * 3 + t1) * 2, MAXPOS - 2));
    const u32 tot = c4[0] + c4[1] + c4[2] + c4[3];
    const u32 ex = scan64(tot) - tot;
    u32* mine = (u32*)(lds + L_LS + u * 1024);
    *(u32x4*)(mine + lane * 4) = u32x4{ex, ex + c4[0], ex + c4[0] + c4[1], ex + c4[0] + c4[1] + c4[2]};
    __builtin_amdgcn_wave_barrier();
    u32 at[3];
#pragma unroll
    for (int i = 0; i < 3; i++) at[i] = L_RS + (mine[ebk[i]] + rank[i]) * 16;
    const u32 gword = (mine[t1] << 10) | cown;
#pragma unroll
    for (int i = 0; i < 3; i++) {
      const u32 shd = (ed[i] & 1u) * 16, shs = (es[i] & 1u) * 16;
      const u32 dxy = __builtin_amdgcn_alignbit(pd[i][1], pd[i][0], shd), sxy = __builtin_amdgcn_alignbit(ps[i][1], ps[i][0], shs);
      const u32 xy = pk_bf16(bf2f(dxy) - bf2f(sxy), bfhi(dxy) - bfhi(sxy));
      const u32 zz = pk_bf16(bf2f(pd[i][1] >> shd) - bf2f(ps[i][1] >> shs), 0.f);
      u32x4 rc = {es[i] | ((ed[i] - ebk[i] * NPB) << 14) | tg[0], ee[i] | tg[1], xy, zz | tg[2]};
      PIN(rc);
      if (t1 + 256 * i < ne) *(u32x4*)(lds + at[i]) = rc;
    }
    if (t1 < NPB * 3) *(u16*)(lds + L_PD + t1 * 2) = (u16)pown;
    arrive(lb, 2);
    await(lds, 2, 4);
    const __amdgpu_buffer_rsrc_t r_rw = rsrc(slot + WS_REC + (size_t)b * CH * 16, CH * 16);
    u32x4 rv[3];
#pragma unroll
    for (int i = 0; i < 3; i++) rv[i] = *(const u32x4*)(lds + L_RS + (t1 + 256 * i) * 16);
#pragma unroll
    for (int i = 0; i < 3; i++) __builtin_amdgcn_raw_buffer_store_b128(rv[i], r_rw, t1 * 16, i * 4096, SC1);
    arrive(lb, 4);
    __hip_atomic_store((gu64*)(slot + WS_GR) + (size_t)b * MAXG + t1, (tag << 20) | gword, RLX_AGENT);
    __builtin_amdgcn_s_setprio(0);
#pragma unroll
    for (int i = 0; i < 10; i++) if (i < 9 || L_SM + i * 4096 + t1 * 16 < L_POS) *(u32x4*)(lds + L_SM + i * 4096 + t1 * 16) = z;
#pragma unroll
    for (int i = 0; i < 5; i++) { const int o = L_POS + i * 4096 + t1 * 16; if (i < 4 || o < L_SRC) *(u32x4*)(lds + o) = z; }
    *(u32x4*)(lds + L_SRC + t1 * 16) = u32x4{OOB, OOB, OOB, OOB};
    if (t1 < 16) *(u32*)(lds + L_SRC + 4096 + t1 * 4) = OOB;
    await(lds, 1, 4);
#pragma unroll
    for (int s = 0; s < 4; s++)
#pragma unroll
      for (int t = 0; t < 3; t++) hb[t][s] = h_frag(lds, t, s, lane);
  }
  __syncthreads();
  ep_lo = RFL(ep_lo); ep_hi = RFL(ep_hi);
  const u64 ep = ((u64)ep_hi << 32) | ep_lo, tag = (ep + 1) & 0xfffffffffffull;
  char* const slot = ws + WS_SLOT0 + (size_t)(ep_lo & 3u) * WS_SLOT;
  const __amdgpu_buffer_rsrc_t r_bt = rsrc(slot + WS_BT, N * 256), r_rec = rsrc(slot + WS_REC, G * CH * 16);
  u32 tg[3];
  rec_tags(tag, tg);
  f32x4 cp[3];
  u32 pw[4] = {0, 0, 0, 0};
  {
    const int cs = wv * 32 + (lane & 31), csc = cs < G ? cs : G - 1;
    gu64* const gp = (gu64*)(slot + WS_GR) + (size_t)csc * MAXG + bk;
    gu64* const fp = (gu64*)(slot + WS_FL) + csc;
    u64 gv = __hip_atomic_load(gp, RLX_AGENT);
    SB();
    {
      f32x4 acc[3] = {f32x4{0, 0, 0, 0}, f32x4{0, 0, 0, 0}, f32x4{0, 0, 0, 0}};
#pragma unroll
      for (int s = 0; s < 4; s++) {
        const u32x4 fw = tr_frag(lds + L_W1, s, lane, 2 * wv + ((lane & 3) >> 1), lane & 1);
#pragma unroll
        for (int t = 0; t < 3; t++) acc[t] = mfma16(hb[t][s], fw, acc[t]);
      }
      char* kb = lds + L_KB + (16 * wv + c16) * 16;
#pragma unroll
      for (int t = 0; t < 2; t++) {
        const f32x4 a = acc[t] * LOG2E;
        *(u32x2*)(kb + (4 * t + g4) * 2048) = u32x2{pk_bf16(a[0], a[1]), pk_bf16(a[2], a[3])};
      }
      if (g4 < 2) {
#pragma unroll
        for (int i = 0; i < 4; i++) *(u16*)(kb + (4 * g4 + i) * 2048 + 8) = (u16)pk_bf16(acc[2][i] * LOG2E, 0.f);
      }
    }
    SB();
    for (u32 spins = 0; !__all((gv >> 20) == tag) && spins < SPINS; spins++) {
      __builtin_amdgcn_s_sleep(4);
      if ((gv >> 20) != tag) gv = __hip_atomic_load(gp, RLX_AGENT);
    }
    const bool on = lane < 32 && cs < G;
    const u32 cnt = on ? ((u32)gv & 1023u) : 0u, st = ((u32)gv >> 10) & 1023u;
    const u32 inc = scan64(cnt);
    const u32 total = __builtin_amdgcn_readlane(inc, 63);
    u32* list = (u32*)(lds + L_LS + wv * 768);
    const u32 base = (cs * CH + st) * 16, p0 = inc - cnt;
    const u32 lim = min(cnt, p0 < 128 ? 128 - p0 : 0u);
#pragma unroll
    for (u32 k = 0; k < 6; k++) list[k < lim ? p0 + k : 128 + lane] = base + k * 16;
    if (__any(lim > 6)) {
#pragma unroll 1
      for (u32 k = 6; k < lim; k++) list[p0 + k] = base + k * 16;
    }
    __builtin_amdgcn_wave_barrier();
    {
      const u32 a0 = list[lane], a1 = list[64 + lane];
      const bool v0 = lane < total, v1 = 64 + lane < total;
      u32x4 r0 = __builtin_amdgcn_raw_buffer_load_b128(r_rec, v0 ? a0 : OOB, 0, SC1);
      u32x4 r1 = __builtin_amdgcn_raw_buffer_load_b128(r_rec, v1 ? a1 : OOB, 0, SC1);
      u64 fv = __hip_atomic_load(fp, RLX_AGENT);
      if (on) *(u32*)(lds + L_BC + cs * 4) = (u32)gv & 0xfffffu;
      SB();
#pragma unroll
      for (int t = 0; t < 3; t++) cp[t] = f32x4{0, 0, 0, 0};
#pragma unroll
      for (int s = 0; s < 4; s++) {
        const u32x4 f1 = tr_frag(lds + L_W0, s, lane, 2 * wv + ((lane & 3) >> 1), lane & 1);
#pragma unroll
        for (int t = 0; t < 3; t++) cp[t] = mfma16(f1, hb[t][s], cp[t]);
      }
#pragma unroll
      for (int t = 0; t < 3; t++) { cp[t] *= LOG2E; PIN(cp[t]); }
      SB();
      r0 = settle(r0, v0, r_rec, a0, tg);
      r1 = settle(r1, v1, r_rec, a1, tg);
      u32 k0 = 0, k1 = 0;
      if (v0) k0 = take_rank(lds, r0);
      if (v1) k1 = take_rank(lds, r1);
      for (u32 spins = 0; !__all(fv == tag) && spins < SPINS; spins++) {
        __builtin_amdgcn_s_sleep(4);
        fv = __hip_atomic_load(fp, RLX_AGENT);
      }
      __builtin_amdgcn_s_waitcnt(0x0f70);
      pw[0] = __builtin_amdgcn_raw_buffer_load_b32(r_bt, v0 && k0 < PFR ? (r0[0] & 0x3fffu) << 8 : OOB, 0, SC1);
      pw[1] = __builtin_amdgcn_raw_buffer_load_b32(r_bt, v0 && k0 < PFR ? ((r0[0] & 0x3fffu) << 8) + 128 : OOB, 0, SC1);
      pw[2] = __builtin_amdgcn_raw_buffer_load_b32(r_bt, v1 && k1 < PFR ? (r1[0] & 0x3fffu) << 8 : OOB, 0, SC1);
      pw[3] = __builtin_amdgcn_raw_buffer_load_b32(r_bt, v1 && k1 < PFR ? ((r1[0] & 0x3fffu) << 8) + 128 : OOB, 0, SC1);
      SB();
      if (v0) put_rec(lds, r0, k0);
      if (v1) put_rec(lds, r1, k1);
    }
    if (__builtin_expect(total > 128, 0)) {
#pragma unroll 1
      for (u32 done = 128; done < total; done += 64) {
        if (__hip_atomic_load((u32*)(lds + L_CNT + 32), __ATOMIC_RELAXED, __HIP_MEMORY_SCOPE_WORKGROUP)) break;
        __builtin_amdgcn_wave_barrier();
#pragma unroll 1
        for (u32 k = p0 < done ? done - p0 : 0u; k < cnt && p0 + k < done + 64; k++) list[p0 + k - done] = base + k * 16;
        __builtin_amdgcn_wave_barrier();
        const bool v0 = done + lane < total;
        const u32 a0 = list[lane];
        u32x4 r0 = __builtin_amdgcn_raw_buffer_load_b128(r_rec, v0 ? a0 : OOB, 0, SC1);
        r0 = settle(r0, v0, r_rec, a0, tg);
        if (v0) put_rec(lds, r0, take_rank(lds, r0));
      }
      __builtin_amdgcn_s_waitcnt(0x0f70);
    }
  }
  __syncthreads();
  const u32* wcnt = (const u32*)(lds + L_CNT);
  const u32 ci = wcnt[lane >> 3], cj = wcnt[lane & 7];
  const int ovf = RFL(wcnt[8]);
  auto load_wh2 = [&]() {
    const u32x4 s_wh = srd(w_h, 65536);
    dma4(s_wh, wv0 + (wv >> 2) * 16384, wv1 + (wv >> 2) * 16384, 32768, lb + L_W0 + wv * 4096);
  };
  u32x4 v[8], wxf[8];
  int win;
  {
    const u64 gt = __ballot(cj * 8 + (lane & 7) > ci * 8 + (lane >> 3));
    const u32 rk = __builtin_popcount((u32)(gt >> (lane & 56)) & 0xffu);
    const int wl = __builtin_ctzll(__ballot(rk == (u32)(wv < 4 ? wv : 11 - wv)));
    win = wl >> 3;
    const int cntw = ovf ? 1 : __builtin_amdgcn_readlane(ci, wl);
    const int nsteps = (cntw + 15) >> 4;
    const u32 lo = r * 4;
    u32 srca = L_SRC + win * CAPW * 4 + hh * 32;
    u32 GA[8], GB[8], off[8];
    const bool cfon = rp == hh;
    u32 cfa = cfon ? (L_CF + (win * CAPW + 8 * rh + rj) * 16) : L_ZERO;
    const u32 cfs = cfon ? 256u : 0u;
    u32 sma = L_SM + (win * CAPW + 8 * hh + (c16 >> 2)) * 40 + (g4 & 1) * 32 + (lane & 3) * 8;
    u32x4 cf, kbA, kbB, sf = {0, 0, 0, 0}, mB0 = {0, 0, 0, 0}, mB1 = {0, 0, 0, 0}, mA0, mA1;
    f32x16 yA, yB;
    {
      const u32x4 i0 = *(const u32x4*)(lds + srca), i1 = *(const u32x4*)(lds + srca + 16);
      const u32x4 i2 = *(const u32x4*)(lds + srca + 64), i3 = *(const u32x4*)(lds + srca + 80);
      cf = *(const u32x4*)(lds + cfa);
      kbA = *(const u32x4*)(lds + L_KB + win * 2048 + (2 * r + hh) * 16);
      kbB = *(const u32x4*)(lds + L_KB + win * 2048 + (64 + 2 * r + hh) * 16);
      srca += 128;
      SB();
#pragma unroll
      for (int j = 0; j < 4; j++) { off[j] = i0[j] + lo; off[4 + j] = i1[j] + lo; }
#pragma unroll
      for (int j = 0; j < 8; j++) GA[j] = __builtin_amdgcn_raw_buffer_load_b32(r_bt, off[j], 0, SC1);
      SB();
#pragma unroll
      for (int j = 0; j < 8; j++) GB[j] = __builtin_amdgcn_raw_buffer_load_b32(r_bt, off[j] + 128, 0, SC1);
      SB();
      asm volatile("" :: "v"(pw[0]), "v"(pw[1]), "v"(pw[2]), "v"(pw[3]));
      yA = mfma32(cf, kbA, zero);
#pragma unroll
      for (int j = 0; j < 4; j++) { off[j] = i2[j] + lo; off[4 + j] = i3[j] + lo; }
      yA = mfma32(sel0, u32x4{GA[0], GA[1], GA[2], GA[3]}, yA);
      yA = mfma32(sel1, u32x4{GA[4], GA[5], GA[6], GA[7]}, yA);
      PIN(yA);
      SB();
#pragma unroll
      for (int j = 0; j < 8; j++) GA[j] = __builtin_amdgcn_raw_buffer_load_b32(r_bt, off[j], 0, SC1);
      SB();
    }
#define MF(d, a, bb, c) do { SB(); d = mfma32(a, bb, c); PIN(d); SB(); } while (0)
#define SA(y, i, j) do { p_ = f32x2{__builtin_amdgcn_exp2f(-y[i]), __builtin_amdgcn_exp2f(-y[(i) + 1])}; \
                         q_ = f32x2{__builtin_amdgcn_exp2f(-y[j]), __builtin_amdgcn_exp2f(-y[(j) + 1])}; SB(); p_ += 1.0f; q_ += 1.0f; SB(); } while (0)
#define SBB(y, i, j, o0, o1) do { p_ = f32x2{__builtin_amdgcn_rcpf(p_[0]), __builtin_amdgcn_rcpf(p_[1])}; \
                         q_ = f32x2{__builtin_amdgcn_rcpf(q_[0]), __builtin_amdgcn_rcpf(q_[1])}; SB(); \
                         p_ *= f32x2{y[i], y[(i) + 1]}; q_ *= f32x2{y[j], y[(j) + 1]}; SB(); \
                         u32 t0_ = pk_bf16(p_[0], p_[1]), t1_ = pk_bf16(q_[0], q_[1]); \
                         asm volatile("" : "+v"(t0_), "+v"(t1_)); o0 = t0_; o1 = t1_; SB(); } while (0)
#define S4(y, i, j, o0, o1) do { SA(y, i, j); SBB(y, i, j, o0, o1); } while (0)
#define LD2(Gx, j, add) do { Gx[j] = __builtin_amdgcn_raw_buffer_load_b32(r_bt, off[j] + (add), 0, SC1); \
                             Gx[(j) + 1] = __builtin_amdgcn_raw_buffer_load_b32(r_bt, off[(j) + 1] + (add), 0, SC1); SB(); } while (0)
    f32x2 p_, q_;
    for (int s = 1; s < nsteps; s++) {
      { const int left = nsteps - s; if (left >= 4) __builtin_amdgcn_s_setprio(3); else if (left == 3) __builtin_amdgcn_s_setprio(2); else if (left == 2) __builtin_amdgcn_s_setprio(1); else __builtin_amdgcn_s_setprio(0); }
      MF(yB, cf, kbB, zero);
      S4(yA, 0, 2, mA0[0], mA0[1]);
      MF(yB, sel0, (u32x4{GB[0], GB[1], GB[2], GB[3]}), yB);
      cfa += cfs;
      cf = *(const u32x4*)(lds + cfa);
      SA(yA, 4, 6);
      LD2(GB, 0, 128);
      SBB(yA, 4, 6, mA0[2], mA0[3]);
      LD2(GB, 2, 128);
      MF(yB, sel1, (u32x4{GB[4], GB[5], GB[6], GB[7]}), yB);
      SA(yA, 8, 10);
      LD2(GB, 4, 128);
      MF(aB0, mB0, sf, aB0);
      SBB(yA, 8, 10, mA1[0], mA1[1]);
      LD2(GB, 6, 128);
      const u32x4 i0 = *(const u32x4*)(lds + srca), i1 = *(const u32x4*)(lds + srca + 16);
      srca += 64;
      SA(yA, 12, 14);
      MF(aB1, mB1, sf, aB1);
      sf = s_frag(lds + sma);
      sma += 640;
      SBB(yA, 12, 14, mA1[2], mA1[3]);
      MF(yA, cf, kbA, zero);
      S4(yB, 0, 2, mB0[0], mB0[1]);
      MF(yA, sel0, (u32x4{GA[0], GA[1], GA[2], GA[3]}), yA);
#pragma unroll
      for (int j = 0; j < 4; j++) { off[j] = i0[j] + lo; off[4 + j] = i1[j] + lo; }
      SA(yB, 4, 6);
      LD2(GA, 0, 0);
      SBB(yB, 4, 6, mB0[2], mB0[3]);
      LD2(GA, 2, 0);
      MF(yA, sel1, (u32x4{GA[4], GA[5], GA[6], GA[7]}), yA);
      SA(yB, 8, 10);
      LD2(GA, 4, 0);
      MF(aA0, mA0, sf, aA0);
      SBB(yB, 8, 10, mB1[0], mB1[1]);
      LD2(GA, 6, 0);
      SA(yB, 12, 14);
      MF(aA1, mA1, sf, aA1);
      SBB(yB, 12, 14, mB1[2], mB1[3]);
    }
    {
      const int rem = cntw - 16 * (nsteps - 1);
      load_wh2();
      MF(yB, cf, kbB, zero);
      mA0 = u32x4{0, 0, 0, 0}; mA1 = u32x4{0, 0, 0, 0};
      S4(yA, 0, 8, mA0[0], mA1[0]);
      MF(yB, sel0, (u32x4{GB[0], GB[1], GB[2], GB[3]}), yB);
      if (rem > 4) S4(yA, 2, 10, mA0[1], mA1[1]);
      MF(yB, sel1, (u32x4{GB[4], GB[5], GB[6], GB[7]}), yB);
      if (rem > 8) S4(yA, 4, 12, mA0[2], mA1[2]);
      MF(aB0, mB0, sf, aB0);
      if (rem > 12) S4(yA, 6, 14, mA0[3], mA1[3]);
      MF(aB1, mB1, sf, aB1);
      sf = s_frag(lds + sma);
      mB0 = u32x4{0, 0, 0, 0}; mB1 = u32x4{0, 0, 0, 0};
      S4(yB, 0, 8, mB0[0], mB1[0]);
      MF(aA0, mA0, sf, aA0);
      if (rem > 4) S4(yB, 2, 10, mB0[1], mB1[1]);
      MF(aA1, mA1, sf, aA1);
      if (rem > 8) S4(yB, 4, 12, mB0[2], mB1[2]);
      if (rem > 12) S4(yB, 6, 14, mB0[3], mB1[3]);
      MF(aB0, mB0, sf, aB0);
      MF(aB1, mB1, sf, aB1);
    }
#pragma unroll
    for (int q8 = 0; q8 < 8; q8++) {
      const f32x16& a0 = q8 < 4 ? aA0 : aB0;
      const f32x16& a1 = q8 < 4 ? aA1 : aB1;
#pragma unroll
      for (int q = 0; q < 4; q++) v[q8][q] = pk_bf16(a0[4 * (q8 & 3) + q], a1[4 * (q8 & 3) + q]);
    }
    if (r < 20 && (r & 3) == 0 && !ovf) {
      char* mi = lds + mi_row(r * 2 + win) + 16 * hh;
#pragma unroll
      for (int q8 = 0; q8 < 8; q8++) *(u32x4*)(mi + 32 * q8) = v[q8];
    }
  }
  if (__builtin_expect(ovf, 0)) {
    float* macc = (float*)(lds + L_SM);
    float* pacc = (float*)(lds + L_SM + NPB * D * 4);
    __syncthreads();
    for (int i = tid; i < NPB * D + NPB * 4; i += NT) macc[i] = 0.f;
    __syncthreads();
    const u32 wxp = *(const u32*)(lds + L_WX + lane * 4);
    const float wx0 = bf2f(wxp), wx1 = bfhi(wxp);
    const u32x4 kc0 = *(const u32x4*)(lds + L_KB + (2 * lane) * 16), kc1 = *(const u32x4*)(lds + L_KB + (2 * lane + 1) * 16);
    const float wd0 = bf2f(kc0[3]) + bfhi(kc0[3]), wd1 = bf2f(kc1[3]) + bfhi(kc1[3]);
    const float wa0 = bfhi(kc0[2]), wa1 = bfhi(kc1[2]);
    u32 rot = 0;
    for (int c = 0; c < G; c++) {
      const u32 oo = RFL(*(const u32*)(lds + L_BC + c * 4));
      const u32 s0 = oo >> 10, n = oo & 1023u;
      for (u32 k = (wv - rot) & 7u; k < n; k += 8) {
        u32x4 rr = __builtin_amdgcn_raw_buffer_load_b128(r_rec, (c * CH + s0 + k) * 16, 0, SC1);
        rr = settle(rr, true, r_rec, (c * CH + s0 + k) * 16, tg);
        const u32 nl = (rr[0] >> 14) & 0x3fu, sl = nl >> 3, wn = nl & 7u;
        const u32 bv = __builtin_amdgcn_raw_buffer_load_b32(r_bt, ((rr[0] & 0x3fffu) << 8) + lane * 4, 0, SC1);
        const float d2 = bfhi(dist2(rr[2], rr[3])), eav = bf2f(rr[1]);
        const float a0 = bf2f(*(const u16*)(lds + L_KB + wn * 2048 + (2 * lane) * 16 + sl * 2));
        const float a1 = bf2f(*(const u16*)(lds + L_KB + wn * 2048 + (2 * lane + 1) * 16 + sl * 2));
        const float y0 = a0 + bf2f(bv) + d2 * wd0 + eav * wa0, y1 = a1 + bfhi(bv) + d2 * wd1 + eav * wa1;
        const u32 mp = silu2(y0, y1);
        const float m0 = bf2f(mp), m1 = bfhi(mp);
        atomicAdd(&macc[nl * D + 2 * lane], m0);
        atomicAdd(&macc[nl * D + 2 * lane + 1], m1);
        float csv = m0 * wx0 + m1 * wx1;
#pragma unroll
        for (int o = 32; o > 0; o >>= 1) csv += __shfl_xor(csv, o, 64);
        if (lane < 3) atomicAdd(&pacc[nl * 4 + lane], csv * (lane == 0 ? bf2f(rr[2]) : lane == 1 ? bfhi(rr[2]) : bf2f(rr[3])));
      }
      rot += n;
    }
    __syncthreads();
    for (int i = tid; i < NPB * D / 2; i += NT) {
      const int n = i >> 6, f = (i & 63) * 2;
      *(u32*)(lds + mi_row(n) + f * 2) = pk_bf16(macc[n * D + f], macc[n * D + f + 1]);
    }
    if (tid < NPB * 3) {
      const int n = tid / 3, c = tid - n * 3;
      if (nb0 + n < N) out[(size_t)N * D + (size_t)nb0 * 3 + tid] = (u16)pk_bf16(bf2f(*(const u16*)(lds + L_PD + tid * 2)) + LN2 * pacc[n * 4 + c], 0.f);
    }
  }
  u32x2 hr[3];
  u32x4 f2[4];
#pragma unroll
  for (int t = 0; t < 3; t++) {
    const int n = tile_node(t, c16);
    hr[t] = *(const u32x2*)(lds + L_HS + n * 256 + 16 * ((2 * wv + (g4 >> 1)) ^ hswz(n)) + 8 * (g4 & 1));
  }
#pragma unroll
  for (int q8 = 0; q8 < 8; q8++) wxf[q8] = *(const u32x4*)(lds + L_WX + (16 * q8 + 8 * hh) * 2);
  const int kind = lane & 3, pnl = (lane >> 2) * 8 + win;
  const bool pon = kind != 0 && lane < 20 && !ovf;
  const u32 p0 = *(const u16*)(lds + L_PD + (pon ? pnl * 6 + kind * 2 - 2 : 0));
  const int mia = mi_row(tile_node(0, c16)) + g4 * 16, mib = mi_row(tile_node(2, c16)) + g4 * 16;
  asm volatile("s_waitcnt vmcnt(0)" ::: "memory");
  __syncthreads();
  {
    u32x4 mb[3][4];
#pragma unroll
    for (int s = 0; s < 4; s++) {
      f2[s] = tr_frag(lds + L_W0, s, lane, 2 * wv + ((lane & 3) >> 1), lane & 1);
      mb[0][s] = *(const u32x4*)(lds + mia + s * 64);
      mb[1][s] = *(const u32x4*)(lds + mia + 8192 + s * 64);
      mb[2][s] = *(const u32x4*)(lds + mib + s * 64);
    }
    SB();
#pragma unroll
    for (int s = 0; s < 4; s++)
#pragma unroll
      for (int t = 0; t < 3; t++) cp[t] = mfma16(f2[s], mb[t][s], cp[t]);
    f32x16 z = zero, z1 = zero;
#pragma unroll
    for (int t = 0; t < 3; t++) {
      const f32x4 y = cp[t];
      f32x4 e;
#pragma unroll
      for (int i = 0; i < 4; i++) e[i] = __builtin_amdgcn_exp2f(-y[i]);
      SB();
      if (t & 1) { z1 = mfma32(wxf[3 * t], v[3 * t], z1); PIN(z1); } else { z = mfma32(wxf[3 * t], v[3 * t], z); PIN(z); }
      SB();
      e += 1.0f;
#pragma unroll
      for (int i = 0; i < 4; i++) e[i] = __builtin_amdgcn_rcpf(e[i]);
      SB();
      if (t & 1) { z = mfma32(wxf[3 * t + 1], v[3 * t + 1], z); PIN(z); } else { z1 = mfma32(wxf[3 * t + 1], v[3 * t + 1], z1); PIN(z1); }
      SB();
      const f32x4 o = f32x4{bf2f(hr[t][0]), bfhi(hr[t][0]), bf2f(hr[t][1]), bfhi(hr[t][1])} + (y * LN2) * e;
      *(u32x2*)(lds + L_SM + tile_node(t, c16) * 272 + wv * 32 + g4 * 8) = u32x2{pk_bf16(o[0], o[1]), pk_bf16(o[2], o[3])};
      SB();
      if (t == 0) { z = mfma32(wxf[2], v[2], z); PIN(z); }
      if (t == 1) { z1 = mfma32(wxf[5], v[5], z1); PIN(z1); }
      SB();
    }
    __syncthreads();
    const __amdgpu_buffer_rsrc_t r_out = rsrc(out, N * 262);
    const u32x4 o0 = *(const u32x4*)(lds + L_SM + (tid >> 4) * 272 + (tid & 15) * 16);
    const u32x4 o1 = *(const u32x4*)(lds + L_SM + (32 + ((tid >> 4) & 7)) * 272 + (tid & 15) * 16);
    const u32 oo = nb0 * 256 + tid * 16;
    __builtin_amdgcn_raw_buffer_store_b128(o0, r_out, oo < N * 256 ? oo : OOB, 0, 0);
    __builtin_amdgcn_raw_buffer_store_b128(o1, r_out, tid < 128 && oo + 8192 < N * 256 ? oo + 8192 : OOB, 0, 0);
    __builtin_amdgcn_raw_buffer_store_b16((u16)pk_bf16(bf2f(p0) + LN2 * (z[0] + z1[0]), 0.f), r_out,
                                          pon ? N * 256 + ((nb0 + pnl) * 3 + kind - 1) * 2 : OOB, 0, 0);
  }
  if (b == 0 && tid == 0) __hip_atomic_store((gu64*)(ws + WS_EP), ep + 1, RLX_AGENT);
}

__global__ __launch_bounds__(128) void k_edge_any(const u16* h, const u16* pos, const u16* ea, const int* src, const int* dst,
                                                  const u16* w_e, const u16* w_x, float* acc, int N) {
  __shared__ float x[2 * D + 2], red[2];
  const int e = blockIdx.x, f = threadIdx.x, s = src[e], d = dst[e];
  const u32 xy = pk_bf16(bf2f(pos[d * 3]) - bf2f(pos[s * 3]), bf2f(pos[d * 3 + 1]) - bf2f(pos[s * 3 + 1]));
  const u32 zz = pk_bf16(bf2f(pos[d * 3 + 2]) - bf2f(pos[s * 3 + 2]), 0.f);
  x[f] = bf2f(h[(size_t)d * D + f]); x[D + f] = bf2f(h[(size_t)s * D + f]);
  if (f == 0) { x[2 * D] = bfhi(dist2(xy, zz)); x[2 * D + 1] = bf2f(ea[e]); }
  __syncthreads();
  float y = 0.f;
  for (int k = 0; k < 2 * D + 2; k++) y += x[k] * bf2f(w_e[k * D + f]);
  const float m = LN2 * bf2f(silu2(y * LOG2E, 0.f));
  atomicAdd(&acc[(size_t)d * D + f], m);
  float c = m * bf2f(w_x[f]);
  for (int o = 32; o > 0; o >>= 1) c += __shfl_xor(c, o, 64);
  if ((f & 63) == 0) red[f >> 6] = c;
  __syncthreads();
  if (f < 3) atomicAdd(&acc[(size_t)N * D + d * 3 + f], (red[0] + red[1]) * (f == 0 ? bf2f(xy) : f == 1 ? bfhi(xy) : bf2f(zz)));
}
__global__ __launch_bounds__(128) void k_node_any(const u16* h, const u16* pos, const u16* w_h, const float* acc, u16* out, int N) {
  __shared__ float x[2 * D];
  const int n = blockIdx.x, f = threadIdx.x;
  x[f] = bf2f(h[(size_t)n * D + f]); x[D + f] = bf2f(pk_bf16(acc[(size_t)n * D + f], 0.f));
  __syncthreads();
  float y = 0.f;
  for (int k = 0; k < 2 * D; k++) y += x[k] * bf2f(w_h[k * D + f]);
  out[(size_t)n * D + f] = (u16)pk_bf16(x[f] + y * __builtin_amdgcn_rcpf(1.f + __builtin_amdgcn_exp2f(-y * LOG2E)), 0.f);
  if (f < 3) out[(size_t)N * D + n * 3 + f] = (u16)pk_bf16(bf2f(pos[n * 3 + f]) + acc[(size_t)N * D + n * 3 + f], 0.f);
}

extern "C" void kernel_launch(void* const* d_in, const int* in_sizes, int n_in, void* d_out, int out_size, void* d_ws,
                              size_t ws_size, hipStream_t stream) {
  const u16 *h = (const u16*)d_in[0], *pos = (const u16*)d_in[1], *ea = (const u16*)d_in[2];
  const int *src = (const int*)d_in[3], *dst = (const int*)d_in[4];
  const u16 *w_e = (const u16*)d_in[5], *w_x = (const u16*)d_in[6], *w_h = (const u16*)d_in[7];
  const int N = in_sizes[0] / D, E = in_sizes[3];
  const int NB = (N + NPB - 1) / NPB, C = (E + CH - 1) / CH;
  const int G = NB > C ? NB : C;
  if (G <= MAXG && N * 6 <= MAXPOS && (N & 7) == 0 && ws_size >= WS_SLOT0 + 4 * WS_SLOT) {
    k_egnn<<<G, NT, 0, stream>>>(h, pos, ea, src, dst, w_e, w_x, w_h, (char*)d_ws, (u16*)d_out, N, E, G);
  } else {
    (void)hipMemsetAsync(d_ws, 0, (size_t)N * (D + 3) * sizeof(float), stream);
    if (E > 0) k_edge_any<<<E, 128, 0, stream>>>(h, pos, ea, src, dst, w_e, w_x, (float*)d_ws, N);
    k_node_any<<<N, 128, 0, stream>>>(h, pos, w_h, (const float*)d_ws, (u16*)d_out, N);
  }
}
```

```cpp
#include <hip/hip_runtime.h>
#include <hip/hip_bf16.h>

typedef unsigned int u32;
typedef unsigned short u16;
typedef unsigned long long u64;
typedef u32 u32x2 __attribute__((ext_vector_type(2)));
typedef u32 u32x4 __attribute__((ext_vector_type(4)));
typedef short s16x4 __attribute__((ext_vector_type(4)));
typedef short bf16x8 __attribute__((ext_vector_type(8)));
typedef float f32x2 __attribute__((ext_vector_type(2)));
typedef float f32x4 __attribute__((ext_vector_type(4)));
typedef float f32x16 __attribute__((ext_vector_type(16)));
typedef __bf16 bf16x2_t __attribute__((ext_vector_type(2)));
typedef __attribute__((address_space(1))) u64 gu64;
typedef __attribute__((address_space(3))) s16x4 lds_s16x4;
typedef __attribute__((address_space(3))) char lds_char;
#define DI __device__ __forceinline__
#define RLX_AGENT __ATOMIC_RELAXED, __HIP_MEMORY_SCOPE_AGENT
#define SC1 16
#define SB() __builtin_amdgcn_sched_barrier(0)
#define PIN(x) asm volatile("" : "+v"(x))
#define RFL(x) __builtin_amdgcn_readfirstlane(x)

constexpr int D = 128;
constexpr int NT = 512;
constexpr int NPB = 40;
constexpr int CH = 640;
constexpr int MAXG = 256;
constexpr int CAPW = 128;
constexpr int MAXPOS = 61440;
constexpr u32 PFR = 16;
constexpr u32 SPINS = 1u << 16;
constexpr float LOG2E = 1.4426950408889634f;
constexpr float LN2 = 0.6931471805599453f;
constexpr u32 OOB = 0xfffff000u;

constexpr size_t WS_EP = 0;
constexpr size_t WS_SLOT0 = (size_t)2 << 20, WS_SLOT = (size_t)6 << 20;
constexpr size_t WS_GR = 0;
constexpr size_t WS_BT = WS_GR + (size_t)MAXG * MAXG * 8;
constexpr size_t WS_REC = WS_BT + (size_t)MAXG * NPB * 256;
constexpr size_t WS_FL = WS_REC + (size_t)MAXG * CH * 16;
static_assert(WS_FL + MAXG * 8 <= WS_SLOT, "slot");

DI float bf2f(u32 b) { return __builtin_bit_cast(float, b << 16); }
DI float bfhi(u32 b) { return __builtin_bit_cast(float, b & 0xffff0000u); }
DI u32 pk_bf16(float lo, float hi) {
  f32x2 v = {lo, hi};
  bf16x2_t r = __builtin_convertvector(v, bf16x2_t);
  return __builtin_bit_cast(u32, r);
}
DI f32x4 mfma16(u32x4 a, u32x4 b, f32x4 c) {
  return __builtin_amdgcn_mfma_f32_16x16x32_bf16(__builtin_bit_cast(bf16x8, a), __builtin_bit_cast(bf16x8, b), c, 0, 0, 0);
}
DI f32x16 mfma32(u32x4 a, u32x4 b, f32x16 c) {
  return __builtin_amdgcn_mfma_f32_32x32x16_bf16(__builtin_bit_cast(bf16x8, a), __builtin_bit_cast(bf16x8, b), c, 0, 0, 0);
}
DI __amdgpu_buffer_rsrc_t rsrc(const void* p, u32 bytes) { return __builtin_amdgcn_make_buffer_rsrc((void*)p, 0, bytes, 0x00020000); }
DI u32x4 srd(const void* p, u32 bytes) { const u64 a = (u64)p; return u32x4{(u32)a, (u32)(a >> 32) & 0xffffu, bytes, 0x00020000u}; }
DI void dma4(u32x4 sd, u32 v0, u32 v1, u32 soff, u32 ldsa) {
  u32 keep;
  asm volatile("s_mov_b32 %0, m0\n\ts_mov_b32 m0, %1\n\ts_nop 0\n\t"
               "buffer_load_dwordx4 %2, %4, %5 offen lds\n\tbuffer_load_dwordx4 %2, %4, %5 offen offset:1024 lds\n\t"
               "buffer_load_dwordx4 %3, %4, %5 offen offset:2048 lds\n\tbuffer_load_dwordx4 %3, %4, %5 offen offset:3072 lds\n\t"
               "s_mov_b32 m0, %0"
               : "=&s"(keep) : "s"(RFL(ldsa)), "v"(v0), "v"(v1), "s"(sd), "s"(soff) : "memory");
}
DI void dma2(u32x4 sd, u32 v0, u32 soff, u32 ldsa) {
  u32 keep;
  asm volatile("s_mov_b32 %0, m0\n\ts_mov_b32 m0, %1\n\ts_nop 0\n\t"
               "buffer_load_dwordx4 %2, %3, %4 offen lds\n\tbuffer_load_dwordx4 %2, %3, %4 offen offset:1024 lds\n\t"
               "s_mov_b32 m0, %0"
               : "=&s"(keep) : "s"(RFL(ldsa)), "v"(v0), "s"(sd), "s"(soff) : "memory");
}
DI u32 scan64(u32 x) {
  x += __builtin_amdgcn_update_dpp(0u, x, 0x111, 0xf, 0xf, false);
  x += __builtin_amdgcn_update_dpp(0u, x, 0x112, 0xf, 0xf, false);
  x += __builtin_amdgcn_update_dpp(0u, x, 0x114, 0xf, 0xf, false);
  x += __builtin_amdgcn_update_dpp(0u, x, 0x118, 0xf, 0xf, false);
  x += __builtin_amdgcn_update_dpp(0u, x, 0x142, 0xa, 0xf, false);
  x += __builtin_amdgcn_update_dpp(0u, x, 0x143, 0xc, 0xf, false);
  return x;
}

constexpr int L_ZERO = 0;
constexpr int L_CNT = L_ZERO + 16;
constexpr int L_BC = L_CNT + 48;
constexpr int L_SY = L_BC + 1024;
constexpr int L_SM = L_SY + 32;
constexpr int L_CF = L_SM + 8 * 8 * 640;
constexpr int L_SRC = L_CF + 8 * CAPW * 16;
constexpr int L_KB = L_SRC + 8 * CAPW * 4 + 64;
constexpr int L_W1 = L_KB + 8 * D * 16;
constexpr int L_W0 = L_W1 + 32768;
constexpr int L_HS = L_W0 + 32768;
constexpr int L_WX = L_HS + NPB * 256;
constexpr int L_PD = L_WX + 256;
constexpr int L_LS = L_PD + 256;
constexpr int L_END = L_LS + 8 * 768;
constexpr int L_RS = L_W0 - CH * 16;
constexpr int L_POS = L_RS - MAXPOS;
static_assert(L_END <= 160 * 1024, "lds");
static_assert(L_POS >= L_SM + 36864 && L_POS <= L_CF && (L_POS % 16) == 0, "pos table");
static_assert((L_SM % 16) == 0 && (L_BC % 16) == 0 && (L_SRC % 16) == 0 && (L_KB % 16) == 0 && (L_W0 % 16) == 0 && (L_HS % 16) == 0 && (L_WX % 16) == 0 && (L_LS % 16) == 0, "align");

DI int tile_node(int t, int c) { return t < 2 ? ((c & 3) * 8 + 4 * t + (c >> 2)) : (32 + (c & 7)); }
DI int mi_row(int nl) { return L_KB + (nl & 7) * 2048 + (nl & 3) * 64 + (nl >> 3) * 272; }
DI int wswz(int k) { return ((k & 3) << 2) | ((k >> 2) & 2); }
DI int hswz(int n) { return ((n & 3) << 2) | ((n >> 3) & 3); }
DI u32x4 tr_frag(const char* img, int s, int lane, int ch, int hf) {
  const int k0 = 8 * (lane >> 4) + ((lane & 15) >> 2);
  const char* p = img + (32 * s + k0) * 256 + 16 * (ch ^ wswz(k0)) + 8 * hf;
  const s16x4 a = __builtin_amdgcn_ds_read_tr16_b64_v4i16((lds_s16x4*)p);
  const s16x4 b = __builtin_amdgcn_ds_read_tr16_b64_v4i16((lds_s16x4*)(p + 1024));
  const u32x2 x = __builtin_bit_cast(u32x2, a), y = __builtin_bit_cast(u32x2, b);
  return u32x4{x[0], x[1], y[0], y[1]};
}
DI u32x4 s_frag(const char* p) {
  const u32x2 x = __builtin_bit_cast(u32x2, __builtin_amdgcn_ds_read_tr16_b64_v4i16((lds_s16x4*)p));
  const u32x2 y = __builtin_bit_cast(u32x2, __builtin_amdgcn_ds_read_tr16_b64_v4i16((lds_s16x4*)(p + 160)));
  return u32x4{x[0], x[1], y[0], y[1]};
}
DI u32x4 h_frag(const char* lds, int t, int s, int lane) {
  const int n = tile_node(t, lane & 15);
  return *(const u32x4*)(lds + L_HS + n * 256 + 16 * ((4 * s + (lane >> 4)) ^ hswz(n)));
}
DI u32 dist2(u32 w2, u32 w3) {
  const float dx = bf2f(w2), dy = bfhi(w2), dz = bf2f(w3);
  const u32 q0 = pk_bf16(dx * dx, dy * dy), q1 = pk_bf16(dz * dz, 0.f);
  const float s2 = bf2f(q0) + bfhi(q0) + bf2f(q1);
  return pk_bf16(s2, s2);
}
DI u32 take_rank(char* lds, u32x4 rc) { return atomicAdd((u32*)(lds + L_CNT) + ((rc[0] >> 14) & 7u), 1u); }
DI void put_rec(char* lds, u32x4 rc, u32 rank) {
  const u32 win = (rc[0] >> 14) & 7u, slot = (rc[0] >> 17) & 7u;
  const u64 oh = 0x3f80ull << (slot * 16);
  const u32x4 cf = {slot < 4 ? (u32)oh : 0u, slot < 4 ? (u32)(oh >> 32) : 0u, (slot == 4 ? 0x3f80u : 0u) | (rc[1] << 16), dist2(rc[2], rc[3])};
  if (rank < CAPW) {
    const u32 k = ((rank & 1u) << 3) + ((rank & 15u) >> 1), sp = win * CAPW + (rank & ~15u) + k;
    *(u32*)(lds + L_SRC + sp * 4) = (rc[0] & 0x3fffu) << 8;
    *(u32x4*)(lds + L_CF + sp * 16) = cf;
    *(u32x2*)(lds + L_SM + sp * 40 + slot * 8) = u32x2{0x3f80u | (rc[2] << 16), __builtin_amdgcn_alignbit(rc[3], rc[2], 16)};
  } else *(u32*)(lds + L_CNT + 32) = 1;
}
DI void rec_tags(u64 tag, u32 (&t)[3]) {
  t[0] = (u32)tag << 20; t[1] = ((u32)tag >> 12) << 16; t[2] = ((u32)(tag >> 28) ^ (u32)tag) << 16;
}
DI bool rec_stale(u32x4 rc, const u32 (&t)[3]) { return (((rc[0] ^ t[0]) >> 20) | (((rc[1] ^ t[1]) | (rc[3] ^ t[2])) >> 16)) != 0; }
DI u32x4 settle(u32x4 rc, bool on, __amdgpu_buffer_rsrc_t r, u32 off, const u32 (&t)[3]) {
  for (u32 spins = 0; __any(on && rec_stale(rc, t)) && spins < SPINS; spins++) {
    __builtin_amdgcn_s_sleep(4);
    if (on && rec_stale(rc, t)) rc = __builtin_amdgcn_raw_buffer_load_b128(r, off, 0, SC1);
  }
  return rc;
}
DI void arrive(u32 lb, int i) {
  asm volatile("s_mov_b64 exec, 1\n\tds_add_u32 %0, %1 offset:%2\n\ts_mov_b64 exec, -1" :: "v"(lb), "v"(1u), "i"(L_SY + 4 * i) : "memory");
}
DI void await(char* lds, int i, u32 n) {
  asm volatile("" ::: "memory");
  while ((u32)RFL(__hip_atomic_load((u32*)(lds + L_SY) + i, __ATOMIC_RELAXED, __HIP_MEMORY_SCOPE_WORKGROUP)) < n) { }
  asm volatile("" ::: "memory");
}
DI u32 silu2(float a, float b) {
  f32x2 yy = {a, b};
  f32x2 e = {__builtin_amdgcn_exp2f(-a), __builtin_amdgcn_exp2f(-b)};
  e += 1.0f;
  f32x2 rc = {__builtin_amdgcn_rcpf(e[0]), __builtin_amdgcn_rcpf(e[1])};
  yy *= rc;
  return pk_bf16(yy[0], yy[1]);
}

__global__ __launch_bounds__(NT, 2) void k_egnn(const u16* __restrict__ h, const u16* __restrict__ pos,
                                                const u16* __restrict__ ea, const int* __restrict__ src,
                                                const int* __restrict__ dst, const u16* __restrict__ w_e,
                                                const u16* __restrict__ w_x, const u16* __restrict__ w_h,
                                                char* __restrict__ ws, u16* __restrict__ out, int N, int E, int G) {
  __shared__ __attribute__((aligned(16))) char lds[L_END];
  const int tid = threadIdx.x, lane = tid & 63, b = blockIdx.x;
  const int wv = RFL(tid >> 6);
  const int u = wv & 3, c16 = lane & 15, g4 = lane >> 4;
  const bool role1 = wv >= 4;
  const u32 lb = (u32)(size_t)(lds_char*)lds;
  if (tid < L_SM / 4) *(u32*)(lds + tid * 4) = 0;
  __syncthreads();
  const int ne = RFL(max(0, min(CH, E - b * CH)));
  u32 ed[3], es[3], ee[3];
  u32x2 epoch;
  {
    const u32x4 s_d = srd(dst + b * CH, ne * 4), s_s = srd(src + b * CH, ne * 4), s_e = srd(ea + b * CH, ne * 2);
    asm volatile("s_cmp_lt_u32 %15, 4\n\ts_cbranch_scc1 1f\n\t"
                 "buffer_load_dword %0, %10, %12, 0 offen\n\tbuffer_load_dword %1, %10, %12, 0 offen offset:1024\n\t"
                 "buffer_load_dword %2, %10, %12, 0 offen offset:2048\n\tbuffer_load_dword %3, %10, %13, 0 offen\n\t"
                 "buffer_load_dword %4, %10, %13, 0 offen offset:1024\n\tbuffer_load_dword %5, %10, %13, 0 offen offset:2048\n\t"
                 "buffer_load_ushort %6, %11, %14, 0 offen\n\tbuffer_load_ushort %7, %11, %14, 0 offen offset:512\n\t"
                 "buffer_load_ushort %8, %11, %14, 0 offen offset:1024\n"
                 "1:\n\tglobal_load_dwordx2 %9, %16, %17 sc1"
                 : "=&v"(ed[0]), "=&v"(ed[1]), "=&v"(ed[2]), "=&v"(es[0]), "=&v"(es[1]), "=&v"(es[2]), "=&v"(ee[0]), "=&v"(ee[1]), "=&v"(ee[2]), "=&v"(epoch)
                 : "v"((tid & 255) * 4), "v"((tid & 255) * 2), "s"(s_d), "s"(s_s), "s"(s_e), "s"(wv), "v"(0u), "s"(ws + WS_EP) : "memory", "scc");
  }
  int bk;
  { const int q = G >> 3, r = G & 7, x = b & 7, i = b >> 3;
    bk = (x < r ? x * (q + 1) : r * (q + 1) + (x - r) * q) + i; }
  const int nb0 = bk * NPB, nn = RFL(max(0, min(NPB, N - nb0)));
  const int r = lane & 31, hh = lane >> 5;
  const int rh = (r >> 2) & 1, rp = r >> 4, rj = (r & 3) | (((r >> 3) & 1) << 2);
  f32x16 aA0, aA1, aB0, aB1, zero;
  u32x4 sel0, sel1;
  auto setup = [&]() {
#pragma unroll
    for (int i = 0; i < 16; i++) zero[i] = 0.f;
    u32x4 z4 = {0, 0, 0, 0};
    PIN(z4);
    aA0 = mfma32(z4, z4, zero); aA1 = mfma32(z4, z4, zero); aB0 = mfma32(z4, z4, zero); aB1 = mfma32(z4, z4, zero);
    const u32 one = rh == hh ? 0x3f80u << (rp * 16) : 0u;
#pragma unroll
    for (int q = 0; q < 4; q++) { sel0[q] = (rj == q) ? one : 0u; sel1[q] = (rj == 4 + q) ? one : 0u; }
    PIN(aA0); PIN(aA1); PIN(aB0); PIN(aB1); PIN(sel0); PIN(sel1);
  };
  u32 ep_lo, ep_hi;
  u32x4 hb[3][4];
  const u32 wv0 = g4 * 256 + 16 * (c16 ^ (g4 << 2)) + u * 4096, wv1 = wv0 ^ 32;
  if (!role1) {
    const u32x4 s_pos = srd(pos, N * 6), s_h = srd(h, N * 256), s_we = srd(w_e, 258 * 256), s_wh = srd(w_h, 65536);
#pragma unroll
    for (int i = 0; i < 4; i++)
      if (i < 3 || u < 3) dma4(s_pos, lane * 16 + u * 4096, lane * 16 + u * 4096, i * 16384, lb + L_POS + i * 16384 + u * 4096);
    dma2(s_h, g4 * 256 + 16 * (c16 ^ ((g4 << 2) | u)) + u * 2048, nb0 * 256, lb + L_HS + u * 2048);
    if (u == 0) dma2(s_h, g4 * 256 + 16 * (c16 ^ (g4 << 2)) + 8192, nb0 * 256, lb + L_HS + 8192);
    dma4(s_we, wv0, wv1, 32768, lb + L_W0 + u * 4096);
    dma4(s_we, wv0, wv1, 32768 + 16384, lb + L_W0 + 16384 + u * 4096);
    u32 wdv, wav, wxv, pf1, pf2;
    {
      const u32x4 s_wx = srd(w_x, 256);
      const u32 xi = (b >> 3) & 31;
      asm volatile("buffer_load_ushort %0, %5, %6, %7 offen\n\tbuffer_load_ushort %1, %5, %6, %7 offen offset:256\n\t"
                   "buffer_load_ushort %2, %5, %8, 0 offen\n\tbuffer_load_dword %3, %9, %6, 0 offen\n\t"
                   "buffer_load_dword %4, %10, %11, 0 offen"
                   : "=&v"(wdv), "=&v"(wav), "=&v"(wxv), "=&v"(pf1), "=&v"(pf2)
                   : "v"((tid & 127) * 2), "s"(s_we), "s"(65536u), "s"(s_wx), "v"(tid < 8 ? (xi * 8 + tid) * 128 : OOB),
                     "v"(tid - 64 < 16u ? (xi * 16 + tid - 64) * 128 : OOB), "s"(s_wh) : "memory");
    }
    asm volatile("s_waitcnt vmcnt(15)\n\tv_readfirstlane_b32 %0, %2\n\tv_readfirstlane_b32 %1, %3\n\ts_nop 1"
                 : "=s"(ep_lo), "=s"(ep_hi) : "v"(epoch[0]), "v"(epoch[1]) : "memory");
    arrive(lb, 0);
    setup();
    asm volatile("s_waitcnt vmcnt(5)" ::: "memory");
    arrive(lb, 1);
    char* const slot = ws + WS_SLOT0 + (size_t)(RFL(ep_lo) & 3u) * WS_SLOT;
    await(lds, 1, 4);
#pragma unroll
    for (int s = 0; s < 4; s++)
#pragma unroll
      for (int t = 0; t < 3; t++) hb[t][s] = h_frag(lds, t, s, lane);
    f32x4 acc[3][2];
#pragma unroll
    for (int t = 0; t < 3; t++) { acc[t][0] = f32x4{0, 0, 0, 0}; acc[t][1] = f32x4{0, 0, 0, 0}; }
#pragma unroll
    for (int s = 0; s < 4; s++) {
      const u32x4 fa = tr_frag(lds + L_W0, s, lane, 4 * u + (lane & 3), 0), fb = tr_frag(lds + L_W0, s, lane, 4 * u + (lane & 3), 1);
#pragma unroll
      for (int t = 0; t < 3; t++) { acc[t][0] = mfma16(fa, hb[t][s], acc[t][0]); acc[t][1] = mfma16(fb, hb[t][s], acc[t][1]); }
    }
    asm volatile("s_waitcnt vmcnt(0)" : "+v"(wdv), "+v"(wav), "+v"(wxv), "+v"(pf1), "+v"(pf2), "+v"(acc[2][1]) :: "memory");
    arrive(lb, 3);
    const __amdgpu_buffer_rsrc_t r_bw = rsrc(slot + WS_BT, N * 256);
#pragma unroll
    for (int t = 0; t < 3; t++) {
      const f32x4 x = acc[t][0] * LOG2E, y = acc[t][1] * LOG2E;
      const u32x4 o = {pk_bf16(x[0], x[1]), pk_bf16(x[2], x[3]), pk_bf16(y[0], y[1]), pk_bf16(y[2], y[3])};
      __builtin_amdgcn_raw_buffer_store_b128(o, r_bw, (t < 2 || c16 < 8) ? (nb0 + tile_node(t, c16)) * 256 + u * 64 + g4 * 16 : OOB, 0, SC1);
    }
    await(lds, 3, 4);
    dma4(s_wh, wv0, wv1, 0, lb + L_W0 + u * 4096);
    dma4(s_wh, wv0, wv1, 16384, lb + L_W0 + 16384 + u * 4096);
    await(lds, 4, 4);
    dma4(s_we, wv0, wv1, 0, lb + L_W1 + u * 4096);
    dma4(s_we, wv0, wv1, 16384, lb + L_W1 + 16384 + u * 4096);
    {
      const float wd = bf2f(wdv) * LOG2E;
      const u32 wdh = pk_bf16(wd, 0.f);
      const u32 wdp = wdh | (pk_bf16(wd - bf2f(wdh), 0.f) << 16), wap = pk_bf16(bf2f(wav) * LOG2E, 0.f);
#pragma unroll
      for (int i = 0; i < 4; i++) {
        char* kb = lds + L_KB + (i * 256 + tid) * 16;
        *(u32*)(kb + 12) = wdp;
        *(u16*)(kb + 10) = (u16)wap;
      }
      if (tid < D) *(u16*)(lds + L_WX + tid * 2) = (u16)wxv;
    }
    asm volatile("s_waitcnt vmcnt(0)" ::: "memory");
    if (lane == 0 && __hip_atomic_fetch_add((u32*)(lds + L_SY) + 5, 1u, __ATOMIC_RELAXED, __HIP_MEMORY_SCOPE_WORKGROUP) == 3)
      __hip_atomic_store((gu64*)(slot + WS_FL) + b, ((((u64)ep_hi << 32) | ep_lo) + 1) & 0xfffffffffffull, RLX_AGENT);
  } else {
    const int t1 = tid - 256;
    __builtin_amdgcn_s_setprio(2);
    const u32x4 z = {0, 0, 0, 0};
    setup();
    asm volatile("s_waitcnt vmcnt(7)" : "+v"(ed[0]), "+v"(ed[1]), "+v"(ed[2]) :: "memory");
    u32 rank[3], ebk[3];
#pragma unroll
    for (int i = 0; i < 3; i++) {
      ebk[i] = ed[i] / NPB;
      rank[i] = 0;
      if (t1 + 256 * i < ne) rank[i] = atomicAdd((u32*)(lds + L_BC) + ebk[i], 1u);
    }
    arrive(lb, 0);
    asm volatile("s_waitcnt vmcnt(0)\n\tv_readfirstlane_b32 %0, %8\n\tv_readfirstlane_b32 %1, %9\n\ts_nop 1"
                 : "=s"(ep_lo), "=s"(ep_hi), "+v"(es[0]), "+v"(es[1]), "+v"(es[2]), "+v"(ee[0]), "+v"(ee[1]), "+v"(ee[2])
                 : "v"(epoch[0]), "v"(epoch[1]) : "memory");
    ep_lo = RFL(ep_lo); ep_hi = RFL(ep_hi);
    char* const slot = ws + WS_SLOT0 + (size_t)(ep_lo & 3u) * WS_SLOT;
    const u64 tag = ((((u64)ep_hi << 32) | ep_lo) + 1) & 0xfffffffffffull;
    u32 tg[3];
    rec_tags(tag, tg);
    await(lds, 0, 8);
    const u32x4 c4 = *(const u32x4*)(lds + L_BC + lane * 16);
    const u32 cown = *(const u32*)(lds + L_BC + t1 * 4);
    u32x2 pd[3], ps[3];
#pragma unroll
    for (int i = 0; i < 3; i++) {
      const u32* qd = (const u32*)(lds + L_POS + ((ed[i] * 6) & ~3u));
      const u32* qs = (const u32*)(lds + L_POS + ((es[i] * 6) & ~3u));
      pd[i] = u32x2{qd[0], qd[1]}; ps[i] = u32x2{qs[0], qs[1]};
    }
    const u32 pown = *(const u16*)(lds + L_POS + min((nb0 * 3 + t1) * 2, MAXPOS - 2));
    const u32 tot = c4[0] + c4[1] + c4[2] + c4[3];
    const u32 ex = scan64(tot) - tot;
    u32* mine = (u32*)(lds + L_LS + u * 1024);
    *(u32x4*)(mine + lane * 4) = u32x4{ex, ex + c4[0], ex + c4[0] + c4[1], ex + c4[0] + c4[1] + c4[2]};
    __builtin_amdgcn_wave_barrier();
    u32 at[3];
#pragma unroll
    for (int i = 0; i < 3; i++) at[i] = L_RS + (mine[ebk[i]] + rank[i]) * 16;
    const u32 gword = (mine[t1] << 10) | cown;
#pragma unroll
    for (int i = 0; i < 3; i++) {
      const u32 shd = (ed[i] & 1u) * 16, shs = (es[i] & 1u) * 16;
      const u32 dxy = __builtin_amdgcn_alignbit(pd[i][1], pd[i][0], shd), sxy = __builtin_amdgcn_alignbit(ps[i][1], ps[i][0], shs);
      const u32 xy = pk_bf16(bf2f(dxy) - bf2f(sxy), bfhi(dxy) - bfhi(sxy));
      const u32 zz = pk_bf16(bf2f(pd[i][1] >> shd) - bf2f(ps[i][1] >> shs), 0.f);
      u32x4 rc = {es[i] | ((ed[i] - ebk[i] * NPB) << 14) | tg[0], ee[i] | tg[1], xy, zz | tg[2]};
      PIN(rc);
      if (t1 + 256 * i < ne) *(u32x4*)(lds + at[i]) = rc;
    }
    if (t1 < NPB * 3) *(u16*)(lds + L_PD + t1 * 2) = (u16)pown;
    arrive(lb, 2);
    await(lds, 2, 4);
    const __amdgpu_buffer_rsrc_t r_rw = rsrc(slot + WS_REC + (size_t)b * CH * 16, CH * 16);
    u32x4 rv[3];
#pragma unroll
    for (int i = 0; i < 3; i++) rv[i] = *(const u32x4*)(lds + L_RS + (t1 + 256 * i) * 16);
#pragma unroll
    for (int i = 0; i < 3; i++) __builtin_amdgcn_raw_buffer_store_b128(rv[i], r_rw, t1 * 16, i * 4096, SC1);
    arrive(lb, 4);
    __hip_atomic_store((gu64*)(slot + WS_GR) + (size_t)b * MAXG + t1, (tag << 20) | gword, RLX_AGENT);
    __builtin_amdgcn_s_setprio(0);
#pragma unroll
    for (int i = 0; i < 10; i++) if (i < 9 || L_SM + i * 4096 + t1 * 16 < L_POS) *(u32x4*)(lds + L_SM + i * 4096 + t1 * 16) = z;
#pragma unroll
    for (int i = 0; i < 5; i++) { const int o = L_POS + i * 4096 + t1 * 16; if (i < 4 || o < L_SRC) *(u32x4*)(lds + o) = z; }
    *(u32x4*)(lds + L_SRC + t1 * 16) = u32x4{OOB, OOB, OOB, OOB};
    if (t1 < 16) *(u32*)(lds + L_SRC + 4096 + t1 * 4) = OOB;
    await(lds, 1, 4);
#pragma unroll
    for (int s = 0; s < 4; s++)
#pragma unroll
      for (int t = 0; t < 3; t++) hb[t][s] = h_frag(lds, t, s, lane);
  }
  __syncthreads();
  ep_lo = RFL(ep_lo); ep_hi = RFL(ep_hi);
  const u64 ep = ((u64)ep_hi << 32) | ep_lo, tag = (ep + 1) & 0xfffffffffffull;
  char* const slot = ws + WS_SLOT0 + (size_t)(ep_lo & 3u) * WS_SLOT;
  const __amdgpu_buffer_rsrc_t r_bt = rsrc(slot + WS_BT, N * 256), r_rec = rsrc(slot + WS_REC, G * CH * 16);
  u32 tg[3];
  rec_tags(tag, tg);
  f32x4 cp[3];
  u32 pw[4] = {0, 0, 0, 0};
  {
    const int cs = wv * 32 + (lane & 31), csc = cs < G ? cs : G - 1;
    gu64* const gp = (gu64*)(slot + WS_GR) + (size_t)csc * MAXG + bk;
    gu64* const fp = (gu64*)(slot + WS_FL) + csc;
    u64 gv = __hip_atomic_load(gp, RLX_AGENT);
    SB();
    {
      f32x4 acc[3] = {f32x4{0, 0, 0, 0}, f32x4{0, 0, 0, 0}, f32x4{0, 0, 0, 0}};
#pragma unroll
      for (int s = 0; s < 4; s++) {
        const u32x4 fw = tr_frag(lds + L_W1, s, lane, 2 * wv + ((lane & 3) >> 1), lane & 1);
#pragma unroll
        for (int t = 0; t < 3; t++) acc[t] = mfma16(hb[t][s], fw, acc[t]);
      }
      char* kb = lds + L_KB + (16 * wv + c16) * 16;
#pragma unroll
      for (int t = 0; t < 2; t++) {
        const f32x4 a = acc[t] * LOG2E;
        *(u32x2*)(kb + (4 * t + g4) * 2048) = u32x2{pk_bf16(a[0], a[1]), pk_bf16(a[2], a[3])};
      }
      if (g4 < 2) {
#pragma unroll
        for (int i = 0; i < 4; i++) *(u16*)(kb + (4 * g4 + i) * 2048 + 8) = (u16)pk_bf16(acc[2][i] * LOG2E, 0.f);
      }
    }
    SB();
    for (u32 spins = 0; !__all((gv >> 20) == tag) && spins < SPINS; spins++) {
      __builtin_amdgcn_s_sleep(4);
      if ((gv >> 20) != tag) gv = __hip_atomic_load(gp, RLX_AGENT);
    }
    const bool on = lane < 32 && cs < G;
    const u32 cnt = on ? ((u32)gv & 1023u) : 0u, st = ((u32)gv >> 10) & 1023u;
    const u32 inc = scan64(cnt);
    const u32 total = __builtin_amdgcn_readlane(inc, 63);
    u32* list = (u32*)(lds + L_LS + wv * 768);
    const u32 base = (cs * CH + st) * 16, p0 = inc - cnt;
    const u32 lim = min(cnt, p0 < 128 ? 128 - p0 : 0u);
#pragma unroll
    for (u32 k = 0; k < 6; k++) list[k < lim ? p0 + k : 128 + lane] = base + k * 16;
    if (__any(lim > 6)) {
#pragma unroll 1
      for (u32 k = 6; k < lim; k++) list[p0 + k] = base + k * 16;
    }
    __builtin_amdgcn_wave_barrier();
    {
      const u32 a0 = list[lane], a1 = list[64 + lane];
      const bool v0 = lane < total, v1 = 64 + lane < total;
      u32x4 r0 = __builtin_amdgcn_raw_buffer_load_b128(r_rec, v0 ? a0 : OOB, 0, SC1);
      u32x4 r1 = __builtin_amdgcn_raw_buffer_load_b128(r_rec, v1 ? a1 : OOB, 0, SC1);
      u64 fv = __hip_atomic_load(fp, RLX_AGENT);
      if (on) *(u32*)(lds + L_BC + cs * 4) = (u32)gv & 0xfffffu;
      SB();
#pragma unroll
      for (int t = 0; t < 3; t++) cp[t] = f32x4{0, 0, 0, 0};
#pragma unroll
      for (int s = 0; s < 4; s++) {
        const u32x4 f1 = tr_frag(lds + L_W0, s, lane, 2 * wv + ((lane & 3) >> 1), lane & 1);
#pragma unroll
        for (int t = 0; t < 3; t++) cp[t] = mfma16(f1, hb[t][s], cp[t]);
      }
#pragma unroll
      for (int t = 0; t < 3; t++) { cp[t] *= LOG2E; PIN(cp[t]); }
      SB();
      r0 = settle(r0, v0, r_rec, a0, tg);
      r1 = settle(r1, v1, r_rec, a1, tg);
      u32 k0 = 0, k1 = 0;
      if (v0) k0 = take_rank(lds, r0);
      if (v1) k1 = take_rank(lds, r1);
      for (u32 spins = 0; !__all(fv == tag) && spins < SPINS; spins++) {
        __builtin_amdgcn_s_sleep(4);
        fv = __hip_atomic_load(fp, RLX_AGENT);
      }
      __builtin_amdgcn_s_waitcnt(0x0f70);
      pw[0] = __builtin_amdgcn_raw_buffer_load_b32(r_bt, v0 && k0 < PFR ? (r0[0] & 0x3fffu) << 8 : OOB, 0, SC1);
      pw[1] = __builtin_amdgcn_raw_buffer_load_b32(r_bt, v0 && k0 < PFR ? ((r0[0] & 0x3fffu) << 8) + 128 : OOB, 0, SC1);
      pw[2] = __builtin_amdgcn_raw_buffer_load_b32(r_bt, v1 && k1 < PFR ? (r1[0] & 0x3fffu) << 8 : OOB, 0, SC1);
      pw[3] = __builtin_amdgcn_raw_buffer_load_b32(r_bt, v1 && k1 < PFR ? ((r1[0] & 0x3fffu) << 8) + 128 : OOB, 0, SC1);
      SB();
      if (v0) put_rec(lds, r0, k0);
      if (v1) put_rec(lds, r1, k1);
    }
    if (__builtin_expect(total > 128, 0)) {
#pragma unroll 1
      for (u32 done = 128; done < total; done += 64) {
        if (__hip_atomic_load((u32*)(lds + L_CNT + 32), __ATOMIC_RELAXED, __HIP_MEMORY_SCOPE_WORKGROUP)) break;
        __builtin_amdgcn_wave_barrier();
#pragma unroll 1
        for (u32 k = p0 < done ? done - p0 : 0u; k < cnt && p0 + k < done + 64; k++) list[p0 + k - done] = base + k * 16;
        __builtin_amdgcn_wave_barrier();
        const bool v0 = done + lane < total;
        const u32 a0 = list[lane];
        u32x4 r0 = __builtin_amdgcn_raw_buffer_load_b128(r_rec, v0 ? a0 : OOB, 0, SC1);
        r0 = settle(r0, v0, r_rec, a0, tg);
        if (v0) put_rec(lds, r0, take_rank(lds, r0));
      }
      __builtin_amdgcn_s_waitcnt(0x0f70);
    }
  }
  __syncthreads();
  const u32* wcnt = (const u32*)(lds + L_CNT);
  const u32 ci = wcnt[lane >> 3], cj = wcnt[lane & 7];
  const int ovf = RFL(wcnt[8]);
  auto load_wh2 = [&]() {
    const u32x4 s_wh = srd(w_h, 65536);
    dma4(s_wh, wv0 + (wv >> 2) * 16384, wv1 + (wv >> 2) * 16384, 32768, lb + L_W0 + wv * 4096);
  };
  u32x4 v[8], wxf[8];
  int win;
  {
    const u64 gt = __ballot(cj * 8 + (lane & 7) > ci * 8 + (lane >> 3));
    const u32 rk = __builtin_popcount((u32)(gt >> (lane & 56)) & 0xffu);
    const int wl = __builtin_ctzll(__ballot(rk == (u32)(wv < 4 ? wv : 11 - wv)));
    win = wl >> 3;
    const int cntw = ovf ? 1 : __builtin_amdgcn_readlane(ci, wl);
    const int nsteps = (cntw + 15) >> 4;
    const u32 lo = r * 4;
    u32 srca = L_SRC + win * CAPW * 4 + hh * 32;
    u32 GA[8], GB[8], off[8];
    const bool cfon = rp == hh;
    u32 cfa = cfon ? (L_CF + (win * CAPW + 8 * rh + rj) * 16) : L_ZERO;
    const u32 cfs = cfon ? 256u : 0u;
    u32 sma = L_SM + (win * CAPW + 8 * hh + (c16 >> 2)) * 40 + (g4 & 1) * 32 + (lane & 3) * 8;
    u32x4 cf, kbA, kbB, sf = {0, 0, 0, 0}, mB0 = {0, 0, 0, 0}, mB1 = {0, 0, 0, 0}, mA0, mA1;
    f32x16 yA, yB;
    {
      const u32x4 i0 = *(const u32x4*)(lds + srca), i1 = *(const u32x4*)(lds + srca + 16);
      const u32x4 i2 = *(const u32x4*)(lds + srca + 64), i3 = *(const u32x4*)(lds + srca + 80);
      cf = *(const u32x4*)(lds + cfa);
      kbA = *(const u32x4*)(lds + L_KB + win * 2048 + (2 * r + hh) * 16);
      kbB = *(const u32x4*)(lds + L_KB + win * 2048 + (64 + 2 * r + hh) * 16);
      srca += 128;
      SB();
#pragma unroll
      for (int j = 0; j < 4; j++) { off[j] = i0[j] + lo; off[4 + j] = i1[j] + lo; }
#pragma unroll
      for (int j = 0; j < 8; j++) GA[j] = __builtin_amdgcn_raw_buffer_load_b32(r_bt, off[j], 0, SC1);
      SB();
#pragma unroll
      for (int j = 0; j < 8; j++) GB[j] = __builtin_amdgcn_raw_buffer_load_b32(r_bt, off[j] + 128, 0, SC1);
      SB();
      asm volatile("" :: "v"(pw[0]), "v"(pw[1]), "v"(pw[2]), "v"(pw[3]));
      yA = mfma32(cf, kbA, zero);
#pragma unroll
      for (int j = 0; j < 4; j++) { off[j] = i2[j] + lo; off[4 + j] = i3[j] + lo; }
      yA = mfma32(sel0, u32x4{GA[0], GA[1], GA[2], GA[3]}, yA);
      yA = mfma32(sel1, u32x4{GA[4], GA[5], GA[6], GA[7]}, yA);
      PIN(yA);
      SB();
#pragma unroll
      for (int j = 0; j < 8; j++) GA[j] = __builtin_amdgcn_raw_buffer_load_b32(r_bt, off[j], 0, SC1);
      SB();
    }
#define MF(d, a, bb, c) do { SB(); d = mfma32(a, bb, c); PIN(d); SB(); } while (0)
#define SA(y, i, j) do { p_ = f32x2{__builtin_amdgcn_exp2f(-y[i]), __builtin_amdgcn_exp2f(-y[(i) + 1])}; \
                         q_ = f32x2{__builtin_amdgcn_exp2f(-y[j]), __builtin_amdgcn_exp2f(-y[(j) + 1])}; SB(); p_ += 1.0f; q_ += 1.0f; SB(); } while (0)
#define SBB(y, i, j, o0, o1) do { p_ = f32x2{__builtin_amdgcn_rcpf(p_[0]), __builtin_amdgcn_rcpf(p_[1])}; \
                         q_ = f32x2{__builtin_amdgcn_rcpf(q_[0]), __builtin_amdgcn_rcpf(q_[1])}; SB(); \
                         p_ *= f32x2{y[i], y[(i) + 1]}; q_ *= f32x2{y[j], y[(j) + 1]}; SB(); \
                         u32 t0_ = pk_bf16(p_[0], p_[1]), t1_ = pk_bf16(q_[0], q_[1]); \
                         asm volatile("" : "+v"(t0_), "+v"(t1_)); o0 = t0_; o1 = t1_; SB(); } while (0)
#define S4(y, i, j, o0, o1) do { SA(y, i, j); SBB(y, i, j, o0, o1); } while (0)
#define LD2(Gx, j, add) do { Gx[j] = __builtin_amdgcn_raw_buffer_load_b32(r_bt, off[j] + (add), 0, SC1); \
                             Gx[(j) + 1] = __builtin_amdgcn_raw_buffer_load_b32(r_bt, off[(j) + 1] + (add), 0, SC1); SB(); } while (0)
    f32x2 p_, q_;
    for (int s = 1; s < nsteps; s++) {
      { const int left = nsteps - s; if (left >= 4) __builtin_amdgcn_s_setprio(3); else if (left == 3) __builtin_amdgcn_s_setprio(2); else if (left == 2) __builtin_amdgcn_s_setprio(1); else __builtin_amdgcn_s_setprio(0); }
      MF(yB, cf, kbB, zero);
      S4(yA, 0, 2, mA0[0], mA0[1]);
      MF(yB, sel0, (u32x4{GB[0], GB[1], GB[2], GB[3]}), yB);
      cfa += cfs;
      cf = *(const u32x4*)(lds + cfa);
      SA(yA, 4, 6);
      LD2(GB, 0, 128);
      SBB(yA, 4, 6, mA0[2], mA0[3]);
      LD2(GB, 2, 128);
      MF(yB, sel1, (u32x4{GB[4], GB[5], GB[6], GB[7]}), yB);
      SA(yA, 8, 10);
      LD2(GB, 4, 128);
      MF(aB0, mB0, sf, aB0);
      SBB(yA, 8, 10, mA1[0], mA1[1]);
      LD2(GB, 6, 128);
      const u32x4 i0 = *(const u32x4*)(lds + srca), i1 = *(const u32x4*)(lds + srca + 16);
      srca += 64;
      SA(yA, 12, 14);
      MF(aB1, mB1, sf, aB1);
      sf = s_frag(lds + sma);
      sma += 640;
      SBB(yA, 12, 14, mA1[2], mA1[3]);
      MF(yA, cf, kbA, zero);
      S4(yB, 0, 2, mB0[0], mB0[1]);
      MF(yA, sel0, (u32x4{GA[0], GA[1], GA[2], GA[3]}), yA);
#pragma unroll
      for (int j = 0; j < 4; j++) { off[j] = i0[j] + lo; off[4 + j] = i1[j] + lo; }
      SA(yB, 4, 6);
      LD2(GA, 0, 0);
      SBB(yB, 4, 6, mB0[2], mB0[3]);
      LD2(GA, 2, 0);
      MF(yA, sel1, (u32x4{GA[4], GA[5], GA[6], GA[7]}), yA);
      SA(yB, 8, 10);
      LD2(GA, 4, 0);
      MF(aA0, mA0, sf, aA0);
      SBB(yB, 8, 10, mB1[0], mB1[1]);
      LD2(GA, 6, 0);
      SA(yB, 12, 14);
      MF(aA1, mA1, sf, aA1);
      SBB(yB, 12, 14, mB1[2], mB1[3]);
    }
    {
      const int rem = cntw - 16 * (nsteps - 1);
      load_wh2();
      MF(yB, cf, kbB, zero);
      mA0 = u32x4{0, 0, 0, 0}; mA1 = u32x4{0, 0, 0, 0};
      S4(yA, 0, 8, mA0[0], mA1[0]);
      MF(yB, sel0, (u32x4{GB[0], GB[1], GB[2], GB[3]}), yB);
      if (rem > 4) S4(yA, 2, 10, mA0[1], mA1[1]);
      MF(yB, sel1, (u32x4{GB[4], GB[5], GB[6], GB[7]}), yB);
      if (rem > 8) S4(yA, 4, 12, mA0[2], mA1[2]);
      MF(aB0, mB0, sf, aB0);
      if (rem > 12) S4(yA, 6, 14, mA0[3], mA1[3]);
      MF(aB1, mB1, sf, aB1);
      sf = s_frag(lds + sma);
      mB0 = u32x4{0, 0, 0, 0}; mB1 = u32x4{0, 0, 0, 0};
      S4(yB, 0, 8, mB0[0], mB1[0]);
      MF(aA0, mA0, sf, aA0);
      if (rem > 4) S4(yB, 2, 10, mB0[1], mB1[1]);
      MF(aA1, mA1, sf, aA1);
      if (rem > 8) S4(yB, 4, 12, mB0[2], mB1[2]);
      if (rem > 12) S4(yB, 6, 14, mB0[3], mB1[3]);
      MF(aB0, mB0, sf, aB0);
      MF(aB1, mB1, sf, aB1);
    }
#pragma unroll
    for (int q8 = 0; q8 < 8; q8++) {
      const f32x16& a0 = q8 < 4 ? aA0 : aB0;
      const f32x16& a1 = q8 < 4 ? aA1 : aB1;
#pragma unroll
      for (int q = 0; q < 4; q++) v[q8][q] = pk_bf16(a0[4 * (q8 & 3) + q], a1[4 * (q8 & 3) + q]);
    }
    if (r < 20 && (r & 3) == 0 && !ovf) {
      char* mi = lds + mi_row(r * 2 + win) + 16 * hh;
#pragma unroll
      for (int q8 = 0; q8 < 8; q8++) *(u32x4*)(mi + 32 * q8) = v[q8];
    }
  }
  if (__builtin_expect(ovf, 0)) {
    float* macc = (float*)(lds + L_SM);
    float* pacc = (float*)(lds + L_SM + NPB * D * 4);
    __syncthreads();
    for (int i = tid; i < NPB * D + NPB * 4; i += NT) macc[i] = 0.f;
    __syncthreads();
    const u32 wxp = *(const u32*)(lds + L_WX + lane * 4);
    const float wx0 = bf2f(wxp), wx1 = bfhi(wxp);
    const u32x4 kc0 = *(const u32x4*)(lds + L_KB + (2 * lane) * 16), kc1 = *(const u32x4*)(lds + L_KB + (2 * lane + 1) * 16);
    const float wd0 = bf2f(kc0[3]) + bfhi(kc0[3]), wd1 = bf2f(kc1[3]) + bfhi(kc1[3]);
    const float wa0 = bfhi(kc0[2]), wa1 = bfhi(kc1[2]);
    u32 rot = 0;
    for (int c = 0; c < G; c++) {
      const u32 oo = RFL(*(const u32*)(lds + L_BC + c * 4));
      const u32 s0 = oo >> 10, n = oo & 1023u;
      for (u32 k = (wv - rot) & 7u; k < n; k += 8) {
        u32x4 rr = __builtin_amdgcn_raw_buffer_load_b128(r_rec, (c * CH + s0 + k) * 16, 0, SC1);
        rr = settle(rr, true, r_rec, (c * CH + s0 + k) * 16, tg);
        const u32 nl = (rr[0] >> 14) & 0x3fu, sl = nl >> 3, wn = nl & 7u;
        const u32 bv = __builtin_amdgcn_raw_buffer_load_b32(r_bt, ((rr[0] & 0x3fffu) << 8) + lane * 4, 0, SC1);
        const float d2 = bfhi(dist2(rr[2], rr[3])), eav = bf2f(rr[1]);
        const float a0 = bf2f(*(const u16*)(lds + L_KB + wn * 2048 + (2 * lane) * 16 + sl * 2));
        const float a1 = bf2f(*(const u16*)(lds + L_KB + wn * 2048 + (2 * lane + 1) * 16 + sl * 2));
        const float y0 = a0 + bf2f(bv) + d2 * wd0 + eav * wa0, y1 = a1 + bfhi(bv) + d2 * wd1 + eav * wa1;
        const u32 mp = silu2(y0, y1);
        const float m0 = bf2f(mp), m1 = bfhi(mp);
        atomicAdd(&macc[nl * D + 2 * lane], m0);
        atomicAdd(&macc[nl * D + 2 * lane + 1], m1);
        float csv = m0 * wx0 + m1 * wx1;
#pragma unroll
        for (int o = 32; o > 0; o >>= 1) csv += __shfl_xor(csv, o, 64);
        if (lane < 3) atomicAdd(&pacc[nl * 4 + lane], csv * (lane == 0 ? bf2f(rr[2]) : lane == 1 ? bfhi(rr[2]) : bf2f(rr[3])));
      }
      rot += n;
    }
    __syncthreads();
    for (int i = tid; i < NPB * D / 2; i += NT) {
      const int n = i >> 6, f = (i & 63) * 2;
      *(u32*)(lds + mi_row(n) + f * 2) = pk_bf16(macc[n * D + f], macc[n * D + f + 1]);
    }
    if (tid < NPB * 3) {
      const int n = tid / 3, c = tid - n * 3;
      if (nb0 + n < N) out[(size_t)N * D + (size_t)nb0 * 3 + tid] = (u16)pk_bf16(bf2f(*(const u16*)(lds + L_PD + tid * 2)) + LN2 * pacc[n * 4 + c], 0.f);
    }
  }
  u32x2 hr[3];
  u32x4 f2[4];
#pragma unroll
  for (int t = 0; t < 3; t++) {
    const int n = tile_node(t, c16);
    hr[t] = *(const u32x2*)(lds + L_HS + n * 256 + 16 * ((2 * wv + (g4 >> 1)) ^ hswz(n)) + 8 * (g4 & 1));
  }
#pragma unroll
  for (int q8 = 0; q8 < 8; q8++) wxf[q8] = *(const u32x4*)(lds + L_WX + (16 * q8 + 8 * hh) * 2);
  const int kind = lane & 3, pnl = (lane >> 2) * 8 + win;
  const bool pon = kind != 0 && lane < 20 && !ovf;
  const u32 p0 = *(const u16*)(lds + L_PD + (pon ? pnl * 6 + kind * 2 - 2 : 0));
  const int mia = mi_row(tile_node(0, c16)) + g4 * 16, mib = mi_row(tile_node(2, c16)) + g4 * 16;
  asm volatile("s_waitcnt vmcnt(0)" ::: "memory");
  __syncthreads();
  {
    u32x4 mb[3][4];
#pragma unroll
    for (int s = 0; s < 4; s++) {
      f2[s] = tr_frag(lds + L_W0, s, lane, 2 * wv + ((lane & 3) >> 1), lane & 1);
      mb[0][s] = *(const u32x4*)(lds + mia + s * 64);
      mb[1][s] = *(const u32x4*)(lds + mia + 8192 + s * 64);
      mb[2][s] = *(const u32x4*)(lds + mib + s * 64);
    }
    SB();
#pragma unroll
    for (int s = 0; s < 4; s++)
#pragma unroll
      for (int t = 0; t < 3; t++) cp[t] = mfma16(f2[s], mb[t][s], cp[t]);
    const __amdgpu_buffer_rsrc_t r_out = rsrc(out + (size_t)nb0 * D, nn * 256);
    f32x16 z = zero, z1 = zero;
#pragma unroll
    for (int t = 0; t < 3; t++) {
      const f32x4 y = cp[t];
      f32x4 e;
#pragma unroll
      for (int i = 0; i < 4; i++) e[i] = __builtin_amdgcn_exp2f(-y[i]);
      SB();
      if (t & 1) { z1 = mfma32(wxf[3 * t], v[3 * t], z1); PIN(z1); } else { z = mfma32(wxf[3 * t], v[3 * t], z); PIN(z); }
      SB();
      e += 1.0f;
#pragma unroll
      for (int i = 0; i < 4; i++) e[i] = __builtin_amdgcn_rcpf(e[i]);
      SB();
      if (t & 1) { z = mfma32(wxf[3 * t + 1], v[3 * t + 1], z); PIN(z); } else { z1 = mfma32(wxf[3 * t + 1], v[3 * t + 1], z1); PIN(z1); }
      SB();
      const f32x4 o = f32x4{bf2f(hr[t][0]), bfhi(hr[t][0]), bf2f(hr[t][1]), bfhi(hr[t][1])} + (y * LN2) * e;
      __builtin_amdgcn_raw_buffer_store_b64(u32x2{pk_bf16(o[0], o[1]), pk_bf16(o[2], o[3])}, r_out,
                                            (t < 2 || c16 < 8) ? tile_node(t, c16) * 256 + wv * 32 + g4 * 8 : OOB, 0, 0);
      SB();
      if (t == 0) { z = mfma32(wxf[2], v[2], z); PIN(z); }
      if (t == 1) { z1 = mfma32(wxf[5], v[5], z1); PIN(z1); }
      SB();
    }
    __builtin_amdgcn_raw_buffer_store_b16((u16)pk_bf16(bf2f(p0) + LN2 * (z[0] + z1[0]), 0.f), rsrc(out + (size_t)N * D + nb0 * 3, nn * 6),
                                          pon ? (pnl * 3 + kind - 1) * 2 : OOB, 0, 0);
  }
  if (b == 0 && tid == 0) __hip_atomic_store((gu64*)(ws + WS_EP), ep + 1, RLX_AGENT);
}

__global__ __launch_bounds__(128) void k_edge_any(const u16* h, const u16* pos, const u16* ea, const int* src, const int* dst,
                                                  const u16* w_e, const u16* w_x, float* acc, int N) {
  __shared__ float x[2 * D + 2], red[2];
  const int e = blockIdx.x, f = threadIdx.x, s = src[e], d = dst[e];
  const u32 xy = pk_bf16(bf2f(pos[d * 3]) - bf2f(pos[s * 3]), bf2f(pos[d * 3 + 1]) - bf2f(pos[s * 3 + 1]));
  const u32 zz = pk_bf16(bf2f(pos[d * 3 + 2]) - bf2f(pos[s * 3 + 2]), 0.f);
  x[f] = bf2f(h[(size_t)d * D + f]); x[D + f] = bf2f(h[(size_t)s * D + f]);
  if (f == 0) { x[2 * D] = bfhi(dist2(xy, zz)); x[2 * D + 1] = bf2f(ea[e]); }
  __syncthreads();
  float y = 0.f;
  for (int k = 0; k < 2 * D + 2; k++) y += x[k] * bf2f(w_e[k * D + f]);
  const float m = LN2 * bf2f(silu2(y * LOG2E, 0.f));
  atomicAdd(&acc[(size_t)d * D + f], m);
  float c = m * bf2f(w_x[f]);
  for (int o = 32; o > 0; o >>= 1) c += __shfl_xor(c, o, 64);
  if ((f & 63) == 0) red[f >> 6] = c;
  __syncthreads();
  if (f < 3) atomicAdd(&acc[(size_t)N * D + d * 3 + f], (red[0] + red[1]) * (f == 0 ? bf2f(xy) : f == 1 ? bfhi(xy) : bf2f(zz)));
}
__global__ __launch_bounds__(128) void k_node_any(const u16* h, const u16* pos, const u16* w_h, const float* acc, u16* out, int N) {
  __shared__ float x[2 * D];
  const int n = blockIdx.x, f = threadIdx.x;
  x[f] = bf2f(h[(size_t)n * D + f]); x[D + f] = bf2f(pk_bf16(acc[(size_t)n * D + f], 0.f));
  __syncthreads();
  float y = 0.f;
  for (int k = 0; k < 2 * D; k++) y += x[k] * bf2f(w_h[k * D + f]);
  out[(size_t)n * D + f] = (u16)pk_bf16(x[f] + y * __builtin_amdgcn_rcpf(1.f + __builtin_amdgcn_exp2f(-y * LOG2E)), 0.f);
  if (f < 3) out[(size_t)N * D + n * 3 + f] = (u16)pk_bf16(bf2f(pos[n * 3 + f]) + acc[(size_t)N * D + n * 3 + f], 0.f);
}

extern "C" void kernel_launch(void* const* d_in, const int* in_sizes, int n_in, void* d_out, int out_size, void* d_ws,
                              size_t ws_size, hipStream_t stream) {
  const u16 *h = (const u16*)d_in[0], *pos = (const u16*)d_in[1], *ea = (const u16*)d_in[2];
  const int *src = (const int*)d_in[3], *dst = (const int*)d_in[4];
  const u16 *w_e = (const u16*)d_in[5], *w_x = (const u16*)d_in[6], *w_h = (const u16*)d_in[7];
  const int N = in_sizes[0] / D, E = in_sizes[3];
  const int NB = (N + NPB - 1) / NPB, C = (E + CH - 1) / CH;
  const int G = NB > C ? NB : C;
  if (G <= MAXG && N * 6 <= MAXPOS && (N & 7) == 0 && ws_size >= WS_SLOT0 + 4 * WS_SLOT) {
    k_egnn<<<G, NT, 0, stream>>>(h, pos, ea, src, dst, w_e, w_x, w_h, (char*)d_ws, (u16*)d_out, N, E, G);
  } else {
    (void)hipMemsetAsync(d_ws, 0, (size_t)N * (D + 3) * sizeof(float), stream);
    if (E > 0) k_edge_any<<<E, 128, 0, stream>>>(h, pos, ea, src, dst, w_e, w_x, (float*)d_ws, N);
    k_node_any<<<N, 128, 0, stream>>>(h, pos, w_h, (const float*)d_ws, (u16*)d_out, N);
  }
}
```

```cpp
#include <hip/hip_runtime.h>
#include <hip/hip_bf16.h>

typedef unsigned int u32;
typedef unsigned short u16;
typedef unsigned long long u64;
typedef u32 u32x2 __attribute__((ext_vector_type(2)));
typedef u32 u32x4 __attribute__((ext_vector_type(4)));
typedef short s16x4 __attribute__((ext_vector_type(4)));
typedef short bf16x8 __attribute__((ext_vector_type(8)));
typedef float f32x2 __attribute__((ext_vector_type(2)));
typedef float f32x4 __attribute__((ext_vector_type(4)));
typedef float f32x16 __attribute__((ext_vector_type(16)));
typedef __bf16 bf16x2_t __attribute__((ext_vector_type(2)));
typedef __attribute__((address_space(1))) u64 gu64;
typedef __attribute__((address_space(3))) s16x4 lds_s16x4;
typedef __attribute__((address_space(3))) char lds_char;
#define DI __device__ __forceinline__
#define RLX_AGENT __ATOMIC_RELAXED, __HIP_MEMORY_SCOPE_AGENT
#define SC1 16
#define SB() __builtin_amdgcn_sched_barrier(0)
#define PIN(x) asm volatile("" : "+v"(x))
#define RFL(x) __builtin_amdgcn_readfirstlane(x)

constexpr int D = 128;
constexpr int NT = 512;
constexpr int NPB = 40;
constexpr int CH = 640;
constexpr int MAXG = 256;
constexpr int CAPW = 128;
constexpr int MAXPOS = 61440;
constexpr u32 PFR = 16;
constexpr u32 SPINS = 1u << 16;
constexpr float LOG2E = 1.4426950408889634f;
constexpr float LN2 = 0.6931471805599453f;
constexpr u32 OOB = 0xfffff000u;

constexpr size_t WS_EP = 0;
constexpr size_t WS_SLOT0 = (size_t)2 << 20, WS_SLOT = (size_t)6 << 20;
constexpr size_t WS_GR = 0;
constexpr size_t WS_BT = WS_GR + (size_t)MAXG * MAXG * 8;
constexpr size_t WS_REC = WS_BT + (size_t)MAXG * NPB * 256;
constexpr size_t WS_FL = WS_REC + (size_t)MAXG * CH * 16;
static_assert(WS_FL + MAXG * 8 <= WS_SLOT, "slot");

DI float bf2f(u32 b) { return __builtin_bit_cast(float, b << 16); }
DI float bfhi(u32 b) { return __builtin_bit_cast(float, b & 0xffff0000u); }
DI u32 pk_bf16(float lo, float hi) {
  f32x2 v = {lo, hi};
  bf16x2_t r = __builtin_convertvector(v, bf16x2_t);
  return __builtin_bit_cast(u32, r);
}
DI f32x4 mfma16(u32x4 a, u32x4 b, f32x4 c) {
  return __builtin_amdgcn_mfma_f32_16x16x32_bf16(__builtin_bit_cast(bf16x8, a), __builtin_bit_cast(bf16x8, b), c, 0, 0, 0);
}
DI f32x16 mfma32(u32x4 a, u32x4 b, f32x16 c) {
  return __builtin_amdgcn_mfma_f32_32x32x16_bf16(__builtin_bit_cast(bf16x8, a), __builtin_bit_cast(bf16x8, b), c, 0, 0, 0);
}
DI __amdgpu_buffer_rsrc_t rsrc(const void* p, u32 bytes) { return __builtin_amdgcn_make_buffer_rsrc((void*)p, 0, bytes, 0x00020000); }
DI u32x4 srd(const void* p, u32 bytes) { const u64 a = (u64)p; return u32x4{(u32)a, (u32)(a >> 32) & 0xffffu, bytes, 0x00020000u}; }
DI void dma4(u32x4 sd, u32 v0, u32 v1, u32 soff, u32 ldsa) {
  u32 keep;
  asm volatile("s_mov_b32 %0, m0\n\ts_mov_b32 m0, %1\n\ts_nop 0\n\t"
               "buffer_load_dwordx4 %2, %4, %5 offen lds\n\tbuffer_load_dwordx4 %2, %4, %5 offen offset:1024 lds\n\t"
               "buffer_load_dwordx4 %3, %4, %5 offen offset:2048 lds\n\tbuffer_load_dwordx4 %3, %4, %5 offen offset:3072 lds\n\t"
               "s_mov_b32 m0, %0"
               : "=&s"(keep) : "s"(RFL(ldsa)), "v"(v0), "v"(v1), "s"(sd), "s"(soff) : "memory");
}
DI void dma2(u32x4 sd, u32 v0, u32 soff, u32 ldsa) {
  u32 keep;
  asm volatile("s_mov_b32 %0, m0\n\ts_mov_b32 m0, %1\n\ts_nop 0\n\t"
               "buffer_load_dwordx4 %2, %3, %4 offen lds\n\tbuffer_load_dwordx4 %2, %3, %4 offen offset:1024 lds\n\t"
               "s_mov_b32 m0, %0"
               : "=&s"(keep) : "s"(RFL(ldsa)), "v"(v0), "s"(sd), "s"(soff) : "memory");
}
DI u32 scan64(u32 x) {
  x += __builtin_amdgcn_update_dpp(0u, x, 0x111, 0xf, 0xf, false);
  x += __builtin_amdgcn_update_dpp(0u, x, 0x112, 0xf, 0xf, false);
  x += __builtin_amdgcn_update_dpp(0u, x, 0x114, 0xf, 0xf, false);
  x += __builtin_amdgcn_update_dpp(0u, x, 0x118, 0xf, 0xf, false);
  x += __builtin_amdgcn_update_dpp(0u, x, 0x142, 0xa, 0xf, false);
  x += __builtin_amdgcn_update_dpp(0u, x, 0x143, 0xc, 0xf, false);
  return x;
}

constexpr int L_ZERO = 0;
constexpr int L_CNT = L_ZERO + 16;
constexpr int L_BC = L_CNT + 48;
constexpr int L_SY = L_BC + 1024;
constexpr int L_SM = L_SY + 32;
constexpr int L_CF = L_SM + 8 * 8 * 640;
constexpr int L_SRC = L_CF + 8 * CAPW * 16;
constexpr int L_KB = L_SRC + 8 * CAPW * 4 + 64;
constexpr int L_W1 = L_KB + 8 * D * 16;
constexpr int L_W0 = L_W1 + 32768;
constexpr int L_HS = L_W0 + 32768;
constexpr int L_WX = L_HS + NPB * 256;
constexpr int L_PD = L_WX + 256;
constexpr int L_LS = L_PD + 256;
constexpr int L_END = L_LS + 8 * 768;
constexpr int L_RS = L_W0 - CH * 16;
constexpr int L_POS = L_RS - MAXPOS;
static_assert(L_END <= 160 * 1024, "lds");
static_assert(L_POS >= L_SM + 36864 && L_POS <= L_CF && (L_POS % 16) == 0, "pos table");
static_assert((L_SM % 16) == 0 && (L_BC % 16) == 0 && (L_SRC % 16) == 0 && (L_KB % 16) == 0 && (L_W0 % 16) == 0 && (L_HS % 16) == 0 && (L_WX % 16) == 0 && (L_LS % 16) == 0, "align");

DI int tile_node(int t, int c) { return t < 2 ? ((c & 3) * 8 + 4 * t + (c >> 2)) : (32 + (c & 7)); }
DI int mi_row(int nl) { return L_KB + (nl & 7) * 2048 + (nl & 3) * 64 + (nl >> 3) * 272; }
DI int wswz(int k) { return ((k & 3) << 2) | ((k >> 2) & 2); }
DI int hswz(int n) { return ((n & 3) << 2) | ((n >> 3) & 3); }
DI u32x4 tr_frag(const char* img, int s, int lane, int ch, int hf) {
  const int k0 = 8 * (lane >> 4) + ((lane & 15) >> 2);
  const char* p = img + (32 * s + k0) * 256 + 16 * (ch ^ wswz(k0)) + 8 * hf;
  const s16x4 a = __builtin_amdgcn_ds_read_tr16_b64_v4i16((lds_s16x4*)p);
  const s16x4 b = __builtin_amdgcn_ds_read_tr16_b64_v4i16((lds_s16x4*)(p + 1024));
  const u32x2 x = __builtin_bit_cast(u32x2, a), y = __builtin_bit_cast(u32x2, b);
  return u32x4{x[0], x[1], y[0], y[1]};
}
DI u32x4 s_frag(const char* p) {
  const u32x2 x = __builtin_bit_cast(u32x2, __builtin_amdgcn_ds_read_tr16_b64_v4i16((lds_s16x4*)p));
  const u32x2 y = __builtin_bit_cast(u32x2, __builtin_amdgcn_ds_read_tr16_b64_v4i16((lds_s16x4*)(p + 160)));
  return u32x4{x[0], x[1], y[0], y[1]};
}
DI u32x4 h_frag(const char* lds, int t, int s, int lane) {
  const int n = tile_node(t, lane & 15);
  return *(const u32x4*)(lds + L_HS + n * 256 + 16 * ((4 * s + (lane >> 4)) ^ hswz(n)));
}
DI u32 dist2(u32 w2, u32 w3) {
  const float dx = bf2f(w2), dy = bfhi(w2), dz = bf2f(w3);
  const u32 q0 = pk_bf16(dx * dx, dy * dy), q1 = pk_bf16(dz * dz, 0.f);
  const float s2 = bf2f(q0) + bfhi(q0) + bf2f(q1);
  return pk_bf16(s2, s2);
}
DI u32 take_rank(char* lds, u32x4 rc) { return atomicAdd((u32*)(lds + L_CNT) + ((rc[0] >> 14) & 7u), 1u); }
DI void put_rec(char* lds, u32x4 rc, u32 rank) {
  const u32 win = (rc[0] >> 14) & 7u, slot = (rc[0] >> 17) & 7u;
  const u64 oh = 0x3f80ull << (slot * 16);
  const u32x4 cf = {slot < 4 ? (u32)oh : 0u, slot < 4 ? (u32)(oh >> 32) : 0u, (slot == 4 ? 0x3f80u : 0u) | (rc[1] << 16), dist2(rc[2], rc[3])};
  if (rank < CAPW) {
    const u32 k = ((rank & 1u) << 3) + ((rank & 15u) >> 1), sp = win * CAPW + (rank & ~15u) + k;
    *(u32*)(lds + L_SRC + sp * 4) = (rc[0] & 0x3fffu) << 8;
    *(u32x4*)(lds + L_CF + sp * 16) = cf;
    *(u32x2*)(lds + L_SM + sp * 40 + slot * 8) = u32x2{0x3f80u | (rc[2] << 16), __builtin_amdgcn_alignbit(rc[3], rc[2], 16)};
  } else *(u32*)(lds + L_CNT + 32) = 1;
}
DI void rec_tags(u64 tag, u32 (&t)[3]) {
  t[0] = (u32)tag << 20; t[1] = ((u32)tag >> 12) << 16; t[2] = ((u32)(tag >> 28) ^ (u32)tag) << 16;
}
DI bool rec_stale(u32x4 rc, const u32 (&t)[3]) { return (((rc[0] ^ t[0]) >> 20) | (((rc[1] ^ t[1]) | (rc[3] ^ t[2])) >> 16)) != 0; }
DI u32x4 settle(u32x4 rc, bool on, __amdgpu_buffer_rsrc_t r, u32 off, const u32 (&t)[3]) {
  for (u32 spins = 0; __any(on && rec_stale(rc, t)) && spins < SPINS; spins++) {
    __builtin_amdgcn_s_sleep(4);
    if (on && rec_stale(rc, t)) rc = __builtin_amdgcn_raw_buffer_load_b128(r, off, 0, SC1);
  }
  return rc;
}
DI void arrive(u32 lb, int i) {
  asm volatile("s_mov_b64 exec, 1\n\tds_add_u32 %0, %1 offset:%2\n\ts_mov_b64 exec, -1" :: "v"(lb), "v"(1u), "i"(L_SY + 4 * i) : "memory");
}
DI void await(char* lds, int i, u32 n) {
  asm volatile("" ::: "memory");
  while ((u32)RFL(__hip_atomic_load((u32*)(lds + L_SY) + i, __ATOMIC_RELAXED, __HIP_MEMORY_SCOPE_WORKGROUP)) < n) { }
  asm volatile("" ::: "memory");
}
DI u32 silu2(float a, float b) {
  f32x2 yy = {a, b};
  f32x2 e = {__builtin_amdgcn_exp2f(-a), __builtin_amdgcn_exp2f(-b)};
  e += 1.0f;
  f32x2 rc = {__builtin_amdgcn_rcpf(e[0]), __builtin_amdgcn_rcpf(e[1])};
  yy *= rc;
  return pk_bf16(yy[0], yy[1]);
}

__global__ __launch_bounds__(NT, 2) void k_egnn(const u16* __restrict__ h, const u16* __restrict__ pos,
                                                const u16* __restrict__ ea, const int* __restrict__ src,
                                                const int* __restrict__ dst, const u16* __restrict__ w_e,
                                                const u16* __restrict__ w_x, const u16* __restrict__ w_h,
                                                char* __restrict__ ws, u16* __restrict__ out, int N, int E, int G) {
  __shared__ __attribute__((aligned(16))) char lds[L_END];
  const int tid = threadIdx.x, lane = tid & 63, b = blockIdx.x;
  const int wv = RFL(tid >> 6);
  const int u = wv & 3, c16 = lane & 15, g4 = lane >> 4;
  const bool role1 = wv >= 4;
  const u32 lb = (u32)(size_t)(lds_char*)lds;
  if (tid < L_SM / 4) *(u32*)(lds + tid * 4) = 0;
  __syncthreads();
  const int ne = RFL(max(0, min(CH, E - b * CH)));
  u32 ed[3], es[3], ee[3];
  u32x2 epoch;
  {
    const u32x4 s_d = srd(dst + b * CH, ne * 4), s_s = srd(src + b * CH, ne * 4), s_e = srd(ea + b * CH, ne * 2);
    asm volatile("s_cmp_lt_u32 %15, 4\n\ts_cbranch_scc1 1f\n\t"
                 "buffer_load_dword %0, %10, %12, 0 offen\n\tbuffer_load_dword %1, %10, %12, 0 offen offset:1024\n\t"
                 "buffer_load_dword %2, %10, %12, 0 offen offset:2048\n\tbuffer_load_dword %3, %10, %13, 0 offen\n\t"
                 "buffer_load_dword %4, %10, %13, 0 offen offset:1024\n\tbuffer_load_dword %5, %10, %13, 0 offen offset:2048\n\t"
                 "buffer_load_ushort %6, %11, %14, 0 offen\n\tbuffer_load_ushort %7, %11, %14, 0 offen offset:512\n\t"
                 "buffer_load_ushort %8, %11, %14, 0 offen offset:1024\n"
                 "1:\n\tglobal_load_dwordx2 %9, %16, %17 sc1"
                 : "=&v"(ed[0]), "=&v"(ed[1]), "=&v"(ed[2]), "=&v"(es[0]), "=&v"(es[1]), "=&v"(es[2]), "=&v"(ee[0]), "=&v"(ee[1]), "=&v"(ee[2]), "=&v"(epoch)
                 : "v"((tid & 255) * 4), "v"((tid & 255) * 2), "s"(s_d), "s"(s_s), "s"(s_e), "s"(wv), "v"(0u), "s"(ws + WS_EP) : "memory", "scc");
  }
  int bk;
  { const int q = G >> 3, r = G & 7, x = b & 7, i = b >> 3;
    bk = (x < r ? x * (q + 1) : r * (q + 1) + (x - r) * q) + i; }
  const int nb0 = bk * NPB;
  const int r = lane & 31, hh = lane >> 5;
  const int rh = (r >> 2) & 1, rp = r >> 4, rj = (r & 3) | (((r >> 3) & 1) << 2);
  f32x16 aA0, aA1, aB0, aB1, zero;
  u32x4 sel0, sel1;
  auto setup = [&]() {
#pragma unroll
    for (int i = 0; i < 16; i++) zero[i] = 0.f;
    u32x4 z4 = {0, 0, 0, 0};
    PIN(z4);
    aA0 = mfma32(z4, z4, zero); aA1 = mfma32(z4, z4, zero); aB0 = mfma32(z4, z4, zero); aB1 = mfma32(z4, z4, zero);
    const u32 one = rh == hh ? 0x3f80u << (rp * 16) : 0u;
#pragma unroll
    for (int q = 0; q < 4; q++) { sel0[q] = (rj == q) ? one : 0u; sel1[q] = (rj == 4 + q) ? one : 0u; }
    PIN(aA0); PIN(aA1); PIN(aB0); PIN(aB1); PIN(sel0); PIN(sel1);
  };
  u32 ep_lo, ep_hi;
  u32x4 hb[3][4];
  const u32 wv0 = g4 * 256 + 16 * (c16 ^ (g4 << 2)) + u * 4096, wv1 = wv0 ^ 32;
  if (!role1) {
    const u32x4 s_pos = srd(pos, N * 6), s_h = srd(h, N * 256), s_we = srd(w_e, 258 * 256), s_wh = srd(w_h, 65536);
#pragma unroll
    for (int i = 0; i < 4; i++)
      if (i < 3 || u < 3) dma4(s_pos, lane * 16 + u * 4096, lane * 16 + u * 4096, i * 16384, lb + L_POS + i * 16384 + u * 4096);
    dma2(s_h, g4 * 256 + 16 * (c16 ^ ((g4 << 2) | u)) + u * 2048, nb0 * 256, lb + L_HS + u * 2048);
    if (u == 0) dma2(s_h, g4 * 256 + 16 * (c16 ^ (g4 << 2)) + 8192, nb0 * 256, lb + L_HS + 8192);
    dma4(s_we, wv0, wv1, 32768, lb + L_W0 + u * 4096);
    dma4(s_we, wv0, wv1, 32768 + 16384, lb + L_W0 + 16384 + u * 4096);
    u32 wdv, wav, wxv, pf1, pf2;
    {
      const u32x4 s_wx = srd(w_x, 256);
      const u32 xi = (b >> 3) & 31;
      asm volatile("buffer_load_ushort %0, %5, %6, %7 offen\n\tbuffer_load_ushort %1, %5, %6, %7 offen offset:256\n\t"
                   "buffer_load_ushort %2, %5, %8, 0 offen\n\tbuffer_load_dword %3, %9, %6, 0 offen\n\t"
                   "buffer_load_dword %4, %10, %11, 0 offen"
                   : "=&v"(wdv), "=&v"(wav), "=&v"(wxv), "=&v"(pf1), "=&v"(pf2)
                   : "v"((tid & 127) * 2), "s"(s_we), "s"(65536u), "s"(s_wx), "v"(tid < 8 ? (xi * 8 + tid) * 128 : OOB),
                     "v"(tid - 64 < 16u ? (xi * 16 + tid - 64) * 128 : OOB), "s"(s_wh) : "memory");
    }
    asm volatile("s_waitcnt vmcnt(15)\n\tv_readfirstlane_b32 %0, %2\n\tv_readfirstlane_b32 %1, %3\n\ts_nop 1"
                 : "=s"(ep_lo), "=s"(ep_hi) : "v"(epoch[0]), "v"(epoch[1]) : "memory");
    arrive(lb, 0);
    setup();
    asm volatile("s_waitcnt vmcnt(5)" ::: "memory");
    arrive(lb, 1);
    char* const slot = ws + WS_SLOT0 + (size_t)(RFL(ep_lo) & 3u) * WS_SLOT;
    await(lds, 1, 4);
#pragma unroll
    for (int s = 0; s < 4; s++)
#pragma unroll
      for (int t = 0; t < 3; t++) hb[t][s] = h_frag(lds, t, s, lane);
    f32x4 acc[3][2];
#pragma unroll
    for (int t = 0; t < 3; t++) { acc[t][0] = f32x4{0, 0, 0, 0}; acc[t][1] = f32x4{0, 0, 0, 0}; }
#pragma unroll
    for (int s = 0; s < 4; s++) {
      const u32x4 fa = tr_frag(lds + L_W0, s, lane, 4 * u + (lane & 3), 0), fb = tr_frag(lds + L_W0, s, lane, 4 * u + (lane & 3), 1);
#pragma unroll
      for (int t = 0; t < 3; t++) { acc[t][0] = mfma16(fa, hb[t][s], acc[t][0]); acc[t][1] = mfma16(fb, hb[t][s], acc[t][1]); }
    }
    asm volatile("s_waitcnt vmcnt(0)" : "+v"(wdv), "+v"(wav), "+v"(wxv), "+v"(pf1), "+v"(pf2), "+v"(acc[2][1]) :: "memory");
    arrive(lb, 3);
    const __amdgpu_buffer_rsrc_t r_bw = rsrc(slot + WS_BT, N * 256);
#pragma unroll
    for (int t = 0; t < 3; t++) {
      const f32x4 x = acc[t][0] * LOG2E, y = acc[t][1] * LOG2E;
      const u32x4 o = {pk_bf16(x[0], x[1]), pk_bf16(x[2], x[3]), pk_bf16(y[0], y[1]), pk_bf16(y[2], y[3])};
      __builtin_amdgcn_raw_buffer_store_b128(o, r_bw, (t < 2 || c16 < 8) ? (nb0 + tile_node(t, c16)) * 256 + u * 64 + g4 * 16 : OOB, 0, SC1);
    }
    await(lds, 3, 4);
    dma4(s_wh, wv0, wv1, 0, lb + L_W0 + u * 4096);
    dma4(s_wh, wv0, wv1, 16384, lb + L_W0 + 16384 + u * 4096);
    await(lds, 4, 4);
    dma4(s_we, wv0, wv1, 0, lb + L_W1 + u * 4096);
    dma4(s_we, wv0, wv1, 16384, lb + L_W1 + 16384 + u * 4096);
    {
      const float wd = bf2f(wdv) * LOG2E;
      const u32 wdh = pk_bf16(wd, 0.f);
      const u32 wdp = wdh | (pk_bf16(wd - bf2f(wdh), 0.f) << 16), wap = pk_bf16(bf2f(wav) * LOG2E, 0.f);
#pragma unroll
      for (int i = 0; i < 4; i++) {
        char* kb = lds + L_KB + (i * 256 + tid) * 16;
        *(u32*)(kb + 12) = wdp;
        *(u16*)(kb + 10) = (u16)wap;
      }
      if (tid < D) *(u16*)(lds + L_WX + tid * 2) = (u16)wxv;
    }
    asm volatile("s_waitcnt vmcnt(0)" ::: "memory");
    if (lane == 0 && __hip_atomic_fetch_add((u32*)(lds + L_SY) + 5, 1u, __ATOMIC_RELAXED, __HIP_MEMORY_SCOPE_WORKGROUP) == 3)
      __hip_atomic_store((gu64*)(slot + WS_FL) + b, ((((u64)ep_hi << 32) | ep_lo) + 1) & 0xfffffffffffull, RLX_AGENT);
  } else {
    const int t1 = tid - 256;
    __builtin_amdgcn_s_setprio(2);
    const u32x4 z = {0, 0, 0, 0};
    setup();
    asm volatile("s_waitcnt vmcnt(7)" : "+v"(ed[0]), "+v"(ed[1]), "+v"(ed[2]) :: "memory");
    u32 rank[3], ebk[3];
#pragma unroll
    for (int i = 0; i < 3; i++) {
      ebk[i] = ed[i] / NPB;
      rank[i] = 0;
      if (t1 + 256 * i < ne) rank[i] = atomicAdd((u32*)(lds + L_BC) + ebk[i], 1u);
    }
    arrive(lb, 0);
    asm volatile("s_waitcnt vmcnt(0)\n\tv_readfirstlane_b32 %0, %8\n\tv_readfirstlane_b32 %1, %9\n\ts_nop 1"
                 : "=s"(ep_lo), "=s"(ep_hi), "+v"(es[0]), "+v"(es[1]), "+v"(es[2]), "+v"(ee[0]), "+v"(ee[1]), "+v"(ee[2])
                 : "v"(epoch[0]), "v"(epoch[1]) : "memory");
    ep_lo = RFL(ep_lo); ep_hi = RFL(ep_hi);
    char* const slot = ws + WS_SLOT0 + (size_t)(ep_lo & 3u) * WS_SLOT;
    const u64 tag = ((((u64)ep_hi << 32) | ep_lo) + 1) & 0xfffffffffffull;
    u32 tg[3];
    rec_tags(tag, tg);
    await(lds, 0, 8);
    const u32x4 c4 = *(const u32x4*)(lds + L_BC + lane * 16);
    const u32 cown = *(const u32*)(lds + L_BC + t1 * 4);
    u32x2 pd[3], ps[3];
#pragma unroll
    for (int i = 0; i < 3; i++) {
      const u32* qd = (const u32*)(lds + L_POS + ((ed[i] * 6) & ~3u));
      const u32* qs = (const u32*)(lds + L_POS + ((es[i] * 6) & ~3u));
      pd[i] = u32x2{qd[0], qd[1]}; ps[i] = u32x2{qs[0], qs[1]};
    }
    const u32 pown = *(const u16*)(lds + L_POS + min((nb0 * 3 + t1) * 2, MAXPOS - 2));
    const u32 tot = c4[0] + c4[1] + c4[2] + c4[3];
    const u32 ex = scan64(tot) - tot;
    u32* mine = (u32*)(lds + L_LS + u * 1024);
    *(u32x4*)(mine + lane * 4) = u32x4{ex, ex + c4[0], ex + c4[0] + c4[1], ex + c4[0] + c4[1] + c4[2]};
    __builtin_amdgcn_wave_barrier();
    u32 at[3];
#pragma unroll
    for (int i = 0; i < 3; i++) at[i] = L_RS + (mine[ebk[i]] + rank[i]) * 16;
    const u32 gword = (mine[t1] << 10) | cown;
#pragma unroll
    for (int i = 0; i < 3; i++) {
      const u32 shd = (ed[i] & 1u) * 16, shs = (es[i] & 1u) * 16;
      const u32 dxy = __builtin_amdgcn_alignbit(pd[i][1], pd[i][0], shd), sxy = __builtin_amdgcn_alignbit(ps[i][1], ps[i][0], shs);
      const u32 xy = pk_bf16(bf2f(dxy) - bf2f(sxy), bfhi(dxy) - bfhi(sxy));
      const u32 zz = pk_bf16(bf2f(pd[i][1] >> shd) - bf2f(ps[i][1] >> shs), 0.f);
      u32x4 rc = {es[i] | ((ed[i] - ebk[i] * NPB) << 14) | tg[0], ee[i] | tg[1], xy, zz | tg[2]};
      PIN(rc);
      if (t1 + 256 * i < ne) *(u32x4*)(lds + at[i]) = rc;
    }
    if (t1 < NPB * 3) *(u16*)(lds + L_PD + t1 * 2) = (u16)pown;
    arrive(lb, 2);
    await(lds, 2, 4);
    const __amdgpu_buffer_rsrc_t r_rw = rsrc(slot + WS_REC + (size_t)b * CH * 16, CH * 16);
    u32x4 rv[3];
#pragma unroll
    for (int i = 0; i < 3; i++) rv[i] = *(const u32x4*)(lds + L_RS + (t1 + 256 * i) * 16);
#pragma unroll
    for (int i = 0; i < 3; i++) __builtin_amdgcn_raw_buffer_store_b128(rv[i], r_rw, t1 * 16, i * 4096, SC1);
    arrive(lb, 4);
    __hip_atomic_store((gu64*)(slot + WS_GR) + (size_t)b * MAXG + t1, (tag << 20) | gword, RLX_AGENT);
    __builtin_amdgcn_s_setprio(0);
#pragma unroll
    for (int i = 0; i < 10; i++) if (i < 9 || L_SM + i * 4096 + t1 * 16 < L_POS) *(u32x4*)(lds + L_SM + i * 4096 + t1 * 16) = z;
#pragma unroll
    for (int i = 0; i < 5; i++) { const int o = L_POS + i * 4096 + t1 * 16; if (i < 4 || o < L_SRC) *(u32x4*)(lds + o) = z; }
    *(u32x4*)(lds + L_SRC + t1 * 16) = u32x4{OOB, OOB, OOB, OOB};
    if (t1 < 16) *(u32*)(lds + L_SRC + 4096 + t1 * 4) = OOB;
    await(lds, 1, 4);
#pragma unroll
    for (int s = 0; s < 4; s++)
#pragma unroll
      for (int t = 0; t < 3; t++) hb[t][s] = h_frag(lds, t, s, lane);
  }
  __syncthreads();
  ep_lo = RFL(ep_lo); ep_hi = RFL(ep_hi);
  const u64 ep = ((u64)ep_hi << 32) | ep_lo, tag = (ep + 1) & 0xfffffffffffull;
  char* const slot = ws + WS_SLOT0 + (size_t)(ep_lo & 3u) * WS_SLOT;
  const __amdgpu_buffer_rsrc_t r_bt = rsrc(slot + WS_BT, N * 256), r_rec = rsrc(slot + WS_REC, G * CH * 16);
  u32 tg[3];
  rec_tags(tag, tg);
  f32x4 cp[3];
  u32 pw[4] = {0, 0, 0, 0};
  {
    const int cs = wv * 32 + (lane & 31), csc = cs < G ? cs : G - 1;
    gu64* const gp = (gu64*)(slot + WS_GR) + (size_t)csc * MAXG + bk;
    gu64* const fp = (gu64*)(slot + WS_FL) + csc;
    u64 gv = __hip_atomic_load(gp, RLX_AGENT);
    SB();
    {
      f32x4 acc[3] = {f32x4{0, 0, 0, 0}, f32x4{0, 0, 0, 0}, f32x4{0, 0, 0, 0}};
#pragma unroll
      for (int s = 0; s < 4; s++) {
        const u32x4 fw = tr_frag(lds + L_W1, s, lane, 2 * wv + ((lane & 3) >> 1), lane & 1);
#pragma unroll
        for (int t = 0; t < 3; t++) acc[t] = mfma16(hb[t][s], fw, acc[t]);
      }
      char* kb = lds + L_KB + (16 * wv + c16) * 16;
#pragma unroll
      for (int t = 0; t < 2; t++) {
        const f32x4 a = acc[t] * LOG2E;
        *(u32x2*)(kb + (4 * t + g4) * 2048) = u32x2{pk_bf16(a[0], a[1]), pk_bf16(a[2], a[3])};
      }
      if (g4 < 2) {
#pragma unroll
        for (int i = 0; i < 4; i++) *(u16*)(kb + (4 * g4 + i) * 2048 + 8) = (u16)pk_bf16(acc[2][i] * LOG2E, 0.f);
      }
    }
    SB();
    for (u32 spins = 0; !__all((gv >> 20) == tag) && spins < SPINS; spins++) {
      __builtin_amdgcn_s_sleep(4);
      if ((gv >> 20) != tag) gv = __hip_atomic_load(gp, RLX_AGENT);
    }
    const bool on = lane < 32 && cs < G;
    const u32 cnt = on ? ((u32)gv & 1023u) : 0u, st = ((u32)gv >> 10) & 1023u;
    const u32 inc = scan64(cnt);
    const u32 total = __builtin_amdgcn_readlane(inc, 63);
    u32* list = (u32*)(lds + L_LS + wv * 768);
    const u32 base = (cs * CH + st) * 16, p0 = inc - cnt;
    const u32 lim = min(cnt, p0 < 128 ? 128 - p0 : 0u);
#pragma unroll
    for (u32 k = 0; k < 6; k++) list[k < lim ? p0 + k : 128 + lane] = base + k * 16;
    if (__any(lim > 6)) {
#pragma unroll 1
      for (u32 k = 6; k < lim; k++) list[p0 + k] = base + k * 16;
    }
    __builtin_amdgcn_wave_barrier();
    {
      const u32 a0 = list[lane], a1 = list[64 + lane];
      const bool v0 = lane < total, v1 = 64 + lane < total;
      u32x4 r0 = __builtin_amdgcn_raw_buffer_load_b128(r_rec, v0 ? a0 : OOB, 0, SC1);
      u32x4 r1 = __builtin_amdgcn_raw_buffer_load_b128(r_rec, v1 ? a1 : OOB, 0, SC1);
      u64 fv = __hip_atomic_load(fp, RLX_AGENT);
      if (on) *(u32*)(lds + L_BC + cs * 4) = (u32)gv & 0xfffffu;
      SB();
#pragma unroll
      for (int t = 0; t < 3; t++) cp[t] = f32x4{0, 0, 0, 0};
#pragma unroll
      for (int s = 0; s < 4; s++) {
        const u32x4 f1 = tr_frag(lds + L_W0, s, lane, 2 * wv + ((lane & 3) >> 1), lane & 1);
#pragma unroll
        for (int t = 0; t < 3; t++) cp[t] = mfma16(f1, hb[t][s], cp[t]);
      }
#pragma unroll
      for (int t = 0; t < 3; t++) { cp[t] *= LOG2E; PIN(cp[t]); }
      SB();
      r0 = settle(r0, v0, r_rec, a0, tg);
      r1 = settle(r1, v1, r_rec, a1, tg);
      u32 k0 = 0, k1 = 0;
      if (v0) k0 = take_rank(lds, r0);
      if (v1) k1 = take_rank(lds, r1);
      for (u32 spins = 0; !__all(fv == tag) && spins < SPINS; spins++) {
        __builtin_amdgcn_s_sleep(4);
        fv = __hip_atomic_load(fp, RLX_AGENT);
      }
      __builtin_amdgcn_s_waitcnt(0x0f70);
      pw[0] = __builtin_amdgcn_raw_buffer_load_b32(r_bt, v0 && k0 < PFR ? (r0[0] & 0x3fffu) << 8 : OOB, 0, SC1);
      pw[1] = __builtin_amdgcn_raw_buffer_load_b32(r_bt, v0 && k0 < PFR ? ((r0[0] & 0x3fffu) << 8) + 128 : OOB, 0, SC1);
      pw[2] = __builtin_amdgcn_raw_buffer_load_b32(r_bt, v1 && k1 < PFR ? (r1[0] & 0x3fffu) << 8 : OOB, 0, SC1);
      pw[3] = __builtin_amdgcn_raw_buffer_load_b32(r_bt, v1 && k1 < PFR ? ((r1[0] & 0x3fffu) << 8) + 128 : OOB, 0, SC1);
      SB();
      if (v0) put_rec(lds, r0, k0);
      if (v1) put_rec(lds, r1, k1);
    }
    if (__builtin_expect(total > 128, 0)) {
#pragma unroll 1
      for (u32 done = 128; done < total; done += 64) {
        if (__hip_atomic_load((u32*)(lds + L_CNT + 32), __ATOMIC_RELAXED, __HIP_MEMORY_SCOPE_WORKGROUP)) break;
        __builtin_amdgcn_wave_barrier();
#pragma unroll 1
        for (u32 k = p0 < done ? done - p0 : 0u; k < cnt && p0 + k < done + 64; k++) list[p0 + k - done] = base + k * 16;
        __builtin_amdgcn_wave_barrier();
        const bool v0 = done + lane < total;
        const u32 a0 = list[lane];
        u32x4 r0 = __builtin_amdgcn_raw_buffer_load_b128(r_rec, v0 ? a0 : OOB, 0, SC1);
        r0 = settle(r0, v0, r_rec, a0, tg);
        if (v0) put_rec(lds, r0, take_rank(lds, r0));
      }
      __builtin_amdgcn_s_waitcnt(0x0f70);
    }
  }
  __syncthreads();
  const u32* wcnt = (const u32*)(lds + L_CNT);
  const u32 ci = wcnt[lane >> 3], cj = wcnt[lane & 7];
  const int ovf = RFL(wcnt[8]);
  auto load_wh2 = [&]() {
    const u32x4 s_wh = srd(w_h, 65536);
    dma4(s_wh, wv0 + (wv >> 2) * 16384, wv1 + (wv >> 2) * 16384, 32768, lb + L_W0 + wv * 4096);
  };
  u32x4 v[8], wxf[8];
  int win;
  {
    const u64 gt = __ballot(cj * 8 + (lane & 7) > ci * 8 + (lane >> 3));
    const u32 rk = __builtin_popcount((u32)(gt >> (lane & 56)) & 0xffu);
    const int wl = __builtin_ctzll(__ballot(rk == (u32)(wv < 4 ? wv : 11 - wv)));
    win = wl >> 3;
    const int cntw = ovf ? 1 : __builtin_amdgcn_readlane(ci, wl);
    const int nsteps = (cntw + 15) >> 4;
    const u32 lo = r * 4;
    u32 srca = L_SRC + win * CAPW * 4 + hh * 32;
    u32 GA[8], GB[8], off[8];
    const bool cfon = rp == hh;
    u32 cfa = cfon ? (L_CF + (win * CAPW + 8 * rh + rj) * 16) : L_ZERO;
    const u32 cfs = cfon ? 256u : 0u;
    u32 sma = L_SM + (win * CAPW + 8 * hh + (c16 >> 2)) * 40 + (g4 & 1) * 32 + (lane & 3) * 8;
    u32x4 cf, kbA, kbB, sf = {0, 0, 0, 0}, mB0 = {0, 0, 0, 0}, mB1 = {0, 0, 0, 0}, mA0, mA1;
    f32x16 yA, yB;
    {
      const u32x4 i0 = *(const u32x4*)(lds + srca), i1 = *(const u32x4*)(lds + srca + 16);
      const u32x4 i2 = *(const u32x4*)(lds + srca + 64), i3 = *(const u32x4*)(lds + srca + 80);
      cf = *(const u32x4*)(lds + cfa);
      kbA = *(const u32x4*)(lds + L_KB + win * 2048 + (2 * r + hh) * 16);
      kbB = *(const u32x4*)(lds + L_KB + win * 2048 + (64 + 2 * r + hh) * 16);
      srca += 128;
      SB();
#pragma unroll
      for (int j = 0; j < 4; j++) { off[j] = i0[j] + lo; off[4 + j] = i1[j] + lo; }
#pragma unroll
      for (int j = 0; j < 8; j++) GA[j] = __builtin_amdgcn_raw_buffer_load_b32(r_bt, off[j], 0, SC1);
      SB();
#pragma unroll
      for (int j = 0; j < 8; j++) GB[j] = __builtin_amdgcn_raw_buffer_load_b32(r_bt, off[j] + 128, 0, SC1);
      SB();
      asm volatile("" :: "v"(pw[0]), "v"(pw[1]), "v"(pw[2]), "v"(pw[3]));
      yA = mfma32(cf, kbA, zero);
#pragma unroll
      for (int j = 0; j < 4; j++) { off[j] = i2[j] + lo; off[4 + j] = i3[j] + lo; }
      yA = mfma32(sel0, u32x4{GA[0], GA[1], GA[2], GA[3]}, yA);
      yA = mfma32(sel1, u32x4{GA[4], GA[5], GA[6], GA[7]}, yA);
      PIN(yA);
      SB();
#pragma unroll
      for (int j = 0; j < 8; j++) GA[j] = __builtin_amdgcn_raw_buffer_load_b32(r_bt, off[j], 0, SC1);
      SB();
    }
#define MF(d, a, bb, c) do { SB(); d = mfma32(a, bb, c); PIN(d); SB(); } while (0)
#define SA(y, i, j) do { p_ = f32x2{__builtin_amdgcn_exp2f(-y[i]), __builtin_amdgcn_exp2f(-y[(i) + 1])}; \
                         q_ = f32x2{__builtin_amdgcn_exp2f(-y[j]), __builtin_amdgcn_exp2f(-y[(j) + 1])}; SB(); p_ += 1.0f; q_ += 1.0f; SB(); } while (0)
#define SBB(y, i, j, o0, o1) do { p_ = f32x2{__builtin_amdgcn_rcpf(p_[0]), __builtin_amdgcn_rcpf(p_[1])}; \
                         q_ = f32x2{__builtin_amdgcn_rcpf(q_[0]), __builtin_amdgcn_rcpf(q_[1])}; SB(); \
                         p_ *= f32x2{y[i], y[(i) + 1]}; q_ *= f32x2{y[j], y[(j) + 1]}; SB(); \
                         u32 t0_ = pk_bf16(p_[0], p_[1]), t1_ = pk_bf16(q_[0], q_[1]); \
                         asm volatile("" : "+v"(t0_), "+v"(t1_)); o0 = t0_; o1 = t1_; SB(); } while (0)
#define S4(y, i, j, o0, o1) do { SA(y, i, j); SBB(y, i, j, o0, o1); } while (0)
#define LD2(Gx, j, add) do { Gx[j] = __builtin_amdgcn_raw_buffer_load_b32(r_bt, off[j] + (add), 0, SC1); \
                             Gx[(j) + 1] = __builtin_amdgcn_raw_buffer_load_b32(r_bt, off[(j) + 1] + (add), 0, SC1); SB(); } while (0)
    f32x2 p_, q_;
    for (int s = 1; s < nsteps; s++) {
      {
        const int left = nsteps - s;
        if (left >= 4) __builtin_amdgcn_s_setprio(3);
        else if (left == 3) __builtin_amdgcn_s_setprio(2);
        else if (left == 2) __builtin_amdgcn_s_setprio(1);
        else __builtin_amdgcn_s_setprio(0);
      }
      MF(yB, cf, kbB, zero);
      S4(yA, 0, 2, mA0[0], mA0[1]);
      MF(yB, sel0, (u32x4{GB[0], GB[1], GB[2], GB[3]}), yB);
      cfa += cfs;
      cf = *(const u32x4*)(lds + cfa);
      SA(yA, 4, 6);
      LD2(GB, 0, 128);
      SBB(yA, 4, 6, mA0[2], mA0[3]);
      LD2(GB, 2, 128);
      MF(yB, sel1, (u32x4{GB[4], GB[5], GB[6], GB[7]}), yB);
      SA(yA, 8, 10);
      LD2(GB, 4, 128);
      MF(aB0, mB0, sf, aB0);
      SBB(yA, 8, 10, mA1[0], mA1[1]);
      LD2(GB, 6, 128);
      const u32x4 i0 = *(const u32x4*)(lds + srca), i1 = *(const u32x4*)(lds + srca + 16);
      srca += 64;
      SA(yA, 12, 14);
      MF(aB1, mB1, sf, aB1);
      sf = s_frag(lds + sma);
      sma += 640;
      SBB(yA, 12, 14, mA1[2], mA1[3]);
      MF(yA, cf, kbA, zero);
      S4(yB, 0, 2, mB0[0], mB0[1]);
      MF(yA, sel0, (u32x4{GA[0], GA[1], GA[2], GA[3]}), yA);
#pragma unroll
      for (int j = 0; j < 4; j++) { off[j] = i0[j] + lo; off[4 + j] = i1[j] + lo; }
      SA(yB, 4, 6);
      LD2(GA, 0, 0);
      SBB(yB, 4, 6, mB0[2], mB0[3]);
      LD2(GA, 2, 0);
      MF(yA, sel1, (u32x4{GA[4], GA[5], GA[6], GA[7]}), yA);
      SA(yB, 8, 10);
      LD2(GA, 4, 0);
      MF(aA0, mA0, sf, aA0);
      SBB(yB, 8, 10, mB1[0], mB1[1]);
      LD2(GA, 6, 0);
      SA(yB, 12, 14);
      MF(aA1, mA1, sf, aA1);
      SBB(yB, 12, 14, mB1[2], mB1[3]);
    }
    {
      const int rem = cntw - 16 * (nsteps - 1);
      load_wh2();
      MF(yB, cf, kbB, zero);
      mA0 = u32x4{0, 0, 0, 0}; mA1 = u32x4{0, 0, 0, 0};
      S4(yA, 0, 8, mA0[0], mA1[0]);
      MF(yB, sel0, (u32x4{GB[0], GB[1], GB[2], GB[3]}), yB);
      if (rem > 4) S4(yA, 2, 10, mA0[1], mA1[1]);
      MF(yB, sel1, (u32x4{GB[4], GB[5], GB[6], GB[7]}), yB);
      if (rem > 8) S4(yA, 4, 12, mA0[2], mA1[2]);
      MF(aB0, mB0, sf, aB0);
      if (rem > 12) S4(yA, 6, 14, mA0[3], mA1[3]);
      MF(aB1, mB1, sf, aB1);
      sf = s_frag(lds + sma);
      mB0 = u32x4{0, 0, 0, 0}; mB1 = u32x4{0, 0, 0, 0};
      S4(yB, 0, 8, mB0[0], mB1[0]);
      MF(aA0, mA0, sf, aA0);
      if (rem > 4) S4(yB, 2, 10, mB0[1], mB1[1]);
      MF(aA1, mA1, sf, aA1);
      if (rem > 8) S4(yB, 4, 12, mB0[2], mB1[2]);
      if (rem > 12) S4(yB, 6, 14, mB0[3], mB1[3]);
      MF(aB0, mB0, sf, aB0);
      MF(aB1, mB1, sf, aB1);
    }
#pragma unroll
    for (int q8 = 0; q8 < 8; q8++) {
      const f32x16& a0 = q8 < 4 ? aA0 : aB0;
      const f32x16& a1 = q8 < 4 ? aA1 : aB1;
#pragma unroll
      for (int q = 0; q < 4; q++) v[q8][q] = pk_bf16(a0[4 * (q8 & 3) + q], a1[4 * (q8 & 3) + q]);
    }
    if (r < 20 && (r & 3) == 0 && !ovf) {
      char* mi = lds + mi_row(r * 2 + win) + 16 * hh;
#pragma unroll
      for (int q8 = 0; q8 < 8; q8++) *(u32x4*)(mi + 32 * q8) = v[q8];
    }
  }
  if (__builtin_expect(ovf, 0)) {
    float* macc = (float*)(lds + L_SM);
    float* pacc = (float*)(lds + L_SM + NPB * D * 4);
    __syncthreads();
    for (int i = tid; i < NPB * D + NPB * 4; i += NT) macc[i] = 0.f;
    __syncthreads();
    const u32 wxp = *(const u32*)(lds + L_WX + lane * 4);
    const float wx0 = bf2f(wxp), wx1 = bfhi(wxp);
    const u32x4 kc0 = *(const u32x4*)(lds + L_KB + (2 * lane) * 16), kc1 = *(const u32x4*)(lds + L_KB + (2 * lane + 1) * 16);
    const float wd0 = bf2f(kc0[3]) + bfhi(kc0[3]), wd1 = bf2f(kc1[3]) + bfhi(kc1[3]);
    const float wa0 = bfhi(kc0[2]), wa1 = bfhi(kc1[2]);
    u32 rot = 0;
    for (int c = 0; c < G; c++) {
      const u32 oo = RFL(*(const u32*)(lds + L_BC + c * 4));
      const u32 s0 = oo >> 10, n = oo & 1023u;
      for (u32 k = (wv - rot) & 7u; k < n; k += 8) {
        u32x4 rr = __builtin_amdgcn_raw_buffer_load_b128(r_rec, (c * CH + s0 + k) * 16, 0, SC1);
        rr = settle(rr, true, r_rec, (c * CH + s0 + k) * 16, tg);
        const u32 nl = (rr[0] >> 14) & 0x3fu, sl = nl >> 3, wn = nl & 7u;
        const u32 bv = __builtin_amdgcn_raw_buffer_load_b32(r_bt, ((rr[0] & 0x3fffu) << 8) + lane * 4, 0, SC1);
        const float d2 = bfhi(dist2(rr[2], rr[3])), eav = bf2f(rr[1]);
        const float a0 = bf2f(*(const u16*)(lds + L_KB + wn * 2048 + (2 * lane) * 16 + sl * 2));
        const float a1 = bf2f(*(const u16*)(lds + L_KB + wn * 2048 + (2 * lane + 1) * 16 + sl * 2));
        const float y0 = a0 + bf2f(bv) + d2 * wd0 + eav * wa0, y1 = a1 + bfhi(bv) + d2 * wd1 + eav * wa1;
        const u32 mp = silu2(y0, y1);
        const float m0 = bf2f(mp), m1 = bfhi(mp);
        atomicAdd(&macc[nl * D + 2 * lane], m0);
        atomicAdd(&macc[nl * D + 2 * lane + 1], m1);
        float csv = m0 * wx0 + m1 * wx1;
#pragma unroll
        for (int o = 32; o > 0; o >>= 1) csv += __shfl_xor(csv, o, 64);
        if (lane < 3) atomicAdd(&pacc[nl * 4 + lane], csv * (lane == 0 ? bf2f(rr[2]) : lane == 1 ? bfhi(rr[2]) : bf2f(rr[3])));
      }
      rot += n;
    }
    __syncthreads();
    for (int i = tid; i < NPB * D / 2; i += NT) {
      const int n = i >> 6, f = (i & 63) * 2;
      *(u32*)(lds + mi_row(n) + f * 2) = pk_bf16(macc[n * D + f], macc[n * D + f + 1]);
    }
    if (tid < NPB * 3) {
      const int n = tid / 3, c = tid - n * 3;
      if (nb0 + n < N) out[(size_t)N * D + (size_t)nb0 * 3 + tid] = (u16)pk_bf16(bf2f(*(const u16*)(lds + L_PD + tid * 2)) + LN2 * pacc[n * 4 + c], 0.f);
    }
  }
  u32x2 hr[3];
  u32x4 f2[4];
#pragma unroll
  for (int t = 0; t < 3; t++) {
    const int n = tile_node(t, c16);
    hr[t] = *(const u32x2*)(lds + L_HS + n * 256 + 16 * ((2 * wv + (g4 >> 1)) ^ hswz(n)) + 8 * (g4 & 1));
  }
#pragma unroll
  for (int q8 = 0; q8 < 8; q8++) wxf[q8] = *(const u32x4*)(lds + L_WX + (16 * q8 + 8 * hh) * 2);
  const int kind = lane & 3, pnl = (lane >> 2) * 8 + win;
  const bool pon = kind != 0 && lane < 20 && !ovf;
  const u32 p0 = *(const u16*)(lds + L_PD + (pon ? pnl * 6 + kind * 2 - 2 : 0));
  const int mia = mi_row(tile_node(0, c16)) + g4 * 16, mib = mi_row(tile_node(2, c16)) + g4 * 16;
  asm volatile("s_waitcnt vmcnt(0)" ::: "memory");
  __syncthreads();
  {
    u32x4 mb[3][4];
#pragma unroll
    for (int s = 0; s < 4; s++) {
      f2[s] = tr_frag(lds + L_W0, s, lane, 2 * wv + ((lane & 3) >> 1), lane & 1);
      mb[0][s] = *(const u32x4*)(lds + mia + s * 64);
      mb[1][s] = *(const u32x4*)(lds + mia + 8192 + s * 64);
      mb[2][s] = *(const u32x4*)(lds + mib + s * 64);
    }
    SB();
#pragma unroll
    for (int s = 0; s < 4; s++)
#pragma unroll
      for (int t = 0; t < 3; t++) cp[t] = mfma16(f2[s], mb[t][s], cp[t]);
    f32x16 z = zero, z1 = zero;
#pragma unroll
    for (int t = 0; t < 3; t++) {
      const f32x4 y = cp[t];
      f32x4 e;
#pragma unroll
      for (int i = 0; i < 4; i++) e[i] = __builtin_amdgcn_exp2f(-y[i]);
      SB();
      if (t & 1) { z1 = mfma32(wxf[3 * t], v[3 * t], z1); PIN(z1); } else { z = mfma32(wxf[3 * t], v[3 * t], z); PIN(z); }
      SB();
      e += 1.0f;
#pragma unroll
      for (int i = 0; i < 4; i++) e[i] = __builtin_amdgcn_rcpf(e[i]);
      SB();
      if (t & 1) { z = mfma32(wxf[3 * t + 1], v[3 * t + 1], z); PIN(z); } else { z1 = mfma32(wxf[3 * t + 1], v[3 * t + 1], z1); PIN(z1); }
      SB();
      const f32x4 o = f32x4{bf2f(hr[t][0]), bfhi(hr[t][0]), bf2f(hr[t][1]), bfhi(hr[t][1])} + (y * LN2) * e;
      *(u32x2*)(lds + L_SM + tile_node(t, c16) * 272 + wv * 32 + g4 * 8) = u32x2{pk_bf16(o[0], o[1]), pk_bf16(o[2], o[3])};
      SB();
      if (t == 0) { z = mfma32(wxf[2], v[2], z); PIN(z); }
      if (t == 1) { z1 = mfma32(wxf[5], v[5], z1); PIN(z1); }
      SB();
    }
    __syncthreads();
    const __amdgpu_buffer_rsrc_t r_out = rsrc(out, N * 262);
    const u32x4 o0 = *(const u32x4*)(lds + L_SM + (tid >> 4) * 272 + (tid & 15) * 16);
    const u32x4 o1 = *(const u32x4*)(lds + L_SM + (32 + ((tid >> 4) & 7)) * 272 + (tid & 15) * 16);
    const u32 oo = nb0 * 256 + tid * 16;
    __builtin_amdgcn_raw_buffer_store_b128(o0, r_out, oo < N * 256 ? oo : OOB, 0, 0);
    __builtin_amdgcn_raw_buffer_store_b128(o1, r_out, tid < 128 && oo + 8192 < N * 256 ? oo + 8192 : OOB, 0, 0);
    __builtin_amdgcn_raw_buffer_store_b16((u16)pk_bf16(bf2f(p0) + LN2 * (z[0] + z1[0]), 0.f), r_out,
                                          pon ? N * 256 + ((nb0 + pnl) * 3 + kind - 1) * 2 : OOB, 0, 0);
  }
  if (b == 0 && tid == 0) __hip_atomic_store((gu64*)(ws + WS_EP), ep + 1, RLX_AGENT);
}

__global__ __launch_bounds__(128) void k_edge_any(const u16* h, const u16* pos, const u16* ea, const int* src, const int* dst,
                                                  const u16* w_e, const u16* w_x, float* acc, int N) {
  __shared__ float x[2 * D + 2], red[2];
  const int e = blockIdx.x, f = threadIdx.x, s = src[e], d = dst[e];
  const u32 xy = pk_bf16(bf2f(pos[d * 3]) - bf2f(pos[s * 3]), bf2f(pos[d * 3 + 1]) - bf2f(pos[s * 3 + 1]));
  const u32 zz = pk_bf16(bf2f(pos[d * 3 + 2]) - bf2f(pos[s * 3 + 2]), 0.f);
  x[f] = bf2f(h[(size_t)d * D + f]); x[D + f] = bf2f(h[(size_t)s * D + f]);
  if (f == 0) { x[2 * D] = bfhi(dist2(xy, zz)); x[2 * D + 1] = bf2f(ea[e]); }
  __syncthreads();
  float y = 0.f;
  for (int k = 0; k < 2 * D + 2; k++) y += x[k] * bf2f(w_e[k * D + f]);
  const float m = LN2 * bf2f(silu2(y * LOG2E, 0.f));
  atomicAdd(&acc[(size_t)d * D + f], m);
  float c = m * bf2f(w_x[f]);
  for (int o = 32; o > 0; o >>= 1) c += __shfl_xor(c, o, 64);
  if ((f & 63) == 0) red[f >> 6] = c;
  __syncthreads();
  if (f < 3) atomicAdd(&acc[(size_t)N * D + d * 3 + f], (red[0] + red[1]) * (f == 0 ? bf2f(xy) : f == 1 ? bfhi(xy) : bf2f(zz)));
}
__global__ __launch_bounds__(128) void k_node_any(const u16* h, const u16* pos, const u16* w_h, const float* acc, u16* out, int N) {
  __shared__ float x[2 * D];
  const int n = blockIdx.x, f = threadIdx.x;
  x[f] = bf2f(h[(size_t)n * D + f]); x[D + f] = bf2f(pk_bf16(acc[(size_t)n * D + f], 0.f));
  __syncthreads();
  float y = 0.f;
  for (int k = 0; k < 2 * D; k++) y += x[k] * bf2f(w_h[k * D + f]);
  out[(size_t)n * D + f] = (u16)pk_bf16(x[f] + y * __builtin_amdgcn_rcpf(1.f + __builtin_amdgcn_exp2f(-y * LOG2E)), 0.f);
  if (f < 3) out[(size_t)N * D + n * 3 + f] = (u16)pk_bf16(bf2f(pos[n * 3 + f]) + acc[(size_t)N * D + n * 3 + f], 0.f);
}

extern "C" void kernel_launch(void* const* d_in, const int* in_sizes, int n_in, void* d_out, int out_size, void* d_ws,
                              size_t ws_size, hipStream_t stream) {
  const u16 *h = (const u16*)d_in[0], *pos = (const u16*)d_in[1], *ea = (const u16*)d_in[2];
  const int *src = (const int*)d_in[3], *dst = (const int*)d_in[4];
  const u16 *w_e = (const u16*)d_in[5], *w_x = (const u16*)d_in[6], *w_h = (const u16*)d_in[7];
  const int N = in_sizes[0] / D, E = in_sizes[3];
  const int NB = (N + NPB - 1) / NPB, C = (E + CH - 1) / CH;
  const int G = NB > C ? NB : C;
  if (G <= MAXG && N * 6 <= MAXPOS && (N & 7) == 0 && ws_size >= WS_SLOT0 + 4 * WS_SLOT) {
    k_egnn<<<G, NT, 0, stream>>>(h, pos, ea, src, dst, w_e, w_x, w_h, (char*)d_ws, (u16*)d_out, N, E, G);
  } else {
    (void)hipMemsetAsync(d_ws, 0, (size_t)N * (D + 3) * sizeof(float), stream);
    if (E > 0) k_edge_any<<<E, 128, 0, stream>>>(h, pos, ea, src, dst, w_e, w_x, (float*)d_ws, N);
    k_node_any<<<N, 128, 0, stream>>>(h, pos, w_h, (const float*)d_ws, (u16*)d_out, N);
  }
}
```
